```python
import torch

def _softmax(x, axis=-1):
    return torch.softmax(x, dim=axis)
def _gelu(x):
    return torch.nn.functional.gelu(x, approximate="tanh")
def _silu(x):
    return torch.nn.functional.silu(x)
def _sigmoid(x):
    return torch.sigmoid(x)
def _layernorm(x, axis=-1, eps=1e-5):
    m = x.mean(dim=axis, keepdim=True)
    v = x.var(dim=axis, keepdim=True, unbiased=False)
    return (x - m) / torch.sqrt(v + eps)
def _scatter_add(tgt, idx, src, axis=0):
    return tgt.clone().index_add_(axis, idx, src)
def _segment_sum(src, seg, n):
    out = torch.zeros((n,) + src.shape[1:], dtype=src.dtype)
    return out.index_add_(0, seg, src)
def _pad(x, pw):
    flat = tuple(v for pair in reversed(pw) for v in pair)
    return torch.nn.functional.pad(x, flat)

kernel_name = 'standalone__modern_conv_block_replknet__vision'

def setup_inputs():

    _w = (lambda a: (lambda t: t.to(torch.bfloat16) if t.is_floating_point() else t)(torch.as_tensor(a)))
    H = W = 384
    C = 96
    K = 3
    return {
        "x": _w((lambda *s: torch.randn(*s, dtype=torch.bfloat16) * 0.05)(1, H, W, C)),
        "dw_large": _w((lambda *s: torch.randn(*s, dtype=torch.bfloat16) * 0.4)(K, K, C) * (1.0 / K)),
        "dw_small": _w((lambda *s: torch.randn(*s, dtype=torch.bfloat16) * 0.4)(5, 5, C) * 0.2),
        "w1": _w((lambda *s: torch.randn(*s, dtype=torch.bfloat16) * 0.4)(C, 4 * C) * (C ** -0.5)),
        "w2": _w((lambda *s: torch.randn(*s, dtype=torch.bfloat16) * 0.1)(4 * C, C) * ((4 * C) ** -0.5)),
    }

def reference(x, dw_large, dw_small, w1, w2):
    B, H, W, C = x.shape
    Kl = dw_large.shape[0]
    pl = (Kl - 1) // 2
    xpl = _pad(x, ((0,0),(pl,pl),(pl,pl),(0,0)))
    out = torch.zeros_like(x)
    for i in range(Kl):
        for j in range(Kl):
            out = out + xpl[:, i:i+H, j:j+W, :] * dw_large[i, j][None, None, None, :]
    Ks = dw_small.shape[0]
    ps = (Ks - 1) // 2
    xps = _pad(x, ((0,0),(ps,ps),(ps,ps),(0,0)))
    for i in range(Ks):
        for j in range(Ks):
            out = out + xps[:, i:i+H, j:j+W, :] * dw_small[i, j][None, None, None, :]
    h = _layernorm(out, axis=-1)
    h = _gelu(torch.einsum('bhwc,ce->bhwe', h, w1))
    h = torch.einsum('bhwe,ec->bhwc', h, w2)
    return x + h
```

```cpp
#include <hip/hip_runtime.h>
#include <hip/hip_bf16.h>
typedef unsigned short u16;
__device__ inline float bf(u16 v) { return __uint_as_float((unsigned)v << 16); }
__device__ inline u16 tobf(float f) { __hip_bfloat16 b = __float2bfloat16(f); return *(u16*)&b; }
__global__ void __launch_bounds__(128) naive(const u16* x, const u16* wl, const u16* ws, const u16* w1, const u16* w2, u16* out) {
  __shared__ float y[96], g[384], red[2];
  int p = blockIdx.x, py = p / 384, px = p % 384, t = threadIdx.x;
  if (t < 96) {
    float a = 0;
    for (int i = 0; i < 5; ++i) for (int j = 0; j < 5; ++j) {
      int yy = py + i - 2, xx = px + j - 2;
      if (yy < 0 || yy >= 384 || xx < 0 || xx >= 384) continue;
      float w = bf(ws[(i * 5 + j) * 96 + t]);
      if (i >= 1 && i <= 3 && j >= 1 && j <= 3) w += bf(wl[((i - 1) * 3 + j - 1) * 96 + t]);
      a += w * bf(x[(yy * 384 + xx) * 96 + t]);
    }
    y[t] = a;
  }
  __syncthreads();
  if (t == 0) { float m = 0; for (int c = 0; c < 96; ++c) m += y[c]; m /= 96; float v = 0; for (int c = 0; c < 96; ++c) v += (y[c] - m) * (y[c] - m); v /= 96; red[0] = m; red[1] = rsqrtf(v + 1e-5f); }
  __syncthreads();
  float m = red[0], r = red[1];
  for (int e = t; e < 384; e += 128) {
    float z = 0; for (int c = 0; c < 96; ++c) z += (y[c] - m) * r * bf(w1[c * 384 + e]);
    g[e] = 0.5f * z * (1.f + tanhf(0.7978845608f * (z + 0.044715f * z * z * z)));
  }
  __syncthreads();
  if (t < 96) { float o = 0; for (int e = 0; e < 384; ++e) o += g[e] * bf(w2[e * 96 + t]); out[p * 96 + t] = tobf(bf(x[p * 96 + t]) + o); }
}
extern "C" void kernel_launch(void* const* d_in, const int* in_sizes, int n_in, void* d_out, int out_size, void* d_ws, size_t ws_size, hipStream_t stream) {
  naive<<<384 * 384, 128, 0, stream>>>((const u16*)d_in[0], (const u16*)d_in[1], (const u16*)d_in[2], (const u16*)d_in[3], (const u16*)d_in[4], (u16*)d_out);
}
```

```cpp
#include <hip/hip_runtime.h>
#include <hip/hip_bf16.h>
typedef unsigned short u16;

__device__ __forceinline__ float bf2f(u16 v) { return __uint_as_float((unsigned)v << 16); }
__device__ __forceinline__ float wsum(float v) {
#pragma unroll
  for (int o = 32; o > 0; o >>= 1) v += __shfl_xor(v, o, 64);
  return v;
}
__global__ void __launch_bounds__(512)
replk_block_kernel(const u16* x, const u16* dwl, const u16* dws, const u16* w1, const u16* w2, u16* out) {
  __shared__ __attribute__((aligned(16))) char lds[163840];
  const int wid = __builtin_amdgcn_readfirstlane(threadIdx.x >> 6);
  const int bid = blockIdx.x;
  int fail;
  asm volatile(
"s_load_dwordx8 s[72:79], %[ka], 0x0\n"
"s_mov_b32 s58, %[wid]\n"
"s_and_b32 s59, s58, 3\n"
"s_and_b32 s66, %[bid], 7\n"
"s_lshr_b32 s67, %[bid], 3\n"
"s_lshr_b32 s68, s66, 1\n"
"s_lshl_b32 s68, s68, 2\n"
"s_lshr_b32 s69, s67, 3\n"
"s_add_u32 s60, s68, s69\n"
"s_and_b32 s68, s66, 1\n"
"s_lshl_b32 s68, s68, 3\n"
"s_and_b32 s69, s67, 7\n"
"s_add_u32 s61, s68, s69\n"
"v_mbcnt_lo_u32_b32 v251, -1, 0\n"
"v_mbcnt_hi_u32_b32 v251, -1, v251\n"
"s_bitcmp1_b32 s60, 0\n"
"s_cselect_b32 s65, 1, -1\n"
"s_getpc_b64 s[94:95]\n"
".Lpc_%=:\n"
"s_and_b32 s66, %[bid], 0xf8\n"
"s_add_u32 s66, s66, s58\n"
"s_lshl_b32 s66, s66, 8\n"
"v_lshl_add_u32 v164, v251, 6, s66\n"
"v_min_u32 v164, .Lz_%=-.Lpc_%=+1536, v164\n"
"s_mov_b64 exec, 15\n"
"global_load_dword v165, v164, s[94:95]\n"
"s_mov_b64 exec, -1\n"
"s_mov_b32 s38, 28311552\n"
"s_mov_b32 s39, 0x20000\n"
"s_mul_i32 s70, s61, 4608\n"
"s_sub_u32 s70, s70, 384\n"
"s_mul_i32 s85, s60, 24\n"
"s_cmp_lt_i32 s65, 0\n"
"s_cselect_b32 s66, 25, -2\n"
"s_add_u32 s85, s85, s66\n"
"s_mul_i32 s66, s58, s65\n"
"s_add_u32 s85, s85, s66\n"
"s_lshr_b32 s86, s58, 1\n"
"s_mul_i32 s86, s86, 5696\n"
"s_bitcmp1_b32 s58, 0\n"
"s_cselect_b32 s66, 79752, 0\n"
"s_add_u32 s86, s86, s66\n"
"v_add_u32 v253, 64, v251\n"
"v_mul_u32_u24 v105, 2622, v253\n"
"v_sub_u32_sdwa v253, v253, v105 dst_sel:DWORD dst_unused:UNUSED_PAD src0_sel:DWORD src1_sel:WORD_1\n"
"v_lshl_add_u32 v253, v253, 4, s70\n"
"v_add_u32 v254, 128, v251\n"
"v_mul_u32_u24 v104, 2622, v254\n"
"v_sub_u32_sdwa v254, v254, v104 dst_sel:DWORD dst_unused:UNUSED_PAD src0_sel:DWORD src1_sel:WORD_1\n"
"v_lshl_add_u32 v254, v254, 4, s70\n"
"v_add_u32 v166, 192, v251\n"
"v_mul_u32_u24 v105, 2622, v166\n"
"v_sub_u32_sdwa v166, v166, v105 dst_sel:DWORD dst_unused:UNUSED_PAD src0_sel:DWORD src1_sel:WORD_1\n"
"v_lshl_add_u32 v166, v166, 4, s70\n"
"s_add_u32 m0, s86, 1024\n"
"s_mul_i32 s68, s85, 73728\n"
"s_waitcnt lgkmcnt(0)\n"
"s_mov_b64 s[36:37], s[72:73]\n"
"s_and_b32 s37, s37, 0xffff\n"
"buffer_load_dwordx4 v253, s[36:39], s68 offen lds\n"
"s_add_u32 m0, s86, 2048\n"
"s_nop 0\n"
"buffer_load_dwordx4 v254, s[36:39], s68 offen lds\n"
"s_add_u32 m0, s86, 3072\n"
"s_nop 0\n"
"buffer_load_dwordx4 v166, s[36:39], s68 offen lds\n"
"s_add_u32 m0, s86, 4096\n"
"s_mov_b64 s[52:53], s[74:75]\n"
"s_mov_b64 s[48:49], s[76:77]\n"
"s_mov_b64 s[40:41], s[78:79]\n"
"s_cmp_ge_u32 s58, 4\n"
"s_cbranch_scc1 .Lc_%=\n"
"s_and_b32 s49, s49, 0xffff\n"
"s_mov_b32 s50, 4800\n"
"s_mov_b32 s51, 0x20000\n"
"s_and_b32 s53, s53, 0xffff\n"
"s_mov_b32 s54, 1728\n"
"s_mov_b32 s55, 0x20000\n"
"s_mul_i32 s66, s59, 60\n"
"v_add_u32 v70, s66, v251\n"
"v_lshlrev_b32 v71, 2, v70\n"
"s_mov_b32 exec_lo, 0xffffffff\n"
"s_mov_b32 exec_hi, 0xfffffff\n"
"buffer_load_dword v74, v71, s[48:51], 0 offen offset:0\n"
"buffer_load_dword v75, v71, s[48:51], 0 offen offset:960\n"
"buffer_load_dword v76, v71, s[48:51], 0 offen offset:1920\n"
"buffer_load_dword v77, v71, s[48:51], 0 offen offset:2880\n"
"buffer_load_dword v78, v71, s[48:51], 0 offen offset:3840\n"
"v_subrev_u32 v72, 192, v71\n"
"v_cmp_gt_u32 vcc, 576, v72\n"
"v_mov_b32 v73, 0x80000000\n"
"s_nop 0\n"
"v_cndmask_b32 v72, v73, v72, vcc\n"
"buffer_load_dword v80, v72, s[52:55], 0 offen offset:0\n"
"buffer_load_dword v81, v72, s[52:55], 0 offen offset:576\n"
"buffer_load_dword v82, v72, s[52:55], 0 offen offset:1152\n"
"s_mov_b64 exec, -1\n"
".Lc_%=:\n"
"v_add_u32 v167, 256, v251\n"
"v_mul_u32_u24 v104, 2622, v167\n"
"v_sub_u32_sdwa v167, v167, v104 dst_sel:DWORD dst_unused:UNUSED_PAD src0_sel:DWORD src1_sel:WORD_1\n"
"v_lshl_add_u32 v167, v167, 4, s70\n"
"buffer_load_dwordx4 v167, s[36:39], s68 offen lds\n"
"s_add_u32 m0, s86, 0\n"
"v_add_u32 v252, 0, v251\n"
"v_mul_u32_u24 v104, 2622, v252\n"
"v_sub_u32_sdwa v252, v252, v104 dst_sel:DWORD dst_unused:UNUSED_PAD src0_sel:DWORD src1_sel:WORD_1\n"
"v_lshl_add_u32 v252, v252, 4, s70\n"
"v_cmp_gt_u32 vcc, 73728, v252\n"
"v_mov_b32 v104, 0x80000000\n"
"s_nop 0\n"
"v_cndmask_b32 v252, v104, v252, vcc\n"
"buffer_load_dwordx4 v252, s[36:39], s68 offen lds\n"
"s_add_u32 m0, s86, 5120\n"
"v_add_u32 v178, 320, v251\n"
"v_mul_u32_u24 v105, 2622, v178\n"
"v_sub_u32_sdwa v178, v178, v105 dst_sel:DWORD dst_unused:UNUSED_PAD src0_sel:DWORD src1_sel:WORD_1\n"
"v_lshl_add_u32 v178, v178, 4, s70\n"
"v_cmp_gt_u32 vcc, 73728, v178\n"
"v_mov_b32 v105, 0x80000000\n"
"s_nop 0\n"
"v_cndmask_b32 v178, v105, v178, vcc\n"
"s_mov_b64 exec, 0x1fffffff\n"
"buffer_load_dwordx4 v178, s[36:39], s68 offen lds\n"
"s_mov_b64 exec, -1\n"
"v_bfe_u32 v59, v251, 3, 1\n"
"v_mul_u32_u24 v59, 2400, v59\n"
"v_bfe_u32 v60, v251, 2, 1\n"
"v_mul_u32_u24 v60, 400, v60\n"
"v_add_u32 v59, v59, v60\n"
"v_and_b32 v60, 3, v251\n"
"v_lshlrev_b32 v60, 2, v60\n"
"v_sub_u32 v59, v60, v59\n"
"s_lshl_b32 s66, s58, 4\n"
"s_add_u32 s66, s66, 139520\n"
"v_add_u32 v59, s66, v59\n"
"v_bfe_u32 v60, v251, 4, 1\n"
"v_lshrrev_b32 v61, 5, v251\n"
"v_mul_u32_u24 v62, 12000, v60\n"
"v_mul_u32_u24 v63, 400, v61\n"
"v_add3_u32 v62, v62, v63, v59\n"
"v_mul_u32_u24 v60, 400, v60\n"
"v_add_u32 v59, v59, v60\n"
"v_mul_i32_i24 v63, -1600, v61\n"
"v_add_u32 v63, v63, v59\n"
"v_mul_i32_i24 v64, -4000, v61\n"
"v_add_u32 v64, v64, v59\n"
"v_mul_i32_i24 v65, 8000, v61\n"
"v_add_u32 v65, v65, v59\n"
"v_mul_i32_i24 v66, 7200, v61\n"
"v_add_u32 v66, v66, v59\n"
"v_mul_i32_i24 v67, -2400, v61\n"
"v_add_u32 v67, v67, v59\n"
"v_and_b32 v59, 1, v251\n"
"v_lshlrev_b32 v59, 4, v59\n"
"v_bfe_u32 v60, v251, 1, 1\n"
"v_lshlrev_b32 v69, v59, v60\n"
"v_xor_b32 v60, 1, v60\n"
"v_lshlrev_b32 v68, v59, v60\n"
"s_and_b32 s41, s41, 0xffff\n"
"s_mov_b32 s42, 73728\n"
"s_mov_b32 s43, 0x20000\n"
"s_mov_b32 s62, 0xc09d30fc\n"
"s_mov_b32 s63, 0x3eeff558\n"
"s_mov_b32 s64, 0x776ff558\n"
"s_mov_b32 s82, 0x3c2aaaab\n"
"s_mov_b32 s83, 0x3727c5ac\n"
"s_mov_b32 s84, 0x7000000\n"
"s_mov_b32 s72, 0x100001\n"
"s_mov_b32 s73, 0x10000100\n"
"s_mov_b32 s74, 0x200002\n"
"s_mov_b32 s75, 0x20000200\n"
"s_mov_b32 s76, 0x400004\n"
"s_mov_b32 s77, 0x40000400\n"
"s_mov_b32 s78, 0x800008\n"
"s_mov_b32 s79, 0x80000800\n"
"s_mov_b64 s[80:81], 0\n"
"s_mov_b32 s71, 0x352aaaab\n"
"s_mul_i32 s66, s60, 24\n"
"s_mul_i32 s67, s59, s65\n"
"s_lshl_b32 s67, s67, 1\n"
"s_add_u32 s66, s66, s67\n"
"s_cmp_lt_i32 s65, 0\n"
"s_cselect_b32 s67, 7, 0\n"
"s_cselect_b32 s57, 1179648, 0\n"
"s_cselect_b32 s69, 0, 1179648\n"
"s_add_u32 s66, s66, s67\n"
"s_mul_i32 s66, s66, 73728\n"
"s_mul_i32 s67, s61, 4608\n"
"s_add_u32 s56, s66, s67\n"
"s_mul_i32 s96, s65, 73728\n"
"v_bfe_u32 v100, v251, 2, 2\n"
"v_and_b32 v101, 3, v251\n"
"v_mul_u32_u24 v114, 0x5555, v101\n"
"v_mul_u32_u24 v116, 5696, v100\n"
"v_mul_u32_u24 v103, 1200, v101\n"
"s_lshl_b32 s66, s58, 3\n"
"v_add3_u32 v116, v116, v103, s66\n"
"v_lshrrev_b32 v102, 5, v251\n"
"v_mul_u32_u24 v102, 192, v102\n"
"v_add_u32 v116, v116, v102\n"
"v_bfe_u32 v102, v251, 4, 1\n"
"v_mul_u32_u24 v115, 74056, v102\n"
"v_sub_u32 v115, v116, v115\n"
"v_add_u32 v115, 79752, v115\n"
"v_mul_u32_u24 v102, 91144, v102\n"
"v_add_u32 v116, v116, v102\n"
"v_lshrrev_b32 v102, 4, v251\n"
"v_xor_b32 v102, v102, v100\n"
"v_lshl_add_u32 v102, v102, 2, v101\n"
"v_lshlrev_b32 v117, 3, v102\n"
"v_and_b32 v102, 1, v100\n"
"v_mul_u32_u24 v102, 9216, v102\n"
"v_lshrrev_b32 v100, 1, v100\n"
"v_mul_u32_u24 v100, 79872, v100\n"
"v_add3_u32 v117, v117, v102, v100\n"
"s_and_b32 s66, s58, 1\n"
"s_mul_i32 s66, s66, 448\n"
"s_lshl_b32 s67, s58, 6\n"
"s_add_u32 s66, s66, s67\n"
"s_add_u32 s66, s66, 56960\n"
"v_add_u32 v117, s66, v117\n"
"s_and_b32 s66, s59, 1\n"
"s_mul_i32 s66, s66, 9216\n"
"s_lshr_b32 s67, s59, 1\n"
"s_mul_i32 s67, s67, 79872\n"
"s_add_u32 s66, s66, s67\n"
"s_add_u32 s66, s66, 56960\n"
"v_lshlrev_b32 v118, 3, v251\n"
"v_add_u32 v118, s66, v118\n"
"v_lshrrev_b32 v100, 2, v251\n"
"v_xor_b32 v100, s59, v100\n"
"v_bfe_u32 v102, v100, 1, 1\n"
"v_mul_u32_u24 v119, 79752, v102\n"
"v_and_b32 v100, 1, v100\n"
"v_mul_u32_u24 v100, 192, v100\n"
"v_add3_u32 v119, v119, v100, v103\n"
"v_and_b32 v102, 48, v251\n"
"s_mul_i32 s66, s59, 5696\n"
"s_add_u32 s66, s66, 6096\n"
"v_add3_u32 v119, v119, v102, s66\n"
"v_mov_b32 v250, 1.0\n"
"v_mov_b32 v240, 0x3c003c00\n"
"v_mov_b32 v241, 0x3c003c00\n"
"v_mov_b32 v242, 0x3c003c00\n"
"v_mov_b32 v243, 0x3c003c00\n"
"v_lshrrev_b32 v59, 4, v251\n"
"v_bfe_u32 v60, v251, 2, 2\n"
"v_cmp_eq_u32 vcc, v59, v60\n"
"v_mov_b32 v59, 0x3f80\n"
"v_mov_b32 v244, 0\n"
"v_mov_b32 v245, 0\n"
"v_mov_b32 v248, 0\n"
"v_mov_b32 v249, 0\n"
"v_cndmask_b32 v59, 0, v59, vcc\n"
"v_mul_u32_u24 v246, v59, v68\n"
"v_mul_u32_u24 v247, v59, v69\n"
"v_bfe_u32 v100, v251, 3, 2\n"
"v_bfe_u32 v255, v251, 1, 2\n"
"v_lshl_add_u32 v255, v100, 3, v255\n"
"v_mul_u32_u24 v255, 768, v255\n"
"v_lshrrev_b32 v100, 5, v251\n"
"v_lshl_add_u32 v255, v100, 4, v255\n"
"v_and_b32 v100, 1, v251\n"
"v_lshl_add_u32 v255, v100, 5, v255\n"
"v_lshlrev_b32 v175, 4, v251\n"
"s_and_b32 s66, s59, 1\n"
"s_mul_i32 s89, s66, 3072\n"
"s_mul_i32 s90, s66, 3080\n"
"s_lshr_b32 s93, s59, 1\n"
"s_lshl_b32 s92, s93, 6\n"
"s_add_u32 s92, s92, s89\n"
"s_mul_i32 s91, s59, 3072\n"
"s_load_dwordx4 s[44:47], %[ka], 0x20\n"
"s_cmp_ge_u32 s58, 4\n"
"s_cbranch_scc1 .Lb_%=\n"
"v_mul_u32_u24 v71, 1366, v70\n"
"v_lshrrev_b32 v71, 16, v71\n"
"v_lshlrev_b32 v71, 4, v71\n"
"v_lshl_add_u32 v71, v70, 3, v71\n"
"s_mul_i32 s87, s65, 2400\n"
"s_mul_i32 s66, s65, -4800\n"
"s_add_u32 s66, s66, 144320\n"
"v_add_u32 v71, s66, v71\n"
"v_mov_b32 v84, 0\n"
"v_mov_b32 v85, 0\n"
"v_mov_b32 v86, 0\n"
"v_mov_b32 v87, 0\n"
"s_mul_i32 s66, s59, 704\n"
"s_add_u32 s66, s66, 136720\n"
"v_lshlrev_b32 v72, 4, v251\n"
"v_add_u32 v73, s66, v72\n"
"v_min_u32 v73, 139504, v73\n"
"s_mov_b32 exec_lo, 0xffffffff\n"
"s_mov_b32 exec_hi, 0xfff\n"
"ds_write_b128 v73, v[84:87]\n"
"ds_write_b128 v73, v[84:87] offset:14400\n"
"s_mul_i32 s66, s59, 2400\n"
"s_add_u32 s66, s66, 141520\n"
"v_add_u32 v73, s66, v72\n"
"s_mov_b32 exec_lo, 0x1ffffff\n"
"s_mov_b32 exec_hi, 0x0\n"
"ds_write_b128 v73, v[84:87]\n"
"s_nop 0\n"
"v_mov_b32 v73, 163776\n"
"s_mov_b32 exec_lo, 0x1\n"
"s_mov_b32 exec_hi, 0x0\n"
"ds_write_b128 v73, v[84:87]\n"
"s_mov_b64 exec, -1\n"
"s_mov_b32 s66, 0x05040100\n"
"s_mov_b32 s67, 0x07060302\n"
"s_mov_b32 exec_lo, 0xffffffff\n"
"s_mov_b32 exec_hi, 0xfffffff\n"
"s_waitcnt vmcnt(10)\n"
"v_perm_b32 v88, v84, v74, s66\n"
"v_perm_b32 v89, v84, v74, s67\n"
"ds_write_b64 v71, v[88:89]\n"
"v_add_u32 v71, s87, v71\n"
"s_waitcnt vmcnt(5)\n"
"v_perm_b32 v90, v80, v75, s66\n"
"v_perm_b32 v91, v80, v75, s67\n"
"ds_write_b64 v71, v[90:91]\n"
"v_add_u32 v71, s87, v71\n"
"s_waitcnt vmcnt(4)\n"
"v_perm_b32 v92, v81, v76, s66\n"
"v_perm_b32 v93, v81, v76, s67\n"
"ds_write_b64 v71, v[92:93]\n"
"v_add_u32 v71, s87, v71\n"
"s_waitcnt vmcnt(3)\n"
"v_perm_b32 v94, v82, v77, s66\n"
"v_perm_b32 v95, v82, v77, s67\n"
"ds_write_b64 v71, v[94:95]\n"
"v_add_u32 v71, s87, v71\n"
"v_perm_b32 v96, v84, v78, s66\n"
"v_perm_b32 v97, v84, v78, s67\n"
"ds_write_b64 v71, v[96:97]\n"
"s_mov_b64 exec, -1\n"
"s_add_u32 m0, s86, 22784\n"
"s_mul_i32 s68, s65, 8\n"
"s_add_u32 s68, s85, s68\n"
"s_mul_i32 s68, s68, 73728\n"
"buffer_load_dwordx4 v252, s[36:39], s68 offen lds\n"
"s_add_u32 m0, s86, 23808\n"
"s_nop 0\n"
"buffer_load_dwordx4 v253, s[36:39], s68 offen lds\n"
"s_add_u32 m0, s86, 24832\n"
"s_nop 0\n"
"buffer_load_dwordx4 v254, s[36:39], s68 offen lds\n"
"s_waitcnt lgkmcnt(0)\n"
"s_barrier\n"
"ds_read_b32 v0, v62 offset:0\n"
"ds_read_b32 v10, v62 offset:128\n"
"ds_read_b32 v20, v62 offset:256\n"
"ds_read_b32 v2, v63 offset:2400\n"
"ds_read_b32 v3, v64 offset:7200\n"
"ds_read_b32 v4, v65 offset:4800\n"
"ds_read_b32 v5, v64 offset:9600\n"
"ds_read_b32 v6, v64 offset:8000\n"
"ds_read_b32 v7, v66 offset:1600\n"
"ds_read_b32 v8, v64 offset:10400\n"
"ds_read_b32 v9, v67 offset:13600\n"
"ds_read_b32 v12, v63 offset:2528\n"
"ds_read_b32 v13, v64 offset:7328\n"
"ds_read_b32 v14, v65 offset:4928\n"
"ds_read_b32 v15, v64 offset:9728\n"
"ds_read_b32 v16, v64 offset:8128\n"
"ds_read_b32 v17, v66 offset:1728\n"
"ds_read_b32 v18, v64 offset:10528\n"
"ds_read_b32 v19, v67 offset:13728\n"
"ds_read_b32 v22, v63 offset:2656\n"
"ds_read_b32 v23, v64 offset:7456\n"
"ds_read_b32 v24, v65 offset:5056\n"
"ds_read_b32 v25, v64 offset:9856\n"
"ds_read_b32 v26, v64 offset:8256\n"
"ds_read_b32 v27, v66 offset:1856\n"
"ds_read_b32 v28, v64 offset:10656\n"
"ds_read_b32 v29, v67 offset:13856\n"
"s_mov_b64 s[34:35], s[46:47]\n"
"s_and_b32 s45, s45, 0xffff\n"
"s_mov_b32 s46, 73728\n"
"s_mov_b32 s47, 0x20000\n"
"s_waitcnt lgkmcnt(14)\n"
"v_mul_u32_u24 v1, v0, v69\n"
"v_mul_u32_u24 v0, v0, v68\n"
"v_mul_u32_u24 v11, v10, v69\n"
"v_mul_u32_u24 v10, v10, v68\n"
"v_mul_u32_u24 v21, v20, v69\n"
"v_mul_u32_u24 v20, v20, v68\n"
"s_waitcnt vmcnt(0)\n"
"s_waitcnt lgkmcnt(0)\n"
"s_barrier\n"
"ds_read_b64 v[30:31], v116 offset:0\n"
"ds_read_b64 v[36:37], v116 offset:400\n"
"ds_read_b64 v[42:43], v116 offset:800\n"
"ds_read_b64 v[32:33], v115 offset:0\n"
"ds_read_b64 v[34:35], v115 offset:5696\n"
"ds_read_b64 v[38:39], v115 offset:400\n"
"ds_read_b64 v[40:41], v115 offset:6096\n"
"ds_read_b64 v[44:45], v115 offset:800\n"
"ds_read_b64 v[46:47], v115 offset:6496\n"
"ds_read_b64 v[48:49], v116 offset:1200\n"
"ds_read_b64 v[50:51], v115 offset:1200\n"
"s_waitcnt lgkmcnt(8)\n"
"v_mfma_f32_16x16x16_bf16 v[90:93], v[0:1], v[30:31], 0\n"
"s_add_u32 m0, s86, 34176\n"
"s_mul_i32 s68, s65, 12\n"
"s_add_u32 s68, s85, s68\n"
"s_mul_i32 s68, s68, 73728\n"
"buffer_load_dwordx4 v252, s[36:39], s68 offen lds\n"
"s_add_u32 m0, s86, 35200\n"
"ds_read_b64 v[52:53], v115 offset:6896\n"
"ds_read_b64 v[54:55], v116 offset:1600\n"
"v_mfma_f32_16x16x16_bf16 v[94:97], v[0:1], v[36:37], 0\n"
"ds_read_b64 v[56:57], v115 offset:1600\n"
"ds_read_b64 v[58:59], v115 offset:7296\n"
"v_mfma_f32_16x16x16_bf16 v[98:101], v[0:1], v[42:43], 0\n"
"ds_read_b64 v[60:61], v116 offset:64\n"
"s_waitcnt lgkmcnt(10)\n"
"v_smfmac_f32_16x16x64_bf16 v[90:93], v[2:5], v[32:39], v114\n"
"ds_read_b64 v[66:67], v116 offset:464\n"
"ds_read_b64 v[72:73], v116 offset:864\n"
"s_waitcnt lgkmcnt(10)\n"
"v_smfmac_f32_16x16x64_bf16 v[94:97], v[2:5], v[38:45], v114\n"
"buffer_load_dwordx4 v253, s[36:39], s68 offen lds\n"
"s_add_u32 m0, s86, 36224\n"
"ds_read_b64 v[62:63], v115 offset:64\n"
"ds_read_b64 v[64:65], v115 offset:5760\n"
"s_waitcnt lgkmcnt(11)\n"
"v_smfmac_f32_16x16x64_bf16 v[90:93], v[6:9], v[40:47], v114\n"
"ds_read_b64 v[68:69], v115 offset:464\n"
"s_waitcnt lgkmcnt(10)\n"
"v_smfmac_f32_16x16x64_bf16 v[98:101], v[2:5], v[44:51], v114\n"
"ds_read_b64 v[70:71], v115 offset:6160\n"
"ds_read_b64 v[74:75], v115 offset:864\n"
"s_waitcnt lgkmcnt(11)\n"
"v_smfmac_f32_16x16x64_bf16 v[94:97], v[6:9], v[46:53], v114\n"
"ds_read_b64 v[76:77], v115 offset:6560\n"
"ds_read_b64 v[78:79], v116 offset:1264\n"
"s_waitcnt lgkmcnt(10)\n"
"v_smfmac_f32_16x16x64_bf16 v[98:101], v[6:9], v[52:59], v114\n"
"buffer_load_dwordx4 v254, s[36:39], s68 offen lds\n"
"s_add_u32 m0, s86, 37248\n"
"ds_read_b64 v[80:81], v115 offset:1264\n"
"v_cvt_pk_f16_f32 v90, v90, v91\n"
"v_cvt_pk_f16_f32 v91, v92, v93\n"
"s_waitcnt lgkmcnt(8)\n"
"v_mfma_f32_16x16x16_bf16 v[102:105], v[10:11], v[60:61], 0\n"
"ds_write_b64 v117, v[90:91] offset:0\n"
"ds_read_b64 v[82:83], v115 offset:6960\n"
"ds_read_b64 v[84:85], v116 offset:1664\n"
"v_mfma_f32_16x16x16_bf16 v[106:109], v[10:11], v[66:67], 0\n"
"ds_read_b64 v[86:87], v115 offset:1664\n"
"ds_read_b64 v[88:89], v115 offset:7360\n"
"v_mfma_f32_16x16x16_bf16 v[110:113], v[10:11], v[72:73], 0\n"
"ds_read_b64 v[30:31], v116 offset:128\n"
"v_cvt_pk_f16_f32 v94, v94, v95\n"
"v_cvt_pk_f16_f32 v95, v96, v97\n"
"s_waitcnt lgkmcnt(11)\n"
"v_smfmac_f32_16x16x64_bf16 v[102:105], v[12:15], v[62:69], v114\n"
"ds_write_b64 v117, v[94:95] offset:3072\n"
"ds_read_b64 v[36:37], v116 offset:528\n"
"ds_read_b64 v[42:43], v116 offset:928\n"
"s_waitcnt lgkmcnt(12)\n"
"v_smfmac_f32_16x16x64_bf16 v[106:109], v[12:15], v[68:75], v114\n"
"buffer_load_dwordx4 v166, s[36:39], s68 offen lds\n"
"s_add_u32 m0, s86, 38272\n"
"ds_read_b64 v[32:33], v115 offset:128\n"
"ds_read_b64 v[34:35], v115 offset:5824\n"
"v_cvt_pk_f16_f32 v98, v98, v99\n"
"v_cvt_pk_f16_f32 v99, v100, v101\n"
"s_waitcnt lgkmcnt(13)\n"
"v_smfmac_f32_16x16x64_bf16 v[102:105], v[16:19], v[70:77], v114\n"
"ds_write_b64 v117, v[98:99] offset:6144\n"
"ds_read_b64 v[38:39], v115 offset:528\n"
"s_waitcnt lgkmcnt(13)\n"
"v_smfmac_f32_16x16x64_bf16 v[110:113], v[12:15], v[74:81], v114\n"
"ds_read_b64 v[40:41], v115 offset:6224\n"
"ds_read_b64 v[44:45], v115 offset:928\n"
"s_waitcnt lgkmcnt(13)\n"
"v_smfmac_f32_16x16x64_bf16 v[106:109], v[16:19], v[76:83], v114\n"
"ds_read_b64 v[46:47], v115 offset:6624\n"
"ds_read_b64 v[48:49], v116 offset:1328\n"
"s_waitcnt lgkmcnt(12)\n"
"v_smfmac_f32_16x16x64_bf16 v[110:113], v[16:19], v[82:89], v114\n"
"buffer_load_dwordx4 v167, s[36:39], s68 offen lds\n"
"s_add_u32 m0, s86, 39296\n"
"ds_read_b64 v[50:51], v115 offset:1328\n"
"v_cvt_pk_f16_f32 v102, v102, v103\n"
"v_cvt_pk_f16_f32 v103, v104, v105\n"
"s_waitcnt lgkmcnt(9)\n"
"v_mfma_f32_16x16x16_bf16 v[90:93], v[20:21], v[30:31], 0\n"
"ds_write_b64 v117, v[102:103] offset:1024\n"
"ds_read_b64 v[52:53], v115 offset:7024\n"
"ds_read_b64 v[54:55], v116 offset:1728\n"
"v_mfma_f32_16x16x16_bf16 v[94:97], v[20:21], v[36:37], 0\n"
"ds_read_b64 v[56:57], v115 offset:1728\n"
"ds_read_b64 v[58:59], v115 offset:7424\n"
"v_mfma_f32_16x16x16_bf16 v[98:101], v[20:21], v[42:43], 0\n"
"v_cvt_pk_f16_f32 v106, v106, v107\n"
"v_cvt_pk_f16_f32 v107, v108, v109\n"
"s_waitcnt lgkmcnt(10)\n"
"v_smfmac_f32_16x16x64_bf16 v[90:93], v[22:25], v[32:39], v114\n"
"ds_write_b64 v117, v[106:107] offset:4096\n"
"s_mov_b64 exec, 0x1fffffff\n"
"buffer_load_dwordx4 v178, s[36:39], s68 offen lds\n"
"s_mov_b64 exec, -1\n"
"s_waitcnt lgkmcnt(9)\n"
"v_smfmac_f32_16x16x64_bf16 v[94:97], v[22:25], v[38:45], v114\n"
"v_cvt_pk_f16_f32 v110, v110, v111\n"
"v_cvt_pk_f16_f32 v111, v112, v113\n"
"s_waitcnt lgkmcnt(8)\n"
"v_smfmac_f32_16x16x64_bf16 v[90:93], v[26:29], v[40:47], v114\n"
"ds_write_b64 v117, v[110:111] offset:7168\n"
"s_waitcnt lgkmcnt(7)\n"
"v_smfmac_f32_16x16x64_bf16 v[98:101], v[22:25], v[44:51], v114\n"
"s_waitcnt lgkmcnt(5)\n"
"v_smfmac_f32_16x16x64_bf16 v[94:97], v[26:29], v[46:53], v114\n"
"s_waitcnt lgkmcnt(2)\n"
"v_smfmac_f32_16x16x64_bf16 v[98:101], v[26:29], v[52:59], v114\n"
"s_nop 0\n"
"v_cvt_pk_f16_f32 v90, v90, v91\n"
"v_cvt_pk_f16_f32 v91, v92, v93\n"
"s_nop 2\n"
"v_cvt_pk_f16_f32 v94, v94, v95\n"
"v_cvt_pk_f16_f32 v95, v96, v97\n"
"v_cvt_pk_f16_f32 v98, v98, v99\n"
"v_cvt_pk_f16_f32 v99, v100, v101\n"
"ds_write_b64 v117, v[90:91] offset:2048\n"
"ds_write_b64 v117, v[94:95] offset:5120\n"
"ds_write_b64 v117, v[98:99] offset:8192\n"
"s_waitcnt lgkmcnt(0)\n"
"s_barrier\n"
"ds_read_b64 v[120:121], v118 offset:0\n"
"ds_read_b64 v[122:123], v118 offset:512\n"
"ds_read_b64 v[124:125], v118 offset:1024\n"
"ds_read_b64 v[126:127], v118 offset:1536\n"
"ds_read_b64 v[128:129], v118 offset:2048\n"
"ds_read_b64 v[130:131], v118 offset:2560\n"
"ds_read_b64 v[132:133], v118 offset:3072\n"
"ds_read_b64 v[134:135], v118 offset:3584\n"
"ds_read_b64 v[136:137], v118 offset:4096\n"
"ds_read_b64 v[138:139], v118 offset:4608\n"
"ds_read_b64 v[140:141], v118 offset:5120\n"
"ds_read_b64 v[142:143], v118 offset:5632\n"
"v_add_u32 v117, 0xffff2180, v117\n"
"v_add_u32 v118, 0xffff2180, v118\n"
"s_waitcnt lgkmcnt(10)\n"
"v_mfma_f32_16x16x32_f16 v[168:171], v[120:123], v[120:123], 0\n"
"s_waitcnt lgkmcnt(8)\n"
"v_mfma_f32_16x16x32_f16 v[168:171], v[124:127], v[124:127], v[168:171]\n"
"s_waitcnt lgkmcnt(6)\n"
"v_mfma_f32_16x16x32_f16 v[168:171], v[128:131], v[128:131], v[168:171]\n"
"v_mfma_f32_16x16x32_f16 v[172:175], v[240:243], v[120:123], 0\n"
"v_mfma_f32_16x16x32_f16 v[172:175], v[240:243], v[124:127], v[172:175]\n"
"v_mfma_f32_16x16x32_f16 v[172:175], v[240:243], v[128:131], v[172:175]\n"
"s_waitcnt lgkmcnt(4)\n"
"v_mfma_f32_16x16x32_f16 v[228:231], v[132:135], v[132:135], 0\n"
"s_waitcnt lgkmcnt(2)\n"
"v_mfma_f32_16x16x32_f16 v[228:231], v[136:139], v[136:139], v[228:231]\n"
"s_waitcnt lgkmcnt(0)\n"
"v_mfma_f32_16x16x32_f16 v[228:231], v[140:143], v[140:143], v[228:231]\n"
"v_mfma_f32_16x16x32_f16 v[232:235], v[240:243], v[132:135], 0\n"
"v_mfma_f32_16x16x32_f16 v[232:235], v[240:243], v[136:139], v[232:235]\n"
"v_mfma_f32_16x16x32_f16 v[232:235], v[240:243], v[140:143], v[232:235]\n"
"s_waitcnt lgkmcnt(0)\n"
"s_barrier\n"
"v_cndmask_b32 v176, 0, v168, s[72:73]\n"
"s_nop 1\n"
"v_cndmask_b32 v236, 0, v228, s[72:73]\n"
"v_cndmask_b32 v176, v176, v169, s[74:75]\n"
"v_cndmask_b32 v236, v236, v229, s[74:75]\n"
"v_cndmask_b32 v176, v176, v170, s[76:77]\n"
"v_cndmask_b32 v236, v236, v230, s[76:77]\n"
"v_cndmask_b32 v176, v176, v171, s[78:79]\n"
"v_cndmask_b32 v236, v236, v231, s[78:79]\n"
"v_mul_f32 v172, s82, v172\n"
"v_mul_f32 v232, s82, v232\n"
"v_fma_f32 v173, -v172, v172, s83\n"
"v_fma_f32 v233, -v232, v232, s83\n"
"v_mfma_f32_16x16x4_f32 v[168:171], v250, v176, 0\n"
"v_mfma_f32_16x16x4_f32 v[228:231], v250, v236, 0\n"
"ds_read_b64 v[180:181], v119 offset:0\n"
"s_nop 9\n"
"v_fma_f32 v176, v168, s82, v173\n"
"v_fma_f32 v236, v228, s82, v233\n"
"v_mul_f32 v168, s71, v168\n"
"v_mul_f32 v228, s71, v228\n"
"v_cmp_nge_f32 vcc, v176, v168\n"
"v_cmp_nge_f32 s[54:55], v236, v228\n"
"v_rsq_f32 v176, v176\n"
"v_rsq_f32 v236, v236\n"
"s_or_b64 s[80:81], s[80:81], vcc\n"
"s_or_b64 s[80:81], s[80:81], s[54:55]\n"
"v_mul_f32 v176, s84, v176\n"
"v_mul_f32 v236, s84, v236\n"
"v_mul_f32 v177, -v172, v176\n"
"v_mul_f32 v237, -v232, v236\n"
"v_fma_mix_f32 v168, v120, v176, v177 op_sel_hi:[1,0,0]\n"
"v_fma_mix_f32 v169, v120, v176, v177 op_sel:[1,0,0] op_sel_hi:[1,0,0]\n"
"ds_read_b64 v[182:183], v119 offset:8\n"
"v_fma_mix_f32 v228, v132, v236, v237 op_sel_hi:[1,0,0]\n"
"v_fma_mix_f32 v229, v132, v236, v237 op_sel:[1,0,0] op_sel_hi:[1,0,0]\n"
"v_fma_mix_f32 v170, v121, v176, v177 op_sel_hi:[1,0,0]\n"
"v_fma_mix_f32 v171, v121, v176, v177 op_sel:[1,0,0] op_sel_hi:[1,0,0]\n"
"v_cvt_pk_bf16_f32 v120, v168, v169\n"
"ds_read_b64 v[184:185], v119 offset:64\n"
"v_fma_mix_f32 v230, v133, v236, v237 op_sel_hi:[1,0,0]\n"
"v_fma_mix_f32 v231, v133, v236, v237 op_sel:[1,0,0] op_sel_hi:[1,0,0]\n"
"v_cvt_pk_bf16_f32 v132, v228, v229\n"
"v_fma_mix_f32 v172, v122, v176, v177 op_sel_hi:[1,0,0]\n"
"v_fma_mix_f32 v173, v122, v176, v177 op_sel:[1,0,0] op_sel_hi:[1,0,0]\n"
"v_cvt_pk_bf16_f32 v121, v170, v171\n"
"ds_read_b64 v[186:187], v119 offset:72\n"
"v_fma_mix_f32 v232, v134, v236, v237 op_sel_hi:[1,0,0]\n"
"v_fma_mix_f32 v233, v134, v236, v237 op_sel:[1,0,0] op_sel_hi:[1,0,0]\n"
"v_cvt_pk_bf16_f32 v133, v230, v231\n"
"v_fma_mix_f32 v174, v123, v176, v177 op_sel_hi:[1,0,0]\n"
"v_fma_mix_f32 v175, v123, v176, v177 op_sel:[1,0,0] op_sel_hi:[1,0,0]\n"
"v_cvt_pk_bf16_f32 v122, v172, v173\n"
"ds_read_b64 v[188:189], v119 offset:128\n"
"v_fma_mix_f32 v234, v135, v236, v237 op_sel_hi:[1,0,0]\n"
"v_fma_mix_f32 v235, v135, v236, v237 op_sel:[1,0,0] op_sel_hi:[1,0,0]\n"
"v_cvt_pk_bf16_f32 v134, v232, v233\n"
"v_fma_mix_f32 v168, v124, v176, v177 op_sel_hi:[1,0,0]\n"
"v_fma_mix_f32 v169, v124, v176, v177 op_sel:[1,0,0] op_sel_hi:[1,0,0]\n"
"v_cvt_pk_bf16_f32 v123, v174, v175\n"
"ds_read_b64 v[190:191], v119 offset:136\n"
"v_fma_mix_f32 v228, v136, v236, v237 op_sel_hi:[1,0,0]\n"
"v_fma_mix_f32 v229, v136, v236, v237 op_sel:[1,0,0] op_sel_hi:[1,0,0]\n"
"v_cvt_pk_bf16_f32 v135, v234, v235\n"
"v_fma_mix_f32 v170, v125, v176, v177 op_sel_hi:[1,0,0]\n"
"v_fma_mix_f32 v171, v125, v176, v177 op_sel:[1,0,0] op_sel_hi:[1,0,0]\n"
"v_cvt_pk_bf16_f32 v124, v168, v169\n"
"ds_read_b64 v[192:193], v119 offset:400\n"
"v_fma_mix_f32 v230, v137, v236, v237 op_sel_hi:[1,0,0]\n"
"v_fma_mix_f32 v231, v137, v236, v237 op_sel:[1,0,0] op_sel_hi:[1,0,0]\n"
"v_cvt_pk_bf16_f32 v136, v228, v229\n"
"v_fma_mix_f32 v172, v126, v176, v177 op_sel_hi:[1,0,0]\n"
"v_fma_mix_f32 v173, v126, v176, v177 op_sel:[1,0,0] op_sel_hi:[1,0,0]\n"
"v_cvt_pk_bf16_f32 v125, v170, v171\n"
"ds_read_b64 v[194:195], v119 offset:408\n"
"v_fma_mix_f32 v232, v138, v236, v237 op_sel_hi:[1,0,0]\n"
"v_fma_mix_f32 v233, v138, v236, v237 op_sel:[1,0,0] op_sel_hi:[1,0,0]\n"
"v_cvt_pk_bf16_f32 v137, v230, v231\n"
"v_fma_mix_f32 v174, v127, v176, v177 op_sel_hi:[1,0,0]\n"
"v_fma_mix_f32 v175, v127, v176, v177 op_sel:[1,0,0] op_sel_hi:[1,0,0]\n"
"v_cvt_pk_bf16_f32 v126, v172, v173\n"
"ds_read_b64 v[196:197], v119 offset:464\n"
"v_fma_mix_f32 v234, v139, v236, v237 op_sel_hi:[1,0,0]\n"
"v_fma_mix_f32 v235, v139, v236, v237 op_sel:[1,0,0] op_sel_hi:[1,0,0]\n"
"v_cvt_pk_bf16_f32 v138, v232, v233\n"
"v_fma_mix_f32 v168, v128, v176, v177 op_sel_hi:[1,0,0]\n"
"v_fma_mix_f32 v169, v128, v176, v177 op_sel:[1,0,0] op_sel_hi:[1,0,0]\n"
"v_cvt_pk_bf16_f32 v127, v174, v175\n"
"ds_read_b64 v[198:199], v119 offset:472\n"
"v_fma_mix_f32 v228, v140, v236, v237 op_sel_hi:[1,0,0]\n"
"v_fma_mix_f32 v229, v140, v236, v237 op_sel:[1,0,0] op_sel_hi:[1,0,0]\n"
"v_cvt_pk_bf16_f32 v139, v234, v235\n"
"v_fma_mix_f32 v170, v129, v176, v177 op_sel_hi:[1,0,0]\n"
"v_fma_mix_f32 v171, v129, v176, v177 op_sel:[1,0,0] op_sel_hi:[1,0,0]\n"
"v_cvt_pk_bf16_f32 v128, v168, v169\n"
"v_fma_mix_f32 v230, v141, v236, v237 op_sel_hi:[1,0,0]\n"
"v_fma_mix_f32 v231, v141, v236, v237 op_sel:[1,0,0] op_sel_hi:[1,0,0]\n"
"v_cvt_pk_bf16_f32 v140, v228, v229\n"
"ds_read_b64 v[200:201], v119 offset:528\n"
"v_fma_mix_f32 v172, v130, v176, v177 op_sel_hi:[1,0,0]\n"
"v_fma_mix_f32 v173, v130, v176, v177 op_sel:[1,0,0] op_sel_hi:[1,0,0]\n"
"v_cvt_pk_bf16_f32 v129, v170, v171\n"
"v_fma_mix_f32 v232, v142, v236, v237 op_sel_hi:[1,0,0]\n"
"v_fma_mix_f32 v233, v142, v236, v237 op_sel:[1,0,0] op_sel_hi:[1,0,0]\n"
"v_cvt_pk_bf16_f32 v141, v230, v231\n"
"ds_read_b64 v[202:203], v119 offset:536\n"
"v_fma_mix_f32 v174, v131, v176, v177 op_sel_hi:[1,0,0]\n"
"v_fma_mix_f32 v175, v131, v176, v177 op_sel:[1,0,0] op_sel_hi:[1,0,0]\n"
"v_cvt_pk_bf16_f32 v130, v172, v173\n"
"v_fma_mix_f32 v234, v143, v236, v237 op_sel_hi:[1,0,0]\n"
"v_fma_mix_f32 v235, v143, v236, v237 op_sel:[1,0,0] op_sel_hi:[1,0,0]\n"
"v_cvt_pk_bf16_f32 v142, v232, v233\n"
"v_cvt_pk_bf16_f32 v131, v174, v175\n"
"v_cvt_pk_bf16_f32 v143, v234, v235\n"
"s_waitcnt vmcnt(0)\n"
"s_waitcnt lgkmcnt(0)\n"
"s_barrier\n"
"ds_read_b64 v[30:31], v116 offset:22784\n"
"ds_read_b64 v[36:37], v116 offset:23184\n"
"ds_read_b64 v[42:43], v116 offset:23584\n"
"ds_read_b64 v[32:33], v115 offset:22784\n"
"ds_read_b64 v[34:35], v115 offset:28480\n"
"ds_read_b64 v[38:39], v115 offset:23184\n"
"ds_read_b64 v[40:41], v115 offset:28880\n"
"ds_read_b64 v[44:45], v115 offset:23584\n"
"ds_read_b64 v[46:47], v115 offset:29280\n"
"ds_read_b64 v[48:49], v116 offset:23984\n"
"ds_read_b64 v[50:51], v115 offset:23984\n"
"s_waitcnt lgkmcnt(8)\n"
"v_mfma_f32_16x16x16_bf16 v[90:93], v[0:1], v[30:31], 0\n"
"s_add_u32 m0, s86, 56960\n"
"s_mul_i32 s68, s65, 20\n"
"s_add_u32 s68, s85, s68\n"
"s_mul_i32 s68, s68, 73728\n"
"buffer_load_dwordx4 v252, s[36:39], s68 offen lds\n"
"s_add_u32 m0, s86, 57984\n"
"ds_read_b64 v[52:53], v115 offset:29680\n"
"ds_read_b64 v[54:55], v116 offset:24384\n"
"v_mfma_f32_16x16x16_bf16 v[94:97], v[0:1], v[36:37], 0\n"
"ds_read_b64 v[56:57], v115 offset:24384\n"
"ds_read_b64 v[58:59], v115 offset:30080\n"
"v_mfma_f32_16x16x16_bf16 v[98:101], v[0:1], v[42:43], 0\n"
"ds_read_b64 v[60:61], v116 offset:22848\n"
"s_waitcnt lgkmcnt(10)\n"
"v_smfmac_f32_16x16x64_bf16 v[90:93], v[2:5], v[32:39], v114\n"
"buffer_load_dwordx4 v253, s[36:39], s68 offen lds\n"
"s_add_u32 m0, s86, 59008\n"
"ds_read_b64 v[66:67], v116 offset:23248\n"
"ds_read_b64 v[72:73], v116 offset:23648\n"
"s_waitcnt lgkmcnt(10)\n"
"v_smfmac_f32_16x16x64_bf16 v[94:97], v[2:5], v[38:45], v114\n"
"ds_read_b64 v[62:63], v115 offset:22848\n"
"ds_read_b64 v[64:65], v115 offset:28544\n"
"s_waitcnt lgkmcnt(11)\n"
"v_smfmac_f32_16x16x64_bf16 v[90:93], v[6:9], v[40:47], v114\n"
"ds_read_b64 v[68:69], v115 offset:23248\n"
"s_waitcnt lgkmcnt(10)\n"
"v_smfmac_f32_16x16x64_bf16 v[98:101], v[2:5], v[44:51], v114\n"
"ds_read_b64 v[70:71], v115 offset:28944\n"
"ds_read_b64 v[74:75], v115 offset:23648\n"
"s_waitcnt lgkmcnt(11)\n"
"v_smfmac_f32_16x16x64_bf16 v[94:97], v[6:9], v[46:53], v114\n"
"buffer_load_dwordx4 v254, s[36:39], s68 offen lds\n"
"s_add_u32 m0, s86, 60032\n"
"ds_read_b64 v[76:77], v115 offset:29344\n"
"ds_read_b64 v[78:79], v116 offset:24048\n"
"s_waitcnt lgkmcnt(10)\n"
"v_smfmac_f32_16x16x64_bf16 v[98:101], v[6:9], v[52:59], v114\n"
"ds_read_b64 v[80:81], v115 offset:24048\n"
"v_cvt_pk_f16_f32 v90, v90, v91\n"
"v_cvt_pk_f16_f32 v91, v92, v93\n"
"s_waitcnt lgkmcnt(8)\n"
"v_mfma_f32_16x16x16_bf16 v[102:105], v[10:11], v[60:61], 0\n"
"ds_write_b64 v117, v[90:91] offset:0\n"
"ds_read_b64 v[82:83], v115 offset:29744\n"
"ds_read_b64 v[84:85], v116 offset:24448\n"
"v_mfma_f32_16x16x16_bf16 v[106:109], v[10:11], v[66:67], 0\n"
"ds_read_b64 v[86:87], v115 offset:24448\n"
"ds_read_b64 v[88:89], v115 offset:30144\n"
"v_mfma_f32_16x16x16_bf16 v[110:113], v[10:11], v[72:73], 0\n"
"buffer_load_dwordx4 v166, s[36:39], s68 offen lds\n"
"s_add_u32 m0, s86, 61056\n"
"ds_read_b64 v[30:31], v116 offset:22912\n"
"v_cvt_pk_f16_f32 v94, v94, v95\n"
"v_cvt_pk_f16_f32 v95, v96, v97\n"
"s_waitcnt lgkmcnt(11)\n"
"v_smfmac_f32_16x16x64_bf16 v[102:105], v[12:15], v[62:69], v114\n"
"ds_write_b64 v117, v[94:95] offset:3072\n"
"ds_read_b64 v[36:37], v116 offset:23312\n"
"ds_read_b64 v[42:43], v116 offset:23712\n"
"s_waitcnt lgkmcnt(12)\n"
"v_smfmac_f32_16x16x64_bf16 v[106:109], v[12:15], v[68:75], v114\n"
"ds_read_b64 v[32:33], v115 offset:22912\n"
"ds_read_b64 v[34:35], v115 offset:28608\n"
"v_cvt_pk_f16_f32 v98, v98, v99\n"
"v_cvt_pk_f16_f32 v99, v100, v101\n"
"s_waitcnt lgkmcnt(13)\n"
"v_smfmac_f32_16x16x64_bf16 v[102:105], v[16:19], v[70:77], v114\n"
"ds_write_b64 v117, v[98:99] offset:6144\n"
"ds_read_b64 v[38:39], v115 offset:23312\n"
"s_waitcnt lgkmcnt(13)\n"
"v_smfmac_f32_16x16x64_bf16 v[110:113], v[12:15], v[74:81], v114\n"
"buffer_load_dwordx4 v167, s[36:39], s68 offen lds\n"
"s_add_u32 m0, s86, 62080\n"
"ds_read_b64 v[40:41], v115 offset:29008\n"
"ds_read_b64 v[44:45], v115 offset:23712\n"
"s_waitcnt lgkmcnt(13)\n"
"v_smfmac_f32_16x16x64_bf16 v[106:109], v[16:19], v[76:83], v114\n"
"ds_read_b64 v[46:47], v115 offset:29408\n"
"ds_read_b64 v[48:49], v116 offset:24112\n"
"s_waitcnt lgkmcnt(12)\n"
"v_smfmac_f32_16x16x64_bf16 v[110:113], v[16:19], v[82:89], v114\n"
"ds_read_b64 v[50:51], v115 offset:24112\n"
"v_cvt_pk_f16_f32 v102, v102, v103\n"
"v_cvt_pk_f16_f32 v103, v104, v105\n"
"s_waitcnt lgkmcnt(9)\n"
"v_mfma_f32_16x16x16_bf16 v[90:93], v[20:21], v[30:31], 0\n"
"ds_write_b64 v117, v[102:103] offset:1024\n"
"ds_read_b64 v[52:53], v115 offset:29808\n"
"ds_read_b64 v[54:55], v116 offset:24512\n"
"v_mfma_f32_16x16x16_bf16 v[94:97], v[20:21], v[36:37], 0\n"
"s_mov_b64 exec, 0x1fffffff\n"
"buffer_load_dwordx4 v178, s[36:39], s68 offen lds\n"
"s_mov_b64 exec, -1\n"
"ds_read_b64 v[56:57], v115 offset:24512\n"
"ds_read_b64 v[58:59], v115 offset:30208\n"
"v_mfma_f32_16x16x16_bf16 v[98:101], v[20:21], v[42:43], 0\n"
"v_cvt_pk_f16_f32 v106, v106, v107\n"
"v_cvt_pk_f16_f32 v107, v108, v109\n"
"s_waitcnt lgkmcnt(10)\n"
"v_smfmac_f32_16x16x64_bf16 v[90:93], v[22:25], v[32:39], v114\n"
"ds_write_b64 v117, v[106:107] offset:4096\n"
"s_waitcnt lgkmcnt(9)\n"
"v_smfmac_f32_16x16x64_bf16 v[94:97], v[22:25], v[38:45], v114\n"
"v_cvt_pk_f16_f32 v110, v110, v111\n"
"v_cvt_pk_f16_f32 v111, v112, v113\n"
"s_waitcnt lgkmcnt(8)\n"
"v_smfmac_f32_16x16x64_bf16 v[90:93], v[26:29], v[40:47], v114\n"
"ds_write_b64 v117, v[110:111] offset:7168\n"
"s_waitcnt lgkmcnt(7)\n"
"v_smfmac_f32_16x16x64_bf16 v[98:101], v[22:25], v[44:51], v114\n"
"s_waitcnt lgkmcnt(5)\n"
"v_smfmac_f32_16x16x64_bf16 v[94:97], v[26:29], v[46:53], v114\n"
"s_waitcnt lgkmcnt(2)\n"
"v_smfmac_f32_16x16x64_bf16 v[98:101], v[26:29], v[52:59], v114\n"
"s_nop 0\n"
"v_cvt_pk_f16_f32 v90, v90, v91\n"
"v_cvt_pk_f16_f32 v91, v92, v93\n"
"s_nop 2\n"
"v_cvt_pk_f16_f32 v94, v94, v95\n"
"v_cvt_pk_f16_f32 v95, v96, v97\n"
"v_cvt_pk_f16_f32 v98, v98, v99\n"
"v_cvt_pk_f16_f32 v99, v100, v101\n"
"ds_write_b64 v117, v[90:91] offset:2048\n"
"ds_write_b64 v117, v[94:95] offset:5120\n"
"ds_write_b64 v117, v[98:99] offset:8192\n"
"s_waitcnt lgkmcnt(0)\n"
"s_barrier\n"
"ds_read_b64 v[144:145], v118 offset:0\n"
"ds_read_b64 v[146:147], v118 offset:512\n"
"ds_read_b64 v[148:149], v118 offset:1024\n"
"ds_read_b64 v[150:151], v118 offset:1536\n"
"ds_read_b64 v[152:153], v118 offset:2048\n"
"ds_read_b64 v[154:155], v118 offset:2560\n"
"ds_read_b64 v[156:157], v118 offset:3072\n"
"ds_read_b64 v[158:159], v118 offset:3584\n"
"ds_read_b64 v[160:161], v118 offset:4096\n"
"ds_read_b64 v[162:163], v118 offset:4608\n"
"ds_read_b64 v[164:165], v118 offset:5120\n"
"ds_read_b64 v[166:167], v118 offset:5632\n"
"ds_read_b64 v[204:205], v119 offset:22784\n"
"ds_read_b64 v[206:207], v119 offset:22792\n"
"ds_read_b64 v[208:209], v119 offset:22848\n"
"ds_read_b64 v[210:211], v119 offset:22856\n"
"ds_read_b64 v[212:213], v119 offset:22912\n"
"ds_read_b64 v[214:215], v119 offset:22920\n"
"ds_read_b64 v[216:217], v119 offset:23184\n"
"ds_read_b64 v[218:219], v119 offset:23192\n"
"ds_read_b64 v[220:221], v119 offset:23248\n"
"ds_read_b64 v[222:223], v119 offset:23256\n"
"ds_read_b64 v[224:225], v119 offset:23312\n"
"ds_read_b64 v[226:227], v119 offset:23320\n"
"v_bfe_u32 v113, v251, 3, 1\n"
"v_mul_u32_u24 v113, 9984, v113\n"
"v_add_u32 v117, v117, v113\n"
"s_lshr_b32 s66, s59, 1\n"
"s_mul_i32 s66, s66, 9984\n"
"v_add_u32 v118, s66, v118\n"
"s_waitcnt lgkmcnt(14)\n"
"v_mfma_f32_16x16x32_f16 v[168:171], v[144:147], v[144:147], 0\n"
"v_mfma_f32_16x16x32_f16 v[168:171], v[148:151], v[148:151], v[168:171]\n"
"v_mfma_f32_16x16x32_f16 v[168:171], v[152:155], v[152:155], v[168:171]\n"
"v_mfma_f32_16x16x32_f16 v[172:175], v[240:243], v[144:147], 0\n"
"v_mfma_f32_16x16x32_f16 v[172:175], v[240:243], v[148:151], v[172:175]\n"
"v_mfma_f32_16x16x32_f16 v[172:175], v[240:243], v[152:155], v[172:175]\n"
"v_mfma_f32_16x16x32_f16 v[228:231], v[156:159], v[156:159], 0\n"
"v_mfma_f32_16x16x32_f16 v[228:231], v[160:163], v[160:163], v[228:231]\n"
"s_waitcnt lgkmcnt(12)\n"
"v_mfma_f32_16x16x32_f16 v[228:231], v[164:167], v[164:167], v[228:231]\n"
"v_mfma_f32_16x16x32_f16 v[232:235], v[240:243], v[156:159], 0\n"
"v_mfma_f32_16x16x32_f16 v[232:235], v[240:243], v[160:163], v[232:235]\n"
"v_mfma_f32_16x16x32_f16 v[232:235], v[240:243], v[164:167], v[232:235]\n"
"s_waitcnt vmcnt(0)\n"
"s_waitcnt lgkmcnt(0)\n"
"s_barrier\n"
"v_cndmask_b32 v176, 0, v168, s[72:73]\n"
"s_nop 0\n"
"v_cndmask_b32 v236, 0, v228, s[72:73]\n"
"v_cndmask_b32 v176, v176, v169, s[74:75]\n"
"v_cndmask_b32 v236, v236, v229, s[74:75]\n"
"v_cndmask_b32 v176, v176, v170, s[76:77]\n"
"v_cndmask_b32 v236, v236, v230, s[76:77]\n"
"v_cndmask_b32 v176, v176, v171, s[78:79]\n"
"v_cndmask_b32 v236, v236, v231, s[78:79]\n"
"v_mul_f32 v172, s82, v172\n"
"v_mul_f32 v232, s82, v232\n"
"v_fma_f32 v173, -v172, v172, s83\n"
"v_fma_f32 v233, -v232, v232, s83\n"
"v_mfma_f32_16x16x4_f32 v[168:171], v250, v176, 0\n"
"v_mfma_f32_16x16x4_f32 v[228:231], v250, v236, 0\n"
"s_mul_i32 s87, s93, 6144\n"
"s_add_u32 s87, s87, s89\n"
"s_add_u32 m0, s87, 24320\n"
"s_add_u32 s66, s92, 0\n"
"buffer_load_dwordx4 v255, s[40:43], s66 offen lds\n"
"s_nop 5\n"
"v_fma_f32 v176, v168, s82, v173\n"
"v_fma_f32 v236, v228, s82, v233\n"
"v_mul_f32 v168, s71, v168\n"
"v_mul_f32 v228, s71, v228\n"
"v_cmp_nge_f32 vcc, v176, v168\n"
"v_cmp_nge_f32 s[54:55], v236, v228\n"
"v_rsq_f32 v176, v176\n"
"v_rsq_f32 v236, v236\n"
"s_or_b64 s[80:81], s[80:81], vcc\n"
"s_or_b64 s[80:81], s[80:81], s[54:55]\n"
"v_mul_f32 v176, s84, v176\n"
"v_mul_f32 v236, s84, v236\n"
"v_mul_f32 v177, -v172, v176\n"
"v_mul_f32 v237, -v232, v236\n"
"v_fma_mix_f32 v168, v144, v176, v177 op_sel_hi:[1,0,0]\n"
"v_fma_mix_f32 v169, v144, v176, v177 op_sel:[1,0,0] op_sel_hi:[1,0,0]\n"
"v_fma_mix_f32 v228, v156, v236, v237 op_sel_hi:[1,0,0]\n"
"v_fma_mix_f32 v229, v156, v236, v237 op_sel:[1,0,0] op_sel_hi:[1,0,0]\n"
"v_fma_mix_f32 v170, v145, v176, v177 op_sel_hi:[1,0,0]\n"
"v_fma_mix_f32 v171, v145, v176, v177 op_sel:[1,0,0] op_sel_hi:[1,0,0]\n"
"v_cvt_pk_bf16_f32 v144, v168, v169\n"
"v_fma_mix_f32 v230, v157, v236, v237 op_sel_hi:[1,0,0]\n"
"v_fma_mix_f32 v231, v157, v236, v237 op_sel:[1,0,0] op_sel_hi:[1,0,0]\n"
"v_cvt_pk_bf16_f32 v156, v228, v229\n"
"v_fma_mix_f32 v172, v146, v176, v177 op_sel_hi:[1,0,0]\n"
"v_fma_mix_f32 v173, v146, v176, v177 op_sel:[1,0,0] op_sel_hi:[1,0,0]\n"
"v_cvt_pk_bf16_f32 v145, v170, v171\n"
"v_fma_mix_f32 v232, v158, v236, v237 op_sel_hi:[1,0,0]\n"
"v_fma_mix_f32 v233, v158, v236, v237 op_sel:[1,0,0] op_sel_hi:[1,0,0]\n"
"v_cvt_pk_bf16_f32 v157, v230, v231\n"
"v_fma_mix_f32 v174, v147, v176, v177 op_sel_hi:[1,0,0]\n"
"v_fma_mix_f32 v175, v147, v176, v177 op_sel:[1,0,0] op_sel_hi:[1,0,0]\n"
"v_cvt_pk_bf16_f32 v146, v172, v173\n"
"v_fma_mix_f32 v234, v159, v236, v237 op_sel_hi:[1,0,0]\n"
"v_fma_mix_f32 v235, v159, v236, v237 op_sel:[1,0,0] op_sel_hi:[1,0,0]\n"
"v_cvt_pk_bf16_f32 v158, v232, v233\n"
"s_add_u32 m0, s87, 25344\n"
"s_add_u32 s66, s92, 24576\n"
"buffer_load_dwordx4 v255, s[40:43], s66 offen lds\n"
"v_fma_mix_f32 v168, v148, v176, v177 op_sel_hi:[1,0,0]\n"
"v_fma_mix_f32 v169, v148, v176, v177 op_sel:[1,0,0] op_sel_hi:[1,0,0]\n"
"v_cvt_pk_bf16_f32 v147, v174, v175\n"
"v_fma_mix_f32 v228, v160, v236, v237 op_sel_hi:[1,0,0]\n"
"v_fma_mix_f32 v229, v160, v236, v237 op_sel:[1,0,0] op_sel_hi:[1,0,0]\n"
"v_cvt_pk_bf16_f32 v159, v234, v235\n"
"v_fma_mix_f32 v170, v149, v176, v177 op_sel_hi:[1,0,0]\n"
"v_fma_mix_f32 v171, v149, v176, v177 op_sel:[1,0,0] op_sel_hi:[1,0,0]\n"
"v_cvt_pk_bf16_f32 v148, v168, v169\n"
"v_fma_mix_f32 v230, v161, v236, v237 op_sel_hi:[1,0,0]\n"
"v_fma_mix_f32 v231, v161, v236, v237 op_sel:[1,0,0] op_sel_hi:[1,0,0]\n"
"v_cvt_pk_bf16_f32 v160, v228, v229\n"
"v_fma_mix_f32 v172, v150, v176, v177 op_sel_hi:[1,0,0]\n"
"v_fma_mix_f32 v173, v150, v176, v177 op_sel:[1,0,0] op_sel_hi:[1,0,0]\n"
"v_cvt_pk_bf16_f32 v149, v170, v171\n"
"v_fma_mix_f32 v232, v162, v236, v237 op_sel_hi:[1,0,0]\n"
"v_fma_mix_f32 v233, v162, v236, v237 op_sel:[1,0,0] op_sel_hi:[1,0,0]\n"
"v_cvt_pk_bf16_f32 v161, v230, v231\n"
"v_fma_mix_f32 v174, v151, v176, v177 op_sel_hi:[1,0,0]\n"
"v_fma_mix_f32 v175, v151, v176, v177 op_sel:[1,0,0] op_sel_hi:[1,0,0]\n"
"v_cvt_pk_bf16_f32 v150, v172, v173\n"
"v_fma_mix_f32 v234, v163, v236, v237 op_sel_hi:[1,0,0]\n"
"v_fma_mix_f32 v235, v163, v236, v237 op_sel:[1,0,0] op_sel_hi:[1,0,0]\n"
"v_cvt_pk_bf16_f32 v162, v232, v233\n"
"v_fma_mix_f32 v168, v152, v176, v177 op_sel_hi:[1,0,0]\n"
"v_fma_mix_f32 v169, v152, v176, v177 op_sel:[1,0,0] op_sel_hi:[1,0,0]\n"
"v_cvt_pk_bf16_f32 v151, v174, v175\n"
"s_add_u32 m0, s87, 26368\n"
"s_add_u32 s66, s92, 49152\n"
"buffer_load_dwordx4 v255, s[40:43], s66 offen lds\n"
"v_fma_mix_f32 v228, v164, v236, v237 op_sel_hi:[1,0,0]\n"
"v_fma_mix_f32 v229, v164, v236, v237 op_sel:[1,0,0] op_sel_hi:[1,0,0]\n"
"v_cvt_pk_bf16_f32 v163, v234, v235\n"
"v_fma_mix_f32 v170, v153, v176, v177 op_sel_hi:[1,0,0]\n"
"v_fma_mix_f32 v171, v153, v176, v177 op_sel:[1,0,0] op_sel_hi:[1,0,0]\n"
"v_cvt_pk_bf16_f32 v152, v168, v169\n"
"v_fma_mix_f32 v230, v165, v236, v237 op_sel_hi:[1,0,0]\n"
"v_fma_mix_f32 v231, v165, v236, v237 op_sel:[1,0,0] op_sel_hi:[1,0,0]\n"
"v_cvt_pk_bf16_f32 v164, v228, v229\n"
"v_fma_mix_f32 v172, v154, v176, v177 op_sel_hi:[1,0,0]\n"
"v_fma_mix_f32 v173, v154, v176, v177 op_sel:[1,0,0] op_sel_hi:[1,0,0]\n"
"v_cvt_pk_bf16_f32 v153, v170, v171\n"
"v_fma_mix_f32 v232, v166, v236, v237 op_sel_hi:[1,0,0]\n"
"v_fma_mix_f32 v233, v166, v236, v237 op_sel:[1,0,0] op_sel_hi:[1,0,0]\n"
"v_cvt_pk_bf16_f32 v165, v230, v231\n"
"v_fma_mix_f32 v174, v155, v176, v177 op_sel_hi:[1,0,0]\n"
"v_fma_mix_f32 v175, v155, v176, v177 op_sel:[1,0,0] op_sel_hi:[1,0,0]\n"
"v_cvt_pk_bf16_f32 v154, v172, v173\n"
"v_fma_mix_f32 v234, v167, v236, v237 op_sel_hi:[1,0,0]\n"
"v_fma_mix_f32 v235, v167, v236, v237 op_sel:[1,0,0] op_sel_hi:[1,0,0]\n"
"v_cvt_pk_bf16_f32 v166, v232, v233\n"
"v_cvt_pk_bf16_f32 v155, v174, v175\n"
"v_cvt_pk_bf16_f32 v167, v234, v235\n"
"ds_read_b64 v[30:31], v116 offset:45568\n"
"ds_read_b64 v[36:37], v116 offset:45968\n"
"ds_read_b64 v[42:43], v116 offset:46368\n"
"ds_read_b64 v[32:33], v115 offset:45568\n"
"ds_read_b64 v[34:35], v115 offset:51264\n"
"ds_read_b64 v[38:39], v115 offset:45968\n"
"ds_read_b64 v[40:41], v115 offset:51664\n"
"ds_read_b64 v[44:45], v115 offset:46368\n"
"ds_read_b64 v[46:47], v115 offset:52064\n"
"ds_read_b64 v[48:49], v116 offset:46768\n"
"ds_read_b64 v[50:51], v115 offset:46768\n"
"ds_read_b64 v[52:53], v115 offset:52464\n"
"ds_read_b64 v[54:55], v116 offset:47168\n"
"ds_read_b64 v[56:57], v115 offset:47168\n"
"ds_read_b64 v[58:59], v115 offset:52864\n"
"s_waitcnt lgkmcnt(12)\n"
"v_mfma_f32_16x16x16_bf16 v[90:93], v[0:1], v[30:31], 0\n"
"ds_read_b64 v[60:61], v116 offset:45632\n"
"ds_read_b64 v[66:67], v116 offset:46032\n"
"v_mfma_f32_16x16x16_bf16 v[94:97], v[0:1], v[36:37], 0\n"
"ds_read_b64 v[72:73], v116 offset:46432\n"
"ds_read_b64 v[62:63], v115 offset:45632\n"
"v_mfma_f32_16x16x16_bf16 v[98:101], v[0:1], v[42:43], 0\n"
"ds_read_b64 v[64:65], v115 offset:51328\n"
"s_waitcnt lgkmcnt(14)\n"
"v_smfmac_f32_16x16x64_bf16 v[90:93], v[2:5], v[32:39], v114\n"
"ds_read_b64 v[68:69], v115 offset:46032\n"
"ds_read_b64 v[70:71], v115 offset:51728\n"
"s_waitcnt lgkmcnt(14)\n"
"v_smfmac_f32_16x16x64_bf16 v[94:97], v[2:5], v[38:45], v114\n"
"ds_read_b64 v[74:75], v115 offset:46432\n"
"ds_read_b64 v[76:77], v115 offset:52128\n"
"s_waitcnt lgkmcnt(14)\n"
"v_smfmac_f32_16x16x64_bf16 v[90:93], v[6:9], v[40:47], v114\n"
"ds_read_b64 v[78:79], v116 offset:46832\n"
"s_waitcnt lgkmcnt(14)\n"
"v_smfmac_f32_16x16x64_bf16 v[98:101], v[2:5], v[44:51], v114\n"
"ds_read_b64 v[80:81], v115 offset:46832\n"
"ds_read_b64 v[82:83], v115 offset:52528\n"
"s_waitcnt lgkmcnt(14)\n"
"v_smfmac_f32_16x16x64_bf16 v[94:97], v[6:9], v[46:53], v114\n"
"ds_read_b64 v[84:85], v116 offset:47232\n"
"ds_read_b64 v[86:87], v115 offset:47232\n"
"s_waitcnt lgkmcnt(14)\n"
"v_smfmac_f32_16x16x64_bf16 v[98:101], v[6:9], v[52:59], v114\n"
"ds_read_b64 v[88:89], v115 offset:52928\n"
"v_cvt_pk_f16_f32 v90, v90, v91\n"
"v_cvt_pk_f16_f32 v91, v92, v93\n"
"s_waitcnt lgkmcnt(12)\n"
"v_mfma_f32_16x16x16_bf16 v[102:105], v[10:11], v[60:61], 0\n"
"ds_write_b64 v117, v[90:91] offset:0\n"
"ds_read_b64 v[30:31], v116 offset:45696\n"
"ds_read_b64 v[36:37], v116 offset:46096\n"
"v_mfma_f32_16x16x16_bf16 v[106:109], v[10:11], v[66:67], 0\n"
"ds_read_b64 v[42:43], v116 offset:46496\n"
"ds_read_b64 v[32:33], v115 offset:45696\n"
"v_mfma_f32_16x16x16_bf16 v[110:113], v[10:11], v[72:73], 0\n"
"ds_read_b64 v[34:35], v115 offset:51392\n"
"v_cvt_pk_f16_f32 v94, v94, v95\n"
"v_cvt_pk_f16_f32 v95, v96, v97\n"
"s_waitcnt lgkmcnt(14)\n"
"v_smfmac_f32_16x16x64_bf16 v[102:105], v[12:15], v[62:69], v114\n"
"ds_write_b64 v117, v[94:95] offset:3072\n"
"ds_read_b64 v[38:39], v115 offset:46096\n"
"ds_read_b64 v[40:41], v115 offset:51792\n"
"s_waitcnt lgkmcnt(14)\n"
"v_smfmac_f32_16x16x64_bf16 v[106:109], v[12:15], v[68:75], v114\n"
"ds_read_b64 v[44:45], v115 offset:46496\n"
"ds_read_b64 v[46:47], v115 offset:52192\n"
"v_cvt_pk_f16_f32 v98, v98, v99\n"
"v_cvt_pk_f16_f32 v99, v100, v101\n"
"v_smfmac_f32_16x16x64_bf16 v[102:105], v[16:19], v[70:77], v114\n"
"ds_write_b64 v117, v[98:99] offset:6144\n"
"ds_read_b64 v[48:49], v116 offset:46896\n"
"s_waitcnt lgkmcnt(14)\n"
"v_smfmac_f32_16x16x64_bf16 v[110:113], v[12:15], v[74:81], v114\n"
"ds_read_b64 v[50:51], v115 offset:46896\n"
"ds_read_b64 v[52:53], v115 offset:52592\n"
"v_smfmac_f32_16x16x64_bf16 v[106:109], v[16:19], v[76:83], v114\n"
"ds_read_b64 v[54:55], v116 offset:47296\n"
"ds_read_b64 v[56:57], v115 offset:47296\n"
"s_waitcnt lgkmcnt(14)\n"
"v_smfmac_f32_16x16x64_bf16 v[110:113], v[16:19], v[82:89], v114\n"
"ds_read_b64 v[58:59], v115 offset:52992\n"
"v_cvt_pk_f16_f32 v102, v102, v103\n"
"v_cvt_pk_f16_f32 v103, v104, v105\n"
"s_waitcnt lgkmcnt(14)\n"
"v_mfma_f32_16x16x16_bf16 v[90:93], v[20:21], v[30:31], 0\n"
"ds_write_b64 v117, v[102:103] offset:1024\n"
"v_mfma_f32_16x16x16_bf16 v[94:97], v[20:21], v[36:37], 0\n"
"v_mfma_f32_16x16x16_bf16 v[98:101], v[20:21], v[42:43], 0\n"
"v_cvt_pk_f16_f32 v106, v106, v107\n"
"v_cvt_pk_f16_f32 v107, v108, v109\n"
"s_waitcnt lgkmcnt(11)\n"
"v_smfmac_f32_16x16x64_bf16 v[90:93], v[22:25], v[32:39], v114\n"
"ds_write_b64 v117, v[106:107] offset:4096\n"
"s_waitcnt lgkmcnt(10)\n"
"v_smfmac_f32_16x16x64_bf16 v[94:97], v[22:25], v[38:45], v114\n"
"v_cvt_pk_f16_f32 v110, v110, v111\n"
"v_cvt_pk_f16_f32 v111, v112, v113\n"
"s_waitcnt lgkmcnt(9)\n"
"v_smfmac_f32_16x16x64_bf16 v[90:93], v[26:29], v[40:47], v114\n"
"ds_write_b64 v117, v[110:111] offset:7168\n"
"s_waitcnt lgkmcnt(7)\n"
"v_smfmac_f32_16x16x64_bf16 v[98:101], v[22:25], v[44:51], v114\n"
"s_waitcnt lgkmcnt(6)\n"
"v_smfmac_f32_16x16x64_bf16 v[94:97], v[26:29], v[46:53], v114\n"
"s_waitcnt lgkmcnt(3)\n"
"v_smfmac_f32_16x16x64_bf16 v[98:101], v[26:29], v[52:59], v114\n"
"s_nop 0\n"
"v_cvt_pk_f16_f32 v90, v90, v91\n"
"v_cvt_pk_f16_f32 v91, v92, v93\n"
"s_nop 2\n"
"v_cvt_pk_f16_f32 v94, v94, v95\n"
"v_cvt_pk_f16_f32 v95, v96, v97\n"
"v_cvt_pk_f16_f32 v98, v98, v99\n"
"v_cvt_pk_f16_f32 v99, v100, v101\n"
"ds_write_b64 v117, v[90:91] offset:2048\n"
"ds_write_b64 v117, v[94:95] offset:5120\n"
"ds_write_b64 v117, v[98:99] offset:8192\n"
"s_waitcnt lgkmcnt(0)\n"
"s_barrier\n"
"ds_read_b64 v[168:169], v118 offset:0\n"
"ds_read_b64 v[170:171], v118 offset:512\n"
"ds_read_b64 v[172:173], v118 offset:1024\n"
"ds_read_b64 v[174:175], v118 offset:1536\n"
"ds_read_b64 v[176:177], v118 offset:2048\n"
"ds_read_b64 v[178:179], v118 offset:2560\n"
"ds_read_b64 v[228:229], v119 offset:45568\n"
"ds_read_b64 v[230:231], v119 offset:45576\n"
"ds_read_b64 v[232:233], v119 offset:45632\n"
"ds_read_b64 v[234:235], v119 offset:45640\n"
"ds_read_b64 v[236:237], v119 offset:45696\n"
"ds_read_b64 v[238:239], v119 offset:45704\n"
"v_and_b32 v252, 60, v251\n"
"v_and_b32 v254, 1, v251\n"
"v_lshl_add_u32 v252, v254, 1, v252\n"
"v_bfe_u32 v254, v251, 1, 1\n"
"v_add_lshl_u32 v252, v252, v254, 3\n"
"v_add_u32 v252, 24320, v252\n"
"v_bfe_u32 v254, v251, 4, 1\n"
"v_mul_u32_u24 v253, 3080, v254\n"
"v_lshrrev_b32 v254, 5, v251\n"
"v_mul_u32_u24 v254, 768, v254\n"
"v_add_u32 v253, v253, v254\n"
"v_bfe_u32 v254, v251, 2, 2\n"
"v_mul_u32_u24 v254, 192, v254\n"
"v_add_u32 v253, v253, v254\n"
"v_and_b32 v254, 3, v251\n"
"v_lshl_add_u32 v253, v254, 4, v253\n"
"v_add_u32 v253, 108336, v253\n"
"v_mfma_f32_16x16x32_bf16 v[0:3], v[246:249], v[180:183], 0\n"
"v_mfma_f32_16x16x32_bf16 v[4:7], v[244:247], v[180:183], 0\n"
"v_mfma_f32_16x16x32_bf16 v[8:11], v[246:249], v[184:187], 0\n"
"v_mfma_f32_16x16x32_bf16 v[12:15], v[244:247], v[184:187], 0\n"
"v_mfma_f32_16x16x32_bf16 v[16:19], v[246:249], v[188:191], 0\n"
"v_mfma_f32_16x16x32_bf16 v[20:23], v[244:247], v[188:191], 0\n"
"s_waitcnt lgkmcnt(10)\n"
"v_mfma_f32_16x16x32_f16 v[96:99], v[168:171], v[168:171], 0\n"
"s_waitcnt lgkmcnt(8)\n"
"v_mfma_f32_16x16x32_f16 v[96:99], v[172:175], v[172:175], v[96:99]\n"
"s_waitcnt lgkmcnt(6)\n"
"v_mfma_f32_16x16x32_f16 v[96:99], v[176:179], v[176:179], v[96:99]\n"
"v_mfma_f32_16x16x32_f16 v[100:103], v[240:243], v[168:171], 0\n"
"v_mfma_f32_16x16x32_f16 v[100:103], v[240:243], v[172:175], v[100:103]\n"
"v_mfma_f32_16x16x32_f16 v[100:103], v[240:243], v[176:179], v[100:103]\n"
"v_mfma_f32_16x16x32_bf16 v[24:27], v[246:249], v[192:195], 0\n"
"v_mfma_f32_16x16x32_bf16 v[28:31], v[244:247], v[192:195], 0\n"
"v_mfma_f32_16x16x32_bf16 v[32:35], v[246:249], v[196:199], 0\n"
"v_mfma_f32_16x16x32_bf16 v[36:39], v[244:247], v[196:199], 0\n"
"v_mfma_f32_16x16x32_bf16 v[40:43], v[246:249], v[200:203], 0\n"
"v_mfma_f32_16x16x32_bf16 v[44:47], v[244:247], v[200:203], 0\n"
"v_cndmask_b32 v104, 0, v96, s[72:73]\n"
"v_cndmask_b32 v104, v104, v97, s[74:75]\n"
"v_cndmask_b32 v104, v104, v98, s[76:77]\n"
"v_cndmask_b32 v104, v104, v99, s[78:79]\n"
"v_mul_f32 v100, s82, v100\n"
"v_fma_f32 v101, -v100, v100, s83\n"
"v_mfma_f32_16x16x4_f32 v[96:99], v250, v104, 0\n"
"s_nop 11\n"
"v_fma_f32 v104, v96, s82, v101\n"
"v_mul_f32 v96, s71, v96\n"
"v_cmp_nge_f32 vcc, v104, v96\n"
"v_rsq_f32 v104, v104\n"
"s_or_b64 s[80:81], s[80:81], vcc\n"
"v_mul_f32 v104, s84, v104\n"
"v_mul_f32 v105, -v100, v104\n"
"s_waitcnt vmcnt(0)\n"
"s_waitcnt lgkmcnt(0)\n"
"s_barrier\n"
"ds_read_b64_tr_b16 v[180:181], v252 offset:0\n"
"ds_read_b64_tr_b16 v[182:183], v252 offset:3072\n"
"ds_read_b64_tr_b16 v[184:185], v252 offset:1024\n"
"ds_read_b64_tr_b16 v[186:187], v252 offset:4096\n"
"ds_read_b64_tr_b16 v[188:189], v252 offset:2048\n"
"ds_read_b64_tr_b16 v[190:191], v252 offset:5120\n"
"ds_read_b64_tr_b16 v[192:193], v252 offset:512\n"
"ds_read_b64_tr_b16 v[194:195], v252 offset:3584\n"
"ds_read_b64_tr_b16 v[196:197], v252 offset:1536\n"
"ds_read_b64_tr_b16 v[198:199], v252 offset:4608\n"
"ds_read_b64_tr_b16 v[200:201], v252 offset:2560\n"
"ds_read_b64_tr_b16 v[202:203], v252 offset:5632\n"
"v_fma_mix_f32 v96, v168, v104, v105 op_sel_hi:[1,0,0]\n"
"v_fma_mix_f32 v97, v168, v104, v105 op_sel:[1,0,0] op_sel_hi:[1,0,0]\n"
"s_mul_i32 s87, s93, 6144\n"
"s_add_u32 s87, s87, s89\n"
"s_add_u32 m0, s87, 36608\n"
"s_add_u32 s66, s92, 128\n"
"buffer_load_dwordx4 v255, s[40:43], s66 offen lds\n"
"v_fma_mix_f32 v98, v169, v104, v105 op_sel_hi:[1,0,0]\n"
"v_fma_mix_f32 v99, v169, v104, v105 op_sel:[1,0,0] op_sel_hi:[1,0,0]\n"
"v_cvt_pk_bf16_f32 v168, v96, v97\n"
"v_fma_mix_f32 v100, v170, v104, v105 op_sel_hi:[1,0,0]\n"
"v_fma_mix_f32 v101, v170, v104, v105 op_sel:[1,0,0] op_sel_hi:[1,0,0]\n"
"v_cvt_pk_bf16_f32 v169, v98, v99\n"
"s_add_u32 m0, s87, 37632\n"
"s_add_u32 s66, s92, 24704\n"
"buffer_load_dwordx4 v255, s[40:43], s66 offen lds\n"
"v_fma_mix_f32 v102, v171, v104, v105 op_sel_hi:[1,0,0]\n"
"v_fma_mix_f32 v103, v171, v104, v105 op_sel:[1,0,0] op_sel_hi:[1,0,0]\n"
"v_cvt_pk_bf16_f32 v170, v100, v101\n"
"v_fma_mix_f32 v96, v172, v104, v105 op_sel_hi:[1,0,0]\n"
"v_fma_mix_f32 v97, v172, v104, v105 op_sel:[1,0,0] op_sel_hi:[1,0,0]\n"
"v_cvt_pk_bf16_f32 v171, v102, v103\n"
"s_add_u32 m0, s87, 38656\n"
"s_add_u32 s66, s92, 49280\n"
"buffer_load_dwordx4 v255, s[40:43], s66 offen lds\n"
"v_fma_mix_f32 v98, v173, v104, v105 op_sel_hi:[1,0,0]\n"
"v_fma_mix_f32 v99, v173, v104, v105 op_sel:[1,0,0] op_sel_hi:[1,0,0]\n"
"v_cvt_pk_bf16_f32 v172, v96, v97\n"
"v_fma_mix_f32 v100, v174, v104, v105 op_sel_hi:[1,0,0]\n"
"v_fma_mix_f32 v101, v174, v104, v105 op_sel:[1,0,0] op_sel_hi:[1,0,0]\n"
"v_cvt_pk_bf16_f32 v173, v98, v99\n"
"s_mul_i32 s87, s93, 6144\n"
"s_add_u32 s87, s87, s89\n"
"s_add_u32 m0, s87, 48896\n"
"s_add_u32 s66, s92, 256\n"
"buffer_load_dwordx4 v255, s[40:43], s66 offen lds\n"
"v_fma_mix_f32 v102, v175, v104, v105 op_sel_hi:[1,0,0]\n"
"v_fma_mix_f32 v103, v175, v104, v105 op_sel:[1,0,0] op_sel_hi:[1,0,0]\n"
"v_cvt_pk_bf16_f32 v174, v100, v101\n"
"v_fma_mix_f32 v96, v176, v104, v105 op_sel_hi:[1,0,0]\n"
"v_fma_mix_f32 v97, v176, v104, v105 op_sel:[1,0,0] op_sel_hi:[1,0,0]\n"
"v_cvt_pk_bf16_f32 v175, v102, v103\n"
"s_add_u32 m0, s87, 49920\n"
"s_add_u32 s66, s92, 24832\n"
"buffer_load_dwordx4 v255, s[40:43], s66 offen lds\n"
"v_fma_mix_f32 v98, v177, v104, v105 op_sel_hi:[1,0,0]\n"
"v_fma_mix_f32 v99, v177, v104, v105 op_sel:[1,0,0] op_sel_hi:[1,0,0]\n"
"v_cvt_pk_bf16_f32 v176, v96, v97\n"
"v_fma_mix_f32 v100, v178, v104, v105 op_sel_hi:[1,0,0]\n"
"v_fma_mix_f32 v101, v178, v104, v105 op_sel:[1,0,0] op_sel_hi:[1,0,0]\n"
"v_cvt_pk_bf16_f32 v177, v98, v99\n"
"s_add_u32 m0, s87, 50944\n"
"s_add_u32 s66, s92, 49408\n"
"buffer_load_dwordx4 v255, s[40:43], s66 offen lds\n"
"v_fma_mix_f32 v102, v179, v104, v105 op_sel_hi:[1,0,0]\n"
"v_fma_mix_f32 v103, v179, v104, v105 op_sel:[1,0,0] op_sel_hi:[1,0,0]\n"
"v_cvt_pk_bf16_f32 v178, v100, v101\n"
"v_cvt_pk_bf16_f32 v179, v102, v103\n"
"v_mfma_f32_16x16x32_bf16 v[96:99], v[246:249], v[228:231], 0\n"
"v_mfma_f32_16x16x32_bf16 v[100:103], v[244:247], v[228:231], 0\n"
"v_mfma_f32_16x16x32_bf16 v[104:107], v[246:249], v[232:235], 0\n"
"v_mfma_f32_16x16x32_bf16 v[108:111], v[244:247], v[232:235], 0\n"
"v_mfma_f32_16x16x32_bf16 v[112:115], v[246:249], v[236:239], 0\n"
"v_mfma_f32_16x16x32_bf16 v[116:119], v[244:247], v[236:239], 0\n"
"s_waitcnt lgkmcnt(6)\n"
"v_mfma_f32_16x16x32_bf16 v[228:231], v[180:183], v[120:123], 0\n"
"v_mfma_f32_16x16x32_bf16 v[228:231], v[184:187], v[124:127], v[228:231]\n"
"v_mfma_f32_16x16x32_bf16 v[228:231], v[188:191], v[128:131], v[228:231]\n"
"s_waitcnt lgkmcnt(0)\n"
"v_mfma_f32_16x16x32_bf16 v[232:235], v[192:195], v[120:123], 0\n"
"v_mfma_f32_16x16x32_bf16 v[232:235], v[196:199], v[124:127], v[232:235]\n"
"v_mfma_f32_16x16x32_bf16 v[232:235], v[200:203], v[128:131], v[232:235]\n"
"v_mfma_f32_16x16x32_bf16 v[48:51], v[246:249], v[204:207], 0\n"
"v_mfma_f32_16x16x32_bf16 v[52:55], v[244:247], v[204:207], 0\n"
"s_nop 1\n"
"v_lshlrev_b32_sdwa v228, 1, v228 dst_sel:DWORD dst_unused:UNUSED_PAD src0_sel:DWORD src1_sel:WORD_1\n"
"v_mfma_f32_16x16x32_bf16 v[56:59], v[246:249], v[208:211], 0\n"
"ds_read_u16_d16 v228, v228 offset:18432\n"
"v_lshlrev_b32_sdwa v229, 1, v229 dst_sel:DWORD dst_unused:UNUSED_PAD src0_sel:DWORD src1_sel:WORD_1\n"
"v_mfma_f32_16x16x32_bf16 v[60:63], v[244:247], v[208:211], 0\n"
"ds_read_u16_d16_hi v229, v229 offset:18432\n"
"v_mfma_f32_16x16x32_bf16 v[64:67], v[246:249], v[212:215], 0\n"
"v_lshlrev_b32_sdwa v230, 1, v230 dst_sel:DWORD dst_unused:UNUSED_PAD src0_sel:DWORD src1_sel:WORD_1\n"
"v_mfma_f32_16x16x32_bf16 v[68:71], v[244:247], v[212:215], 0\n"
"ds_read_u16_d16 v230, v230 offset:18432\n"
"v_lshlrev_b32_sdwa v231, 1, v231 dst_sel:DWORD dst_unused:UNUSED_PAD src0_sel:DWORD src1_sel:WORD_1\n"
"v_mfma_f32_16x16x32_bf16 v[236:239], v[180:183], v[132:135], 0\n"
"ds_read_b64_tr_b16 v[180:181], v252 offset:6144\n"
"ds_read_b64_tr_b16 v[182:183], v252 offset:9216\n"
"ds_read_u16_d16_hi v231, v231 offset:18432\n"
"v_mfma_f32_16x16x32_bf16 v[236:239], v[184:187], v[136:139], v[236:239]\n"
"ds_read_b64_tr_b16 v[184:185], v252 offset:7168\n"
"ds_read_b64_tr_b16 v[186:187], v252 offset:10240\n"
"v_lshlrev_b32_sdwa v232, 1, v232 dst_sel:DWORD dst_unused:UNUSED_PAD src0_sel:DWORD src1_sel:WORD_1\n"
"v_mfma_f32_16x16x32_bf16 v[236:239], v[188:191], v[140:143], v[236:239]\n"
"ds_read_b64_tr_b16 v[188:189], v252 offset:8192\n"
"ds_read_b64_tr_b16 v[190:191], v252 offset:11264\n"
"ds_read_u16_d16 v232, v232 offset:18432\n"
"v_lshlrev_b32_sdwa v233, 1, v233 dst_sel:DWORD dst_unused:UNUSED_PAD src0_sel:DWORD src1_sel:WORD_1\n"
"v_mfma_f32_16x16x32_bf16 v[240:243], v[192:195], v[132:135], 0\n"
"ds_read_b64_tr_b16 v[192:193], v252 offset:6656\n"
"ds_read_b64_tr_b16 v[194:195], v252 offset:9728\n"
"ds_read_u16_d16_hi v233, v233 offset:18432\n"
"v_mfma_f32_16x16x32_bf16 v[240:243], v[196:199], v[136:139], v[240:243]\n"
"ds_read_b64_tr_b16 v[196:197], v252 offset:7680\n"
"ds_read_b64_tr_b16 v[198:199], v252 offset:10752\n"
"v_lshlrev_b32_sdwa v234, 1, v234 dst_sel:DWORD dst_unused:UNUSED_PAD src0_sel:DWORD src1_sel:WORD_1\n"
"v_mfma_f32_16x16x32_bf16 v[240:243], v[200:203], v[140:143], v[240:243]\n"
"ds_read_b64_tr_b16 v[200:201], v252 offset:8704\n"
"ds_read_b64_tr_b16 v[202:203], v252 offset:11776\n"
"ds_read_u16_d16 v234, v234 offset:18432\n"
"v_lshlrev_b32_sdwa v235, 1, v235 dst_sel:DWORD dst_unused:UNUSED_PAD src0_sel:DWORD src1_sel:WORD_1\n"
"v_mfma_f32_16x16x32_bf16 v[72:75], v[246:249], v[216:219], 0\n"
"ds_read_b64_tr_b16 v[204:205], v253 offset:0\n"
"ds_read_b64_tr_b16 v[206:207], v253 offset:1536\n"
"ds_read_u16_d16_hi v235, v235 offset:18432\n"
"v_mfma_f32_16x16x32_bf16 v[76:79], v[244:247], v[216:219], 0\n"
"ds_read_b64_tr_b16 v[208:209], v253 offset:8\n"
"ds_read_b64_tr_b16 v[210:211], v253 offset:1544\n"
"ds_read_b64_tr_b16 v[216:217], v253 offset:72\n"
"ds_read_b64_tr_b16 v[218:219], v253 offset:1608\n"
"v_lshlrev_b32_sdwa v236, 1, v236 dst_sel:DWORD dst_unused:UNUSED_PAD src0_sel:DWORD src1_sel:WORD_1\n"
"v_mfma_f32_16x16x32_bf16 v[80:83], v[246:249], v[220:223], 0\n"
"ds_read_b64_tr_b16 v[212:213], v253 offset:64\n"
"ds_read_b64_tr_b16 v[214:215], v253 offset:1600\n"
"ds_read_u16_d16 v236, v236 offset:18432\n"
"v_lshlrev_b32_sdwa v237, 1, v237 dst_sel:DWORD dst_unused:UNUSED_PAD src0_sel:DWORD src1_sel:WORD_1\n"
"v_mfma_f32_16x16x32_bf16 v[84:87], v[244:247], v[220:223], 0\n"
"ds_read_b64_tr_b16 v[220:221], v253 offset:128\n"
"ds_read_b64_tr_b16 v[222:223], v253 offset:1664\n"
"ds_read_u16_d16_hi v237, v237 offset:18432\n"
"v_mfma_f32_16x16x32_bf16 v[88:91], v[246:249], v[224:227], 0\n"
"v_lshlrev_b32_sdwa v238, 1, v238 dst_sel:DWORD dst_unused:UNUSED_PAD src0_sel:DWORD src1_sel:WORD_1\n"
"v_mfma_f32_16x16x32_bf16 v[92:95], v[244:247], v[224:227], 0\n"
"ds_read_b64_tr_b16 v[224:225], v253 offset:136\n"
"ds_read_b64_tr_b16 v[226:227], v253 offset:1672\n"
"ds_read_u16_d16 v238, v238 offset:18432\n"
"v_lshlrev_b32_sdwa v239, 1, v239 dst_sel:DWORD dst_unused:UNUSED_PAD src0_sel:DWORD src1_sel:WORD_1\n"
"s_waitcnt vmcnt(0)\n"
"v_mov_b32 v244, 163776\n"
"v_mov_b32 v245, 1\n"
"s_mov_b64 exec, 1\n"
"ds_add_u32 v244, v245\n"
"s_mov_b64 exec, -1\n"
"s_waitcnt lgkmcnt(14)\n"
"v_mfma_f32_16x16x32_bf16 v[244:247], v[180:183], v[120:123], 0\n"
"ds_read_u16_d16_hi v239, v239 offset:18432\n"
"v_or_b32 v228, v228, v229\n"
"v_mfma_f32_16x16x32_bf16 v[244:247], v[184:187], v[124:127], v[244:247]\n"
"v_or_b32 v229, v230, v231\n"
"v_lshlrev_b32_sdwa v240, 1, v240 dst_sel:DWORD dst_unused:UNUSED_PAD src0_sel:DWORD src1_sel:WORD_1\n"
"v_mfma_f32_16x16x32_bf16 v[244:247], v[188:191], v[128:131], v[244:247]\n"
"ds_read_u16_d16 v240, v240 offset:18432\n"
"v_lshlrev_b32_sdwa v241, 1, v241 dst_sel:DWORD dst_unused:UNUSED_PAD src0_sel:DWORD src1_sel:WORD_1\n"
"v_mfma_f32_16x16x32_bf16 v[248:251], v[192:195], v[120:123], 0\n"
"ds_read_u16_d16_hi v241, v241 offset:18432\n"
"v_or_b32 v230, v232, v233\n"
"v_mfma_f32_16x16x32_bf16 v[248:251], v[196:199], v[124:127], v[248:251]\n"
"v_or_b32 v231, v234, v235\n"
"v_lshlrev_b32_sdwa v242, 1, v242 dst_sel:DWORD dst_unused:UNUSED_PAD src0_sel:DWORD src1_sel:WORD_1\n"
"v_mfma_f32_16x16x32_bf16 v[248:251], v[200:203], v[128:131], v[248:251]\n"
"ds_read_u16_d16 v242, v242 offset:18432\n"
"v_lshlrev_b32_sdwa v243, 1, v243 dst_sel:DWORD dst_unused:UNUSED_PAD src0_sel:DWORD src1_sel:WORD_1\n"
"s_waitcnt lgkmcnt(12)\n"
"v_mfma_f32_16x16x32_bf16 v[0:3], v[204:207], v[228:231], v[0:3]\n"
"ds_read_u16_d16_hi v243, v243 offset:18432\n"
"v_mfma_f32_16x16x32_bf16 v[4:7], v[208:211], v[228:231], v[4:7]\n"
"v_lshlrev_b32_sdwa v244, 1, v244 dst_sel:DWORD dst_unused:UNUSED_PAD src0_sel:DWORD src1_sel:WORD_1\n"
"v_mfma_f32_16x16x32_bf16 v[8:11], v[212:215], v[228:231], v[8:11]\n"
"ds_read_u16_d16 v244, v244 offset:18432\n"
"v_lshlrev_b32_sdwa v245, 1, v245 dst_sel:DWORD dst_unused:UNUSED_PAD src0_sel:DWORD src1_sel:WORD_1\n"
"s_waitcnt lgkmcnt(8)\n"
"v_mfma_f32_16x16x32_bf16 v[12:15], v[216:219], v[228:231], v[12:15]\n"
"ds_read_u16_d16_hi v245, v245 offset:18432\n"
"v_mfma_f32_16x16x32_bf16 v[16:19], v[220:223], v[228:231], v[16:19]\n"
"v_lshlrev_b32_sdwa v246, 1, v246 dst_sel:DWORD dst_unused:UNUSED_PAD src0_sel:DWORD src1_sel:WORD_1\n"
"v_mfma_f32_16x16x32_bf16 v[20:23], v[224:227], v[228:231], v[20:23]\n"
"ds_read_u16_d16 v246, v246 offset:18432\n"
"v_lshlrev_b32_sdwa v247, 1, v247 dst_sel:DWORD dst_unused:UNUSED_PAD src0_sel:DWORD src1_sel:WORD_1\n"
"v_mfma_f32_16x16x32_bf16 v[228:231], v[180:183], v[132:135], 0\n"
"s_waitcnt lgkmcnt(3)\n"
"ds_read_u16_d16_hi v247, v247 offset:18432\n"
"v_or_b32 v236, v236, v237\n"
"v_mfma_f32_16x16x32_bf16 v[228:231], v[184:187], v[136:139], v[228:231]\n"
"v_or_b32 v237, v238, v239\n"
"v_lshlrev_b32_sdwa v248, 1, v248 dst_sel:DWORD dst_unused:UNUSED_PAD src0_sel:DWORD src1_sel:WORD_1\n"
"v_mfma_f32_16x16x32_bf16 v[228:231], v[188:191], v[140:143], v[228:231]\n"
"ds_read_u16_d16 v248, v248 offset:18432\n"
"v_lshlrev_b32_sdwa v249, 1, v249 dst_sel:DWORD dst_unused:UNUSED_PAD src0_sel:DWORD src1_sel:WORD_1\n"
"v_mfma_f32_16x16x32_bf16 v[232:235], v[192:195], v[132:135], 0\n"
"ds_read_u16_d16_hi v249, v249 offset:18432\n"
"v_or_b32 v238, v240, v241\n"
"v_mfma_f32_16x16x32_bf16 v[232:235], v[196:199], v[136:139], v[232:235]\n"
"v_or_b32 v239, v242, v243\n"
"v_lshlrev_b32_sdwa v250, 1, v250 dst_sel:DWORD dst_unused:UNUSED_PAD src0_sel:DWORD src1_sel:WORD_1\n"
"v_mfma_f32_16x16x32_bf16 v[232:235], v[200:203], v[140:143], v[232:235]\n"
"ds_read_u16_d16 v250, v250 offset:18432\n"
"v_lshlrev_b32_sdwa v251, 1, v251 dst_sel:DWORD dst_unused:UNUSED_PAD src0_sel:DWORD src1_sel:WORD_1\n"
"v_mfma_f32_16x16x32_bf16 v[24:27], v[204:207], v[236:239], v[24:27]\n"
"ds_read_b64_tr_b16 v[204:205], v253 offset:6160\n"
"ds_read_b64_tr_b16 v[206:207], v253 offset:7696\n"
"ds_read_u16_d16_hi v251, v251 offset:18432\n"
"v_mfma_f32_16x16x32_bf16 v[28:31], v[208:211], v[236:239], v[28:31]\n"
"ds_read_b64_tr_b16 v[208:209], v253 offset:6168\n"
"ds_read_b64_tr_b16 v[210:211], v253 offset:7704\n"
"v_lshlrev_b32_sdwa v228, 1, v228 dst_sel:DWORD dst_unused:UNUSED_PAD src0_sel:DWORD src1_sel:WORD_1\n"
"v_mfma_f32_16x16x32_bf16 v[32:35], v[212:215], v[236:239], v[32:35]\n"
"ds_read_b64_tr_b16 v[212:213], v253 offset:6224\n"
"ds_read_b64_tr_b16 v[214:215], v253 offset:7760\n"
"ds_read_u16_d16 v228, v228 offset:18432\n"
"v_lshlrev_b32_sdwa v229, 1, v229 dst_sel:DWORD dst_unused:UNUSED_PAD src0_sel:DWORD src1_sel:WORD_1\n"
"v_mfma_f32_16x16x32_bf16 v[36:39], v[216:219], v[236:239], v[36:39]\n"
"ds_read_b64_tr_b16 v[216:217], v253 offset:6232\n"
"ds_read_b64_tr_b16 v[218:219], v253 offset:7768\n"
"ds_read_u16_d16_hi v229, v229 offset:18432\n"
"v_mfma_f32_16x16x32_bf16 v[40:43], v[220:223], v[236:239], v[40:43]\n"
"ds_read_b64_tr_b16 v[220:221], v253 offset:6288\n"
"ds_read_b64_tr_b16 v[222:223], v253 offset:7824\n"
"v_lshlrev_b32_sdwa v230, 1, v230 dst_sel:DWORD dst_unused:UNUSED_PAD src0_sel:DWORD src1_sel:WORD_1\n"
"v_mfma_f32_16x16x32_bf16 v[44:47], v[224:227], v[236:239], v[44:47]\n"
"ds_read_b64_tr_b16 v[224:225], v253 offset:6296\n"
"ds_read_b64_tr_b16 v[226:227], v253 offset:7832\n"
"ds_read_u16_d16 v230, v230 offset:18432\n"
"v_lshlrev_b32_sdwa v231, 1, v231 dst_sel:DWORD dst_unused:UNUSED_PAD src0_sel:DWORD src1_sel:WORD_1\n"
"v_mov_b32 v236, 163776\n"
"s_setprio 0\n"
".Lpa_%=:\n"
"ds_read_b32 v237, v236\n"
"s_waitcnt lgkmcnt(0)\n"
"v_readfirstlane_b32 s66, v237\n"
"s_nop 1\n"
"s_cmp_lt_u32 s66, 8\n"
"s_cbranch_scc1 .Lpa_%=\n"
"v_mfma_f32_16x16x32_bf16 v[236:239], v[180:183], v[144:147], 0\n"
"ds_read_b64_tr_b16 v[180:181], v252 offset:12288\n"
"ds_read_b64_tr_b16 v[182:183], v252 offset:15360\n"
"ds_read_u16_d16_hi v231, v231 offset:18432\n"
"v_or_b32 v244, v244, v245\n"
"v_mfma_f32_16x16x32_bf16 v[236:239], v[184:187], v[148:151], v[236:239]\n"
"ds_read_b64_tr_b16 v[184:185], v252 offset:13312\n"
"ds_read_b64_tr_b16 v[186:187], v252 offset:16384\n"
"v_or_b32 v245, v246, v247\n"
"v_lshlrev_b32_sdwa v232, 1, v232 dst_sel:DWORD dst_unused:UNUSED_PAD src0_sel:DWORD src1_sel:WORD_1\n"
"v_mfma_f32_16x16x32_bf16 v[236:239], v[188:191], v[152:155], v[236:239]\n"
"ds_read_b64_tr_b16 v[188:189], v252 offset:14336\n"
"ds_read_b64_tr_b16 v[190:191], v252 offset:17408\n"
"ds_read_u16_d16 v232, v232 offset:18432\n"
"v_lshlrev_b32_sdwa v233, 1, v233 dst_sel:DWORD dst_unused:UNUSED_PAD src0_sel:DWORD src1_sel:WORD_1\n"
"v_mfma_f32_16x16x32_bf16 v[240:243], v[192:195], v[144:147], 0\n"
"ds_read_b64_tr_b16 v[192:193], v252 offset:12800\n"
"ds_read_b64_tr_b16 v[194:195], v252 offset:15872\n"
"ds_read_u16_d16_hi v233, v233 offset:18432\n"
"v_or_b32 v246, v248, v249\n"
"v_mfma_f32_16x16x32_bf16 v[240:243], v[196:199], v[148:151], v[240:243]\n"
"ds_read_b64_tr_b16 v[196:197], v252 offset:13824\n"
"ds_read_b64_tr_b16 v[198:199], v252 offset:16896\n"
"v_or_b32 v247, v250, v251\n"
"v_lshlrev_b32_sdwa v234, 1, v234 dst_sel:DWORD dst_unused:UNUSED_PAD src0_sel:DWORD src1_sel:WORD_1\n"
"v_mfma_f32_16x16x32_bf16 v[240:243], v[200:203], v[152:155], v[240:243]\n"
"ds_read_b64_tr_b16 v[200:201], v252 offset:14848\n"
"ds_read_b64_tr_b16 v[202:203], v252 offset:17920\n"
"ds_read_u16_d16 v234, v234 offset:18432\n"
"v_lshlrev_b32_sdwa v235, 1, v235 dst_sel:DWORD dst_unused:UNUSED_PAD src0_sel:DWORD src1_sel:WORD_1\n"
"v_mfma_f32_16x16x32_bf16 v[0:3], v[204:207], v[244:247], v[0:3]\n"
"ds_read_u16_d16_hi v235, v235 offset:18432\n"
"v_mfma_f32_16x16x32_bf16 v[4:7], v[208:211], v[244:247], v[4:7]\n"
"v_lshlrev_b32_sdwa v236, 1, v236 dst_sel:DWORD dst_unused:UNUSED_PAD src0_sel:DWORD src1_sel:WORD_1\n"
"v_mfma_f32_16x16x32_bf16 v[8:11], v[212:215], v[244:247], v[8:11]\n"
"ds_read_u16_d16 v236, v236 offset:18432\n"
"v_lshlrev_b32_sdwa v237, 1, v237 dst_sel:DWORD dst_unused:UNUSED_PAD src0_sel:DWORD src1_sel:WORD_1\n"
"v_mfma_f32_16x16x32_bf16 v[12:15], v[216:219], v[244:247], v[12:15]\n"
"ds_read_u16_d16_hi v237, v237 offset:18432\n"
"v_mfma_f32_16x16x32_bf16 v[16:19], v[220:223], v[244:247], v[16:19]\n"
"v_lshlrev_b32_sdwa v238, 1, v238 dst_sel:DWORD dst_unused:UNUSED_PAD src0_sel:DWORD src1_sel:WORD_1\n"
"v_mfma_f32_16x16x32_bf16 v[20:23], v[224:227], v[244:247], v[20:23]\n"
"ds_read_u16_d16 v238, v238 offset:18432\n"
"v_lshlrev_b32_sdwa v239, 1, v239 dst_sel:DWORD dst_unused:UNUSED_PAD src0_sel:DWORD src1_sel:WORD_1\n"
"s_waitcnt lgkmcnt(13)\n"
"v_mfma_f32_16x16x32_bf16 v[244:247], v[180:183], v[120:123], 0\n"
"s_waitcnt lgkmcnt(3)\n"
"ds_read_u16_d16_hi v239, v239 offset:18432\n"
"v_or_b32 v228, v228, v229\n"
"v_mfma_f32_16x16x32_bf16 v[244:247], v[184:187], v[124:127], v[244:247]\n"
"v_or_b32 v229, v230, v231\n"
"v_lshlrev_b32_sdwa v240, 1, v240 dst_sel:DWORD dst_unused:UNUSED_PAD src0_sel:DWORD src1_sel:WORD_1\n"
"v_mfma_f32_16x16x32_bf16 v[244:247], v[188:191], v[128:131], v[244:247]\n"
"ds_read_u16_d16 v240, v240 offset:18432\n"
"v_lshlrev_b32_sdwa v241, 1, v241 dst_sel:DWORD dst_unused:UNUSED_PAD src0_sel:DWORD src1_sel:WORD_1\n"
"v_mfma_f32_16x16x32_bf16 v[248:251], v[192:195], v[120:123], 0\n"
"ds_read_u16_d16_hi v241, v241 offset:18432\n"
"v_or_b32 v230, v232, v233\n"
"v_mfma_f32_16x16x32_bf16 v[248:251], v[196:199], v[124:127], v[248:251]\n"
"v_or_b32 v231, v234, v235\n"
"v_lshlrev_b32_sdwa v242, 1, v242 dst_sel:DWORD dst_unused:UNUSED_PAD src0_sel:DWORD src1_sel:WORD_1\n"
"v_mfma_f32_16x16x32_bf16 v[248:251], v[200:203], v[128:131], v[248:251]\n"
"ds_read_u16_d16 v242, v242 offset:18432\n"
"v_lshlrev_b32_sdwa v243, 1, v243 dst_sel:DWORD dst_unused:UNUSED_PAD src0_sel:DWORD src1_sel:WORD_1\n"
"v_mfma_f32_16x16x32_bf16 v[24:27], v[204:207], v[228:231], v[24:27]\n"
"ds_read_u16_d16_hi v243, v243 offset:18432\n"
"v_mfma_f32_16x16x32_bf16 v[28:31], v[208:211], v[228:231], v[28:31]\n"
"v_lshlrev_b32_sdwa v244, 1, v244 dst_sel:DWORD dst_unused:UNUSED_PAD src0_sel:DWORD src1_sel:WORD_1\n"
"v_mfma_f32_16x16x32_bf16 v[32:35], v[212:215], v[228:231], v[32:35]\n"
"ds_read_u16_d16 v244, v244 offset:18432\n"
"v_lshlrev_b32_sdwa v245, 1, v245 dst_sel:DWORD dst_unused:UNUSED_PAD src0_sel:DWORD src1_sel:WORD_1\n"
"v_mfma_f32_16x16x32_bf16 v[36:39], v[216:219], v[228:231], v[36:39]\n"
"ds_read_u16_d16_hi v245, v245 offset:18432\n"
"v_mfma_f32_16x16x32_bf16 v[40:43], v[220:223], v[228:231], v[40:43]\n"
"v_lshlrev_b32_sdwa v246, 1, v246 dst_sel:DWORD dst_unused:UNUSED_PAD src0_sel:DWORD src1_sel:WORD_1\n"
"v_mfma_f32_16x16x32_bf16 v[44:47], v[224:227], v[228:231], v[44:47]\n"
"ds_read_u16_d16 v246, v246 offset:18432\n"
"v_lshlrev_b32_sdwa v247, 1, v247 dst_sel:DWORD dst_unused:UNUSED_PAD src0_sel:DWORD src1_sel:WORD_1\n"
"v_mfma_f32_16x16x32_bf16 v[228:231], v[180:183], v[132:135], 0\n"
"s_waitcnt lgkmcnt(3)\n"
"ds_read_u16_d16_hi v247, v247 offset:18432\n"
"v_or_b32 v236, v236, v237\n"
"v_mfma_f32_16x16x32_bf16 v[228:231], v[184:187], v[136:139], v[228:231]\n"
"v_or_b32 v237, v238, v239\n"
"v_lshlrev_b32_sdwa v248, 1, v248 dst_sel:DWORD dst_unused:UNUSED_PAD src0_sel:DWORD src1_sel:WORD_1\n"
"v_mfma_f32_16x16x32_bf16 v[228:231], v[188:191], v[140:143], v[228:231]\n"
"ds_read_u16_d16 v248, v248 offset:18432\n"
"v_lshlrev_b32_sdwa v249, 1, v249 dst_sel:DWORD dst_unused:UNUSED_PAD src0_sel:DWORD src1_sel:WORD_1\n"
"v_mfma_f32_16x16x32_bf16 v[232:235], v[192:195], v[132:135], 0\n"
"ds_read_u16_d16_hi v249, v249 offset:18432\n"
"v_or_b32 v238, v240, v241\n"
"v_mfma_f32_16x16x32_bf16 v[232:235], v[196:199], v[136:139], v[232:235]\n"
"v_or_b32 v239, v242, v243\n"
"v_lshlrev_b32_sdwa v250, 1, v250 dst_sel:DWORD dst_unused:UNUSED_PAD src0_sel:DWORD src1_sel:WORD_1\n"
"v_mfma_f32_16x16x32_bf16 v[232:235], v[200:203], v[140:143], v[232:235]\n"
"ds_read_u16_d16 v250, v250 offset:18432\n"
"v_lshlrev_b32_sdwa v251, 1, v251 dst_sel:DWORD dst_unused:UNUSED_PAD src0_sel:DWORD src1_sel:WORD_1\n"
"v_mfma_f32_16x16x32_bf16 v[48:51], v[204:207], v[236:239], v[48:51]\n"
"ds_read_b64_tr_b16 v[204:205], v253 offset:12320\n"
"ds_read_b64_tr_b16 v[206:207], v253 offset:13856\n"
"ds_read_u16_d16_hi v251, v251 offset:18432\n"
"v_mfma_f32_16x16x32_bf16 v[52:55], v[208:211], v[236:239], v[52:55]\n"
"ds_read_b64_tr_b16 v[208:209], v253 offset:12328\n"
"ds_read_b64_tr_b16 v[210:211], v253 offset:13864\n"
"v_lshlrev_b32_sdwa v228, 1, v228 dst_sel:DWORD dst_unused:UNUSED_PAD src0_sel:DWORD src1_sel:WORD_1\n"
"v_mfma_f32_16x16x32_bf16 v[56:59], v[212:215], v[236:239], v[56:59]\n"
"ds_read_b64_tr_b16 v[212:213], v253 offset:12384\n"
"ds_read_b64_tr_b16 v[214:215], v253 offset:13920\n"
"ds_read_u16_d16 v228, v228 offset:18432\n"
"v_lshlrev_b32_sdwa v229, 1, v229 dst_sel:DWORD dst_unused:UNUSED_PAD src0_sel:DWORD src1_sel:WORD_1\n"
"v_mfma_f32_16x16x32_bf16 v[60:63], v[216:219], v[236:239], v[60:63]\n"
"ds_read_b64_tr_b16 v[216:217], v253 offset:12392\n"
"ds_read_b64_tr_b16 v[218:219], v253 offset:13928\n"
"ds_read_u16_d16_hi v229, v229 offset:18432\n"
"v_mfma_f32_16x16x32_bf16 v[64:67], v[220:223], v[236:239], v[64:67]\n"
"ds_read_b64_tr_b16 v[220:221], v253 offset:12448\n"
"ds_read_b64_tr_b16 v[222:223], v253 offset:13984\n"
"v_lshlrev_b32_sdwa v230, 1, v230 dst_sel:DWORD dst_unused:UNUSED_PAD src0_sel:DWORD src1_sel:WORD_1\n"
"v_mfma_f32_16x16x32_bf16 v[68:71], v[224:227], v[236:239], v[68:71]\n"
"ds_read_b64_tr_b16 v[224:225], v253 offset:12456\n"
"ds_read_b64_tr_b16 v[226:227], v253 offset:13992\n"
"ds_read_u16_d16 v230, v230 offset:18432\n"
"v_lshlrev_b32_sdwa v231, 1, v231 dst_sel:DWORD dst_unused:UNUSED_PAD src0_sel:DWORD src1_sel:WORD_1\n"
"v_mfma_f32_16x16x32_bf16 v[236:239], v[180:183], v[144:147], 0\n"
"s_waitcnt lgkmcnt(13)\n"
"ds_read_u16_d16_hi v231, v231 offset:18432\n"
"v_or_b32 v244, v244, v245\n"
"v_mfma_f32_16x16x32_bf16 v[236:239], v[184:187], v[148:151], v[236:239]\n"
"v_or_b32 v245, v246, v247\n"
"v_lshlrev_b32_sdwa v232, 1, v232 dst_sel:DWORD dst_unused:UNUSED_PAD src0_sel:DWORD src1_sel:WORD_1\n"
"v_mfma_f32_16x16x32_bf16 v[236:239], v[188:191], v[152:155], v[236:239]\n"
"ds_read_u16_d16 v232, v232 offset:18432\n"
"v_lshlrev_b32_sdwa v233, 1, v233 dst_sel:DWORD dst_unused:UNUSED_PAD src0_sel:DWORD src1_sel:WORD_1\n"
"v_mfma_f32_16x16x32_bf16 v[240:243], v[192:195], v[144:147], 0\n"
"ds_read_u16_d16_hi v233, v233 offset:18432\n"
"v_or_b32 v246, v248, v249\n"
"v_mfma_f32_16x16x32_bf16 v[240:243], v[196:199], v[148:151], v[240:243]\n"
"v_or_b32 v247, v250, v251\n"
"v_lshlrev_b32_sdwa v234, 1, v234 dst_sel:DWORD dst_unused:UNUSED_PAD src0_sel:DWORD src1_sel:WORD_1\n"
"v_mfma_f32_16x16x32_bf16 v[240:243], v[200:203], v[152:155], v[240:243]\n"
"ds_read_u16_d16 v234, v234 offset:18432\n"
"v_lshlrev_b32_sdwa v235, 1, v235 dst_sel:DWORD dst_unused:UNUSED_PAD src0_sel:DWORD src1_sel:WORD_1\n"
"s_waitcnt lgkmcnt(13)\n"
"v_mfma_f32_16x16x32_bf16 v[0:3], v[204:207], v[244:247], v[0:3]\n"
"ds_read_u16_d16_hi v235, v235 offset:18432\n"
"v_mfma_f32_16x16x32_bf16 v[4:7], v[208:211], v[244:247], v[4:7]\n"
"v_lshlrev_b32_sdwa v236, 1, v236 dst_sel:DWORD dst_unused:UNUSED_PAD src0_sel:DWORD src1_sel:WORD_1\n"
"v_mfma_f32_16x16x32_bf16 v[8:11], v[212:215], v[244:247], v[8:11]\n"
"ds_read_u16_d16 v236, v236 offset:18432\n"
"v_lshlrev_b32_sdwa v237, 1, v237 dst_sel:DWORD dst_unused:UNUSED_PAD src0_sel:DWORD src1_sel:WORD_1\n"
"s_waitcnt lgkmcnt(7)\n"
"v_mfma_f32_16x16x32_bf16 v[12:15], v[216:219], v[244:247], v[12:15]\n"
"ds_read_u16_d16_hi v237, v237 offset:18432\n"
"v_mfma_f32_16x16x32_bf16 v[16:19], v[220:223], v[244:247], v[16:19]\n"
"v_lshlrev_b32_sdwa v238, 1, v238 dst_sel:DWORD dst_unused:UNUSED_PAD src0_sel:DWORD src1_sel:WORD_1\n"
"v_mfma_f32_16x16x32_bf16 v[20:23], v[224:227], v[244:247], v[20:23]\n"
"ds_read_u16_d16 v238, v238 offset:18432\n"
"v_lshlrev_b32_sdwa v239, 1, v239 dst_sel:DWORD dst_unused:UNUSED_PAD src0_sel:DWORD src1_sel:WORD_1\n"
"v_mfma_f32_16x16x32_bf16 v[244:247], v[180:183], v[156:159], 0\n"
"ds_read_b64_tr_b16 v[180:181], v252 offset:18432\n"
"ds_read_b64_tr_b16 v[182:183], v252 offset:21504\n"
"s_waitcnt lgkmcnt(5)\n"
"ds_read_u16_d16_hi v239, v239 offset:18432\n"
"v_or_b32 v228, v228, v229\n"
"v_mfma_f32_16x16x32_bf16 v[244:247], v[184:187], v[160:163], v[244:247]\n"
"ds_read_b64_tr_b16 v[184:185], v252 offset:19456\n"
"ds_read_b64_tr_b16 v[186:187], v252 offset:22528\n"
"v_or_b32 v229, v230, v231\n"
"v_lshlrev_b32_sdwa v240, 1, v240 dst_sel:DWORD dst_unused:UNUSED_PAD src0_sel:DWORD src1_sel:WORD_1\n"
"v_mfma_f32_16x16x32_bf16 v[244:247], v[188:191], v[164:167], v[244:247]\n"
"ds_read_b64_tr_b16 v[188:189], v252 offset:20480\n"
"ds_read_b64_tr_b16 v[190:191], v252 offset:23552\n"
"ds_read_u16_d16 v240, v240 offset:18432\n"
"v_lshlrev_b32_sdwa v241, 1, v241 dst_sel:DWORD dst_unused:UNUSED_PAD src0_sel:DWORD src1_sel:WORD_1\n"
"v_mfma_f32_16x16x32_bf16 v[248:251], v[192:195], v[156:159], 0\n"
"ds_read_b64_tr_b16 v[192:193], v252 offset:18944\n"
"ds_read_b64_tr_b16 v[194:195], v252 offset:22016\n"
"ds_read_u16_d16_hi v241, v241 offset:18432\n"
"v_or_b32 v230, v232, v233\n"
"v_mfma_f32_16x16x32_bf16 v[248:251], v[196:199], v[160:163], v[248:251]\n"
"ds_read_b64_tr_b16 v[196:197], v252 offset:19968\n"
"ds_read_b64_tr_b16 v[198:199], v252 offset:23040\n"
"v_or_b32 v231, v234, v235\n"
"v_lshlrev_b32_sdwa v242, 1, v242 dst_sel:DWORD dst_unused:UNUSED_PAD src0_sel:DWORD src1_sel:WORD_1\n"
"v_mfma_f32_16x16x32_bf16 v[248:251], v[200:203], v[164:167], v[248:251]\n"
"ds_read_b64_tr_b16 v[200:201], v252 offset:20992\n"
"ds_read_b64_tr_b16 v[202:203], v252 offset:24064\n"
"ds_read_u16_d16 v242, v242 offset:18432\n"
"v_lshlrev_b32_sdwa v243, 1, v243 dst_sel:DWORD dst_unused:UNUSED_PAD src0_sel:DWORD src1_sel:WORD_1\n"
"v_mfma_f32_16x16x32_bf16 v[24:27], v[204:207], v[228:231], v[24:27]\n"
"ds_read_u16_d16_hi v243, v243 offset:18432\n"
"v_mfma_f32_16x16x32_bf16 v[28:31], v[208:211], v[228:231], v[28:31]\n"
"v_lshlrev_b32_sdwa v244, 1, v244 dst_sel:DWORD dst_unused:UNUSED_PAD src0_sel:DWORD src1_sel:WORD_1\n"
"v_mfma_f32_16x16x32_bf16 v[32:35], v[212:215], v[228:231], v[32:35]\n"
"ds_read_u16_d16 v244, v244 offset:18432\n"
"v_lshlrev_b32_sdwa v245, 1, v245 dst_sel:DWORD dst_unused:UNUSED_PAD src0_sel:DWORD src1_sel:WORD_1\n"
"v_mfma_f32_16x16x32_bf16 v[36:39], v[216:219], v[228:231], v[36:39]\n"
"ds_read_u16_d16_hi v245, v245 offset:18432\n"
"v_mfma_f32_16x16x32_bf16 v[40:43], v[220:223], v[228:231], v[40:43]\n"
"v_lshlrev_b32_sdwa v246, 1, v246 dst_sel:DWORD dst_unused:UNUSED_PAD src0_sel:DWORD src1_sel:WORD_1\n"
"v_mfma_f32_16x16x32_bf16 v[44:47], v[224:227], v[228:231], v[44:47]\n"
"ds_read_u16_d16 v246, v246 offset:18432\n"
"v_lshlrev_b32_sdwa v247, 1, v247 dst_sel:DWORD dst_unused:UNUSED_PAD src0_sel:DWORD src1_sel:WORD_1\n"
"s_waitcnt lgkmcnt(13)\n"
"v_mfma_f32_16x16x32_bf16 v[228:231], v[180:183], v[120:123], 0\n"
"s_waitcnt lgkmcnt(3)\n"
"ds_read_u16_d16_hi v247, v247 offset:18432\n"
"v_or_b32 v236, v236, v237\n"
"v_mfma_f32_16x16x32_bf16 v[228:231], v[184:187], v[124:127], v[228:231]\n"
"v_or_b32 v237, v238, v239\n"
"v_lshlrev_b32_sdwa v248, 1, v248 dst_sel:DWORD dst_unused:UNUSED_PAD src0_sel:DWORD src1_sel:WORD_1\n"
"v_mfma_f32_16x16x32_bf16 v[228:231], v[188:191], v[128:131], v[228:231]\n"
"ds_read_u16_d16 v248, v248 offset:18432\n"
"v_lshlrev_b32_sdwa v249, 1, v249 dst_sel:DWORD dst_unused:UNUSED_PAD src0_sel:DWORD src1_sel:WORD_1\n"
"v_mfma_f32_16x16x32_bf16 v[232:235], v[192:195], v[120:123], 0\n"
"ds_read_u16_d16_hi v249, v249 offset:18432\n"
"v_or_b32 v238, v240, v241\n"
"v_mfma_f32_16x16x32_bf16 v[232:235], v[196:199], v[124:127], v[232:235]\n"
"v_or_b32 v239, v242, v243\n"
"v_lshlrev_b32_sdwa v250, 1, v250 dst_sel:DWORD dst_unused:UNUSED_PAD src0_sel:DWORD src1_sel:WORD_1\n"
"v_mfma_f32_16x16x32_bf16 v[232:235], v[200:203], v[128:131], v[232:235]\n"
"ds_read_u16_d16 v250, v250 offset:18432\n"
"v_lshlrev_b32_sdwa v251, 1, v251 dst_sel:DWORD dst_unused:UNUSED_PAD src0_sel:DWORD src1_sel:WORD_1\n"
"v_mfma_f32_16x16x32_bf16 v[48:51], v[204:207], v[236:239], v[48:51]\n"
"ds_read_u16_d16_hi v251, v251 offset:18432\n"
"v_mfma_f32_16x16x32_bf16 v[52:55], v[208:211], v[236:239], v[52:55]\n"
"v_lshlrev_b32_sdwa v228, 1, v228 dst_sel:DWORD dst_unused:UNUSED_PAD src0_sel:DWORD src1_sel:WORD_1\n"
"v_mfma_f32_16x16x32_bf16 v[56:59], v[212:215], v[236:239], v[56:59]\n"
"ds_read_u16_d16 v228, v228 offset:18432\n"
"v_lshlrev_b32_sdwa v229, 1, v229 dst_sel:DWORD dst_unused:UNUSED_PAD src0_sel:DWORD src1_sel:WORD_1\n"
"v_mfma_f32_16x16x32_bf16 v[60:63], v[216:219], v[236:239], v[60:63]\n"
"ds_read_u16_d16_hi v229, v229 offset:18432\n"
"v_mfma_f32_16x16x32_bf16 v[64:67], v[220:223], v[236:239], v[64:67]\n"
"v_lshlrev_b32_sdwa v230, 1, v230 dst_sel:DWORD dst_unused:UNUSED_PAD src0_sel:DWORD src1_sel:WORD_1\n"
"v_mfma_f32_16x16x32_bf16 v[68:71], v[224:227], v[236:239], v[68:71]\n"
"ds_read_u16_d16 v230, v230 offset:18432\n"
"v_lshlrev_b32_sdwa v231, 1, v231 dst_sel:DWORD dst_unused:UNUSED_PAD src0_sel:DWORD src1_sel:WORD_1\n"
"v_mfma_f32_16x16x32_bf16 v[236:239], v[180:183], v[132:135], 0\n"
"s_waitcnt lgkmcnt(3)\n"
"ds_read_u16_d16_hi v231, v231 offset:18432\n"
"v_or_b32 v244, v244, v245\n"
"v_mfma_f32_16x16x32_bf16 v[236:239], v[184:187], v[136:139], v[236:239]\n"
"v_or_b32 v245, v246, v247\n"
"v_lshlrev_b32_sdwa v232, 1, v232 dst_sel:DWORD dst_unused:UNUSED_PAD src0_sel:DWORD src1_sel:WORD_1\n"
"v_mfma_f32_16x16x32_bf16 v[236:239], v[188:191], v[140:143], v[236:239]\n"
"ds_read_u16_d16 v232, v232 offset:18432\n"
"v_lshlrev_b32_sdwa v233, 1, v233 dst_sel:DWORD dst_unused:UNUSED_PAD src0_sel:DWORD src1_sel:WORD_1\n"
"v_mfma_f32_16x16x32_bf16 v[240:243], v[192:195], v[132:135], 0\n"
"ds_read_u16_d16_hi v233, v233 offset:18432\n"
"v_or_b32 v246, v248, v249\n"
"v_mfma_f32_16x16x32_bf16 v[240:243], v[196:199], v[136:139], v[240:243]\n"
"v_or_b32 v247, v250, v251\n"
"v_lshlrev_b32_sdwa v234, 1, v234 dst_sel:DWORD dst_unused:UNUSED_PAD src0_sel:DWORD src1_sel:WORD_1\n"
"v_mfma_f32_16x16x32_bf16 v[240:243], v[200:203], v[140:143], v[240:243]\n"
"ds_read_u16_d16 v234, v234 offset:18432\n"
"v_lshlrev_b32_sdwa v235, 1, v235 dst_sel:DWORD dst_unused:UNUSED_PAD src0_sel:DWORD src1_sel:WORD_1\n"
"v_mfma_f32_16x16x32_bf16 v[72:75], v[204:207], v[244:247], v[72:75]\n"
"ds_read_b64_tr_b16 v[204:205], v253 offset:18480\n"
"ds_read_b64_tr_b16 v[206:207], v253 offset:20016\n"
"ds_read_u16_d16_hi v235, v235 offset:18432\n"
"v_mfma_f32_16x16x32_bf16 v[76:79], v[208:211], v[244:247], v[76:79]\n"
"ds_read_b64_tr_b16 v[208:209], v253 offset:18488\n"
"ds_read_b64_tr_b16 v[210:211], v253 offset:20024\n"
"v_lshlrev_b32_sdwa v236, 1, v236 dst_sel:DWORD dst_unused:UNUSED_PAD src0_sel:DWORD src1_sel:WORD_1\n"
"v_mfma_f32_16x16x32_bf16 v[80:83], v[212:215], v[244:247], v[80:83]\n"
"ds_read_b64_tr_b16 v[212:213], v253 offset:18544\n"
"ds_read_b64_tr_b16 v[214:215], v253 offset:20080\n"
"ds_read_u16_d16 v236, v236 offset:18432\n"
"v_lshlrev_b32_sdwa v237, 1, v237 dst_sel:DWORD dst_unused:UNUSED_PAD src0_sel:DWORD src1_sel:WORD_1\n"
"v_mfma_f32_16x16x32_bf16 v[84:87], v[216:219], v[244:247], v[84:87]\n"
"ds_read_b64_tr_b16 v[216:217], v253 offset:18552\n"
"ds_read_b64_tr_b16 v[218:219], v253 offset:20088\n"
"ds_read_u16_d16_hi v237, v237 offset:18432\n"
"v_mfma_f32_16x16x32_bf16 v[88:91], v[220:223], v[244:247], v[88:91]\n"
"ds_read_b64_tr_b16 v[220:221], v253 offset:18608\n"
"ds_read_b64_tr_b16 v[222:223], v253 offset:20144\n"
"v_lshlrev_b32_sdwa v238, 1, v238 dst_sel:DWORD dst_unused:UNUSED_PAD src0_sel:DWORD src1_sel:WORD_1\n"
"v_mfma_f32_16x16x32_bf16 v[92:95], v[224:227], v[244:247], v[92:95]\n"
"ds_read_b64_tr_b16 v[224:225], v253 offset:18616\n"
"ds_read_b64_tr_b16 v[226:227], v253 offset:20152\n"
"ds_read_u16_d16 v238, v238 offset:18432\n"
"v_lshlrev_b32_sdwa v239, 1, v239 dst_sel:DWORD dst_unused:UNUSED_PAD src0_sel:DWORD src1_sel:WORD_1\n"
"v_mfma_f32_16x16x32_bf16 v[244:247], v[180:183], v[144:147], 0\n"
"s_waitcnt lgkmcnt(13)\n"
"ds_read_u16_d16_hi v239, v239 offset:18432\n"
"v_or_b32 v228, v228, v229\n"
"v_mfma_f32_16x16x32_bf16 v[244:247], v[184:187], v[148:151], v[244:247]\n"
"v_or_b32 v229, v230, v231\n"
"v_lshlrev_b32_sdwa v240, 1, v240 dst_sel:DWORD dst_unused:UNUSED_PAD src0_sel:DWORD src1_sel:WORD_1\n"
"v_mfma_f32_16x16x32_bf16 v[244:247], v[188:191], v[152:155], v[244:247]\n"
"ds_read_u16_d16 v240, v240 offset:18432\n"
"v_lshlrev_b32_sdwa v241, 1, v241 dst_sel:DWORD dst_unused:UNUSED_PAD src0_sel:DWORD src1_sel:WORD_1\n"
"v_mfma_f32_16x16x32_bf16 v[248:251], v[192:195], v[144:147], 0\n"
"ds_read_u16_d16_hi v241, v241 offset:18432\n"
"v_or_b32 v230, v232, v233\n"
"v_mfma_f32_16x16x32_bf16 v[248:251], v[196:199], v[148:151], v[248:251]\n"
"v_or_b32 v231, v234, v235\n"
"v_lshlrev_b32_sdwa v242, 1, v242 dst_sel:DWORD dst_unused:UNUSED_PAD src0_sel:DWORD src1_sel:WORD_1\n"
"v_mfma_f32_16x16x32_bf16 v[248:251], v[200:203], v[152:155], v[248:251]\n"
"ds_read_u16_d16 v242, v242 offset:18432\n"
"v_lshlrev_b32_sdwa v243, 1, v243 dst_sel:DWORD dst_unused:UNUSED_PAD src0_sel:DWORD src1_sel:WORD_1\n"
"s_waitcnt lgkmcnt(13)\n"
"v_mfma_f32_16x16x32_bf16 v[0:3], v[204:207], v[228:231], v[0:3]\n"
"ds_read_u16_d16_hi v243, v243 offset:18432\n"
"v_mfma_f32_16x16x32_bf16 v[4:7], v[208:211], v[228:231], v[4:7]\n"
"v_lshlrev_b32_sdwa v244, 1, v244 dst_sel:DWORD dst_unused:UNUSED_PAD src0_sel:DWORD src1_sel:WORD_1\n"
"v_mfma_f32_16x16x32_bf16 v[8:11], v[212:215], v[228:231], v[8:11]\n"
"ds_read_u16_d16 v244, v244 offset:18432\n"
"v_lshlrev_b32_sdwa v245, 1, v245 dst_sel:DWORD dst_unused:UNUSED_PAD src0_sel:DWORD src1_sel:WORD_1\n"
"s_waitcnt lgkmcnt(7)\n"
"v_mfma_f32_16x16x32_bf16 v[12:15], v[216:219], v[228:231], v[12:15]\n"
"ds_read_u16_d16_hi v245, v245 offset:18432\n"
"v_mfma_f32_16x16x32_bf16 v[16:19], v[220:223], v[228:231], v[16:19]\n"
"v_lshlrev_b32_sdwa v246, 1, v246 dst_sel:DWORD dst_unused:UNUSED_PAD src0_sel:DWORD src1_sel:WORD_1\n"
"v_mfma_f32_16x16x32_bf16 v[20:23], v[224:227], v[228:231], v[20:23]\n"
"ds_read_u16_d16 v246, v246 offset:18432\n"
"v_lshlrev_b32_sdwa v247, 1, v247 dst_sel:DWORD dst_unused:UNUSED_PAD src0_sel:DWORD src1_sel:WORD_1\n"
"v_mfma_f32_16x16x32_bf16 v[228:231], v[180:183], v[156:159], 0\n"
"ds_read_b64_tr_b16 v[180:181], v252 offset:24576\n"
"ds_read_b64_tr_b16 v[182:183], v252 offset:27648\n"
"s_waitcnt lgkmcnt(5)\n"
"ds_read_u16_d16_hi v247, v247 offset:18432\n"
"v_or_b32 v236, v236, v237\n"
"v_mfma_f32_16x16x32_bf16 v[228:231], v[184:187], v[160:163], v[228:231]\n"
"ds_read_b64_tr_b16 v[184:185], v252 offset:25600\n"
"ds_read_b64_tr_b16 v[186:187], v252 offset:28672\n"
"v_or_b32 v237, v238, v239\n"
"v_lshlrev_b32_sdwa v248, 1, v248 dst_sel:DWORD dst_unused:UNUSED_PAD src0_sel:DWORD src1_sel:WORD_1\n"
"v_mfma_f32_16x16x32_bf16 v[228:231], v[188:191], v[164:167], v[228:231]\n"
"ds_read_b64_tr_b16 v[188:189], v252 offset:26624\n"
"ds_read_b64_tr_b16 v[190:191], v252 offset:29696\n"
"ds_read_u16_d16 v248, v248 offset:18432\n"
"v_lshlrev_b32_sdwa v249, 1, v249 dst_sel:DWORD dst_unused:UNUSED_PAD src0_sel:DWORD src1_sel:WORD_1\n"
"v_mfma_f32_16x16x32_bf16 v[232:235], v[192:195], v[156:159], 0\n"
"ds_read_b64_tr_b16 v[192:193], v252 offset:25088\n"
"ds_read_b64_tr_b16 v[194:195], v252 offset:28160\n"
"ds_read_u16_d16_hi v249, v249 offset:18432\n"
"v_or_b32 v238, v240, v241\n"
"v_mfma_f32_16x16x32_bf16 v[232:235], v[196:199], v[160:163], v[232:235]\n"
"ds_read_b64_tr_b16 v[196:197], v252 offset:26112\n"
"ds_read_b64_tr_b16 v[198:199], v252 offset:29184\n"
"v_or_b32 v239, v242, v243\n"
"v_lshlrev_b32_sdwa v250, 1, v250 dst_sel:DWORD dst_unused:UNUSED_PAD src0_sel:DWORD src1_sel:WORD_1\n"
"v_mfma_f32_16x16x32_bf16 v[232:235], v[200:203], v[164:167], v[232:235]\n"
"ds_read_b64_tr_b16 v[200:201], v252 offset:27136\n"
"ds_read_b64_tr_b16 v[202:203], v252 offset:30208\n"
"ds_read_u16_d16 v250, v250 offset:18432\n"
"v_lshlrev_b32_sdwa v251, 1, v251 dst_sel:DWORD dst_unused:UNUSED_PAD src0_sel:DWORD src1_sel:WORD_1\n"
"v_mfma_f32_16x16x32_bf16 v[24:27], v[204:207], v[236:239], v[24:27]\n"
"ds_read_u16_d16_hi v251, v251 offset:18432\n"
"v_mfma_f32_16x16x32_bf16 v[28:31], v[208:211], v[236:239], v[28:31]\n"
"v_lshlrev_b32_sdwa v228, 1, v228 dst_sel:DWORD dst_unused:UNUSED_PAD src0_sel:DWORD src1_sel:WORD_1\n"
"v_mfma_f32_16x16x32_bf16 v[32:35], v[212:215], v[236:239], v[32:35]\n"
"ds_read_u16_d16 v228, v228 offset:18432\n"
"v_lshlrev_b32_sdwa v229, 1, v229 dst_sel:DWORD dst_unused:UNUSED_PAD src0_sel:DWORD src1_sel:WORD_1\n"
"v_mfma_f32_16x16x32_bf16 v[36:39], v[216:219], v[236:239], v[36:39]\n"
"ds_read_u16_d16_hi v229, v229 offset:18432\n"
"v_mfma_f32_16x16x32_bf16 v[40:43], v[220:223], v[236:239], v[40:43]\n"
"v_lshlrev_b32_sdwa v230, 1, v230 dst_sel:DWORD dst_unused:UNUSED_PAD src0_sel:DWORD src1_sel:WORD_1\n"
"v_mfma_f32_16x16x32_bf16 v[44:47], v[224:227], v[236:239], v[44:47]\n"
"ds_read_u16_d16 v230, v230 offset:18432\n"
"v_lshlrev_b32_sdwa v231, 1, v231 dst_sel:DWORD dst_unused:UNUSED_PAD src0_sel:DWORD src1_sel:WORD_1\n"
"s_waitcnt lgkmcnt(13)\n"
"v_mfma_f32_16x16x32_bf16 v[236:239], v[180:183], v[120:123], 0\n"
"s_waitcnt lgkmcnt(3)\n"
"ds_read_u16_d16_hi v231, v231 offset:18432\n"
"v_or_b32 v244, v244, v245\n"
"v_mfma_f32_16x16x32_bf16 v[236:239], v[184:187], v[124:127], v[236:239]\n"
"v_or_b32 v245, v246, v247\n"
"v_lshlrev_b32_sdwa v232, 1, v232 dst_sel:DWORD dst_unused:UNUSED_PAD src0_sel:DWORD src1_sel:WORD_1\n"
"v_mfma_f32_16x16x32_bf16 v[236:239], v[188:191], v[128:131], v[236:239]\n"
"ds_read_u16_d16 v232, v232 offset:18432\n"
"v_lshlrev_b32_sdwa v233, 1, v233 dst_sel:DWORD dst_unused:UNUSED_PAD src0_sel:DWORD src1_sel:WORD_1\n"
"v_mfma_f32_16x16x32_bf16 v[240:243], v[192:195], v[120:123], 0\n"
"ds_read_u16_d16_hi v233, v233 offset:18432\n"
"v_or_b32 v246, v248, v249\n"
"v_mfma_f32_16x16x32_bf16 v[240:243], v[196:199], v[124:127], v[240:243]\n"
"v_or_b32 v247, v250, v251\n"
"v_lshlrev_b32_sdwa v234, 1, v234 dst_sel:DWORD dst_unused:UNUSED_PAD src0_sel:DWORD src1_sel:WORD_1\n"
"v_mfma_f32_16x16x32_bf16 v[240:243], v[200:203], v[128:131], v[240:243]\n"
"ds_read_u16_d16 v234, v234 offset:18432\n"
"v_lshlrev_b32_sdwa v235, 1, v235 dst_sel:DWORD dst_unused:UNUSED_PAD src0_sel:DWORD src1_sel:WORD_1\n"
"v_mfma_f32_16x16x32_bf16 v[48:51], v[204:207], v[244:247], v[48:51]\n"
"ds_read_u16_d16_hi v235, v235 offset:18432\n"
"v_mfma_f32_16x16x32_bf16 v[52:55], v[208:211], v[244:247], v[52:55]\n"
"v_lshlrev_b32_sdwa v236, 1, v236 dst_sel:DWORD dst_unused:UNUSED_PAD src0_sel:DWORD src1_sel:WORD_1\n"
"v_mfma_f32_16x16x32_bf16 v[56:59], v[212:215], v[244:247], v[56:59]\n"
"ds_read_u16_d16 v236, v236 offset:18432\n"
"v_lshlrev_b32_sdwa v237, 1, v237 dst_sel:DWORD dst_unused:UNUSED_PAD src0_sel:DWORD src1_sel:WORD_1\n"
"v_mfma_f32_16x16x32_bf16 v[60:63], v[216:219], v[244:247], v[60:63]\n"
"ds_read_u16_d16_hi v237, v237 offset:18432\n"
"v_mfma_f32_16x16x32_bf16 v[64:67], v[220:223], v[244:247], v[64:67]\n"
"v_lshlrev_b32_sdwa v238, 1, v238 dst_sel:DWORD dst_unused:UNUSED_PAD src0_sel:DWORD src1_sel:WORD_1\n"
"v_mfma_f32_16x16x32_bf16 v[68:71], v[224:227], v[244:247], v[68:71]\n"
"ds_read_u16_d16 v238, v238 offset:18432\n"
"v_lshlrev_b32_sdwa v239, 1, v239 dst_sel:DWORD dst_unused:UNUSED_PAD src0_sel:DWORD src1_sel:WORD_1\n"
"v_mfma_f32_16x16x32_bf16 v[244:247], v[180:183], v[132:135], 0\n"
"s_waitcnt lgkmcnt(3)\n"
"ds_read_u16_d16_hi v239, v239 offset:18432\n"
"v_or_b32 v228, v228, v229\n"
"v_mfma_f32_16x16x32_bf16 v[244:247], v[184:187], v[136:139], v[244:247]\n"
"v_or_b32 v229, v230, v231\n"
"v_lshlrev_b32_sdwa v240, 1, v240 dst_sel:DWORD dst_unused:UNUSED_PAD src0_sel:DWORD src1_sel:WORD_1\n"
"v_mfma_f32_16x16x32_bf16 v[244:247], v[188:191], v[140:143], v[244:247]\n"
"ds_read_u16_d16 v240, v240 offset:18432\n"
"v_lshlrev_b32_sdwa v241, 1, v241 dst_sel:DWORD dst_unused:UNUSED_PAD src0_sel:DWORD src1_sel:WORD_1\n"
"v_mfma_f32_16x16x32_bf16 v[248:251], v[192:195], v[132:135], 0\n"
"ds_read_u16_d16_hi v241, v241 offset:18432\n"
"v_or_b32 v230, v232, v233\n"
"v_mfma_f32_16x16x32_bf16 v[248:251], v[196:199], v[136:139], v[248:251]\n"
"v_or_b32 v231, v234, v235\n"
"v_lshlrev_b32_sdwa v242, 1, v242 dst_sel:DWORD dst_unused:UNUSED_PAD src0_sel:DWORD src1_sel:WORD_1\n"
"v_mfma_f32_16x16x32_bf16 v[248:251], v[200:203], v[140:143], v[248:251]\n"
"ds_read_u16_d16 v242, v242 offset:18432\n"
"v_lshlrev_b32_sdwa v243, 1, v243 dst_sel:DWORD dst_unused:UNUSED_PAD src0_sel:DWORD src1_sel:WORD_1\n"
"v_mfma_f32_16x16x32_bf16 v[72:75], v[204:207], v[228:231], v[72:75]\n"
"ds_read_b64_tr_b16 v[204:205], v253 offset:24640\n"
"ds_read_b64_tr_b16 v[206:207], v253 offset:26176\n"
"ds_read_u16_d16_hi v243, v243 offset:18432\n"
"v_mfma_f32_16x16x32_bf16 v[76:79], v[208:211], v[228:231], v[76:79]\n"
"ds_read_b64_tr_b16 v[208:209], v253 offset:24648\n"
"ds_read_b64_tr_b16 v[210:211], v253 offset:26184\n"
"v_lshlrev_b32_sdwa v244, 1, v244 dst_sel:DWORD dst_unused:UNUSED_PAD src0_sel:DWORD src1_sel:WORD_1\n"
"v_mfma_f32_16x16x32_bf16 v[80:83], v[212:215], v[228:231], v[80:83]\n"
"ds_read_b64_tr_b16 v[212:213], v253 offset:24704\n"
"ds_read_b64_tr_b16 v[214:215], v253 offset:26240\n"
"ds_read_u16_d16 v244, v244 offset:18432\n"
"v_lshlrev_b32_sdwa v245, 1, v245 dst_sel:DWORD dst_unused:UNUSED_PAD src0_sel:DWORD src1_sel:WORD_1\n"
"v_mfma_f32_16x16x32_bf16 v[84:87], v[216:219], v[228:231], v[84:87]\n"
"ds_read_b64_tr_b16 v[216:217], v253 offset:24712\n"
"ds_read_b64_tr_b16 v[218:219], v253 offset:26248\n"
"ds_read_u16_d16_hi v245, v245 offset:18432\n"
"v_mfma_f32_16x16x32_bf16 v[88:91], v[220:223], v[228:231], v[88:91]\n"
"ds_read_b64_tr_b16 v[220:221], v253 offset:24768\n"
"ds_read_b64_tr_b16 v[222:223], v253 offset:26304\n"
"v_lshlrev_b32_sdwa v246, 1, v246 dst_sel:DWORD dst_unused:UNUSED_PAD src0_sel:DWORD src1_sel:WORD_1\n"
"v_mfma_f32_16x16x32_bf16 v[92:95], v[224:227], v[228:231], v[92:95]\n"
"ds_read_b64_tr_b16 v[224:225], v253 offset:24776\n"
"ds_read_b64_tr_b16 v[226:227], v253 offset:26312\n"
"ds_read_u16_d16 v246, v246 offset:18432\n"
"v_lshlrev_b32_sdwa v247, 1, v247 dst_sel:DWORD dst_unused:UNUSED_PAD src0_sel:DWORD src1_sel:WORD_1\n"
"v_mfma_f32_16x16x32_bf16 v[228:231], v[180:183], v[144:147], 0\n"
"s_waitcnt lgkmcnt(13)\n"
"ds_read_u16_d16_hi v247, v247 offset:18432\n"
"v_or_b32 v236, v236, v237\n"
"v_mfma_f32_16x16x32_bf16 v[228:231], v[184:187], v[148:151], v[228:231]\n"
"v_or_b32 v237, v238, v239\n"
"v_lshlrev_b32_sdwa v248, 1, v248 dst_sel:DWORD dst_unused:UNUSED_PAD src0_sel:DWORD src1_sel:WORD_1\n"
"v_mfma_f32_16x16x32_bf16 v[228:231], v[188:191], v[152:155], v[228:231]\n"
"ds_read_u16_d16 v248, v248 offset:18432\n"
"v_lshlrev_b32_sdwa v249, 1, v249 dst_sel:DWORD dst_unused:UNUSED_PAD src0_sel:DWORD src1_sel:WORD_1\n"
"v_mfma_f32_16x16x32_bf16 v[232:235], v[192:195], v[144:147], 0\n"
"ds_read_u16_d16_hi v249, v249 offset:18432\n"
"v_or_b32 v238, v240, v241\n"
"v_mfma_f32_16x16x32_bf16 v[232:235], v[196:199], v[148:151], v[232:235]\n"
"v_or_b32 v239, v242, v243\n"
"v_lshlrev_b32_sdwa v250, 1, v250 dst_sel:DWORD dst_unused:UNUSED_PAD src0_sel:DWORD src1_sel:WORD_1\n"
"v_mfma_f32_16x16x32_bf16 v[232:235], v[200:203], v[152:155], v[232:235]\n"
"ds_read_u16_d16 v250, v250 offset:18432\n"
"v_lshlrev_b32_sdwa v251, 1, v251 dst_sel:DWORD dst_unused:UNUSED_PAD src0_sel:DWORD src1_sel:WORD_1\n"
"s_waitcnt lgkmcnt(13)\n"
"v_mfma_f32_16x16x32_bf16 v[0:3], v[204:207], v[236:239], v[0:3]\n"
"ds_read_u16_d16_hi v251, v251 offset:18432\n"
"v_mfma_f32_16x16x32_bf16 v[4:7], v[208:211], v[236:239], v[4:7]\n"
"v_lshlrev_b32_sdwa v228, 1, v228 dst_sel:DWORD dst_unused:UNUSED_PAD src0_sel:DWORD src1_sel:WORD_1\n"
"v_mfma_f32_16x16x32_bf16 v[8:11], v[212:215], v[236:239], v[8:11]\n"
"ds_read_u16_d16 v228, v228 offset:18432\n"
"v_lshlrev_b32_sdwa v229, 1, v229 dst_sel:DWORD dst_unused:UNUSED_PAD src0_sel:DWORD src1_sel:WORD_1\n"
"s_waitcnt lgkmcnt(7)\n"
"v_mfma_f32_16x16x32_bf16 v[12:15], v[216:219], v[236:239], v[12:15]\n"
"ds_read_u16_d16_hi v229, v229 offset:18432\n"
"v_mfma_f32_16x16x32_bf16 v[16:19], v[220:223], v[236:239], v[16:19]\n"
"v_lshlrev_b32_sdwa v230, 1, v230 dst_sel:DWORD dst_unused:UNUSED_PAD src0_sel:DWORD src1_sel:WORD_1\n"
"v_mfma_f32_16x16x32_bf16 v[20:23], v[224:227], v[236:239], v[20:23]\n"
"ds_read_u16_d16 v230, v230 offset:18432\n"
"v_lshlrev_b32_sdwa v231, 1, v231 dst_sel:DWORD dst_unused:UNUSED_PAD src0_sel:DWORD src1_sel:WORD_1\n"
"v_mfma_f32_16x16x32_bf16 v[236:239], v[180:183], v[156:159], 0\n"
"s_waitcnt lgkmcnt(3)\n"
"ds_read_u16_d16_hi v231, v231 offset:18432\n"
"v_or_b32 v244, v244, v245\n"
"v_mfma_f32_16x16x32_bf16 v[236:239], v[184:187], v[160:163], v[236:239]\n"
"v_or_b32 v245, v246, v247\n"
"v_lshlrev_b32_sdwa v232, 1, v232 dst_sel:DWORD dst_unused:UNUSED_PAD src0_sel:DWORD src1_sel:WORD_1\n"
"v_mfma_f32_16x16x32_bf16 v[236:239], v[188:191], v[164:167], v[236:239]\n"
"ds_read_u16_d16 v232, v232 offset:18432\n"
"v_lshlrev_b32_sdwa v233, 1, v233 dst_sel:DWORD dst_unused:UNUSED_PAD src0_sel:DWORD src1_sel:WORD_1\n"
"v_mfma_f32_16x16x32_bf16 v[240:243], v[192:195], v[156:159], 0\n"
"ds_read_u16_d16_hi v233, v233 offset:18432\n"
"v_or_b32 v246, v248, v249\n"
"v_mfma_f32_16x16x32_bf16 v[240:243], v[196:199], v[160:163], v[240:243]\n"
"v_or_b32 v247, v250, v251\n"
"v_lshlrev_b32_sdwa v234, 1, v234 dst_sel:DWORD dst_unused:UNUSED_PAD src0_sel:DWORD src1_sel:WORD_1\n"
"v_mfma_f32_16x16x32_bf16 v[240:243], v[200:203], v[164:167], v[240:243]\n"
"ds_read_u16_d16 v234, v234 offset:18432\n"
"v_lshlrev_b32_sdwa v235, 1, v235 dst_sel:DWORD dst_unused:UNUSED_PAD src0_sel:DWORD src1_sel:WORD_1\n"
"v_mfma_f32_16x16x32_bf16 v[24:27], v[204:207], v[244:247], v[24:27]\n"
"ds_read_u16_d16_hi v235, v235 offset:18432\n"
"v_mfma_f32_16x16x32_bf16 v[28:31], v[208:211], v[244:247], v[28:31]\n"
"v_lshlrev_b32_sdwa v236, 1, v236 dst_sel:DWORD dst_unused:UNUSED_PAD src0_sel:DWORD src1_sel:WORD_1\n"
"v_mfma_f32_16x16x32_bf16 v[32:35], v[212:215], v[244:247], v[32:35]\n"
"ds_read_u16_d16 v236, v236 offset:18432\n"
"v_lshlrev_b32_sdwa v237, 1, v237 dst_sel:DWORD dst_unused:UNUSED_PAD src0_sel:DWORD src1_sel:WORD_1\n"
"v_mfma_f32_16x16x32_bf16 v[36:39], v[216:219], v[244:247], v[36:39]\n"
"ds_read_u16_d16_hi v237, v237 offset:18432\n"
"v_mfma_f32_16x16x32_bf16 v[40:43], v[220:223], v[244:247], v[40:43]\n"
"v_lshlrev_b32_sdwa v238, 1, v238 dst_sel:DWORD dst_unused:UNUSED_PAD src0_sel:DWORD src1_sel:WORD_1\n"
"v_mfma_f32_16x16x32_bf16 v[44:47], v[224:227], v[244:247], v[44:47]\n"
"ds_read_u16_d16 v238, v238 offset:18432\n"
"v_lshlrev_b32_sdwa v239, 1, v239 dst_sel:DWORD dst_unused:UNUSED_PAD src0_sel:DWORD src1_sel:WORD_1\n"
"v_mfma_f32_16x16x32_bf16 v[244:247], v[180:183], v[168:171], 0\n"
"ds_read_b64_tr_b16 v[180:181], v252 offset:30720\n"
"ds_read_b64_tr_b16 v[182:183], v252 offset:33792\n"
"s_waitcnt lgkmcnt(5)\n"
"ds_read_u16_d16_hi v239, v239 offset:18432\n"
"v_or_b32 v228, v228, v229\n"
"v_mfma_f32_16x16x32_bf16 v[244:247], v[184:187], v[172:175], v[244:247]\n"
"ds_read_b64_tr_b16 v[184:185], v252 offset:31744\n"
"ds_read_b64_tr_b16 v[186:187], v252 offset:34816\n"
"v_or_b32 v229, v230, v231\n"
"v_lshlrev_b32_sdwa v240, 1, v240 dst_sel:DWORD dst_unused:UNUSED_PAD src0_sel:DWORD src1_sel:WORD_1\n"
"v_mfma_f32_16x16x32_bf16 v[244:247], v[188:191], v[176:179], v[244:247]\n"
"ds_read_b64_tr_b16 v[188:189], v252 offset:32768\n"
"ds_read_b64_tr_b16 v[190:191], v252 offset:35840\n"
"ds_read_u16_d16 v240, v240 offset:18432\n"
"v_lshlrev_b32_sdwa v241, 1, v241 dst_sel:DWORD dst_unused:UNUSED_PAD src0_sel:DWORD src1_sel:WORD_1\n"
"v_mfma_f32_16x16x32_bf16 v[248:251], v[192:195], v[168:171], 0\n"
"ds_read_b64_tr_b16 v[192:193], v252 offset:31232\n"
"ds_read_b64_tr_b16 v[194:195], v252 offset:34304\n"
"ds_read_u16_d16_hi v241, v241 offset:18432\n"
"v_or_b32 v230, v232, v233\n"
"v_mfma_f32_16x16x32_bf16 v[248:251], v[196:199], v[172:175], v[248:251]\n"
"ds_read_b64_tr_b16 v[196:197], v252 offset:32256\n"
"ds_read_b64_tr_b16 v[198:199], v252 offset:35328\n"
"v_or_b32 v231, v234, v235\n"
"v_lshlrev_b32_sdwa v242, 1, v242 dst_sel:DWORD dst_unused:UNUSED_PAD src0_sel:DWORD src1_sel:WORD_1\n"
"v_mfma_f32_16x16x32_bf16 v[248:251], v[200:203], v[176:179], v[248:251]\n"
"ds_read_b64_tr_b16 v[200:201], v252 offset:33280\n"
"ds_read_b64_tr_b16 v[202:203], v252 offset:36352\n"
"ds_read_u16_d16 v242, v242 offset:18432\n"
"v_lshlrev_b32_sdwa v243, 1, v243 dst_sel:DWORD dst_unused:UNUSED_PAD src0_sel:DWORD src1_sel:WORD_1\n"
"v_mfma_f32_16x16x32_bf16 v[48:51], v[204:207], v[228:231], v[48:51]\n"
"ds_read_u16_d16_hi v243, v243 offset:18432\n"
"v_mfma_f32_16x16x32_bf16 v[52:55], v[208:211], v[228:231], v[52:55]\n"
"v_lshlrev_b32_sdwa v244, 1, v244 dst_sel:DWORD dst_unused:UNUSED_PAD src0_sel:DWORD src1_sel:WORD_1\n"
"v_mfma_f32_16x16x32_bf16 v[56:59], v[212:215], v[228:231], v[56:59]\n"
"ds_read_u16_d16 v244, v244 offset:18432\n"
"v_lshlrev_b32_sdwa v245, 1, v245 dst_sel:DWORD dst_unused:UNUSED_PAD src0_sel:DWORD src1_sel:WORD_1\n"
"v_mfma_f32_16x16x32_bf16 v[60:63], v[216:219], v[228:231], v[60:63]\n"
"ds_read_u16_d16_hi v245, v245 offset:18432\n"
"v_mfma_f32_16x16x32_bf16 v[64:67], v[220:223], v[228:231], v[64:67]\n"
"v_lshlrev_b32_sdwa v246, 1, v246 dst_sel:DWORD dst_unused:UNUSED_PAD src0_sel:DWORD src1_sel:WORD_1\n"
"v_mfma_f32_16x16x32_bf16 v[68:71], v[224:227], v[228:231], v[68:71]\n"
"ds_read_u16_d16 v246, v246 offset:18432\n"
"v_lshlrev_b32_sdwa v247, 1, v247 dst_sel:DWORD dst_unused:UNUSED_PAD src0_sel:DWORD src1_sel:WORD_1\n"
"s_waitcnt lgkmcnt(13)\n"
"v_mfma_f32_16x16x32_bf16 v[228:231], v[180:183], v[120:123], 0\n"
"s_waitcnt lgkmcnt(3)\n"
"ds_read_u16_d16_hi v247, v247 offset:18432\n"
"v_or_b32 v236, v236, v237\n"
"v_mfma_f32_16x16x32_bf16 v[228:231], v[184:187], v[124:127], v[228:231]\n"
"v_or_b32 v237, v238, v239\n"
"v_lshlrev_b32_sdwa v248, 1, v248 dst_sel:DWORD dst_unused:UNUSED_PAD src0_sel:DWORD src1_sel:WORD_1\n"
"v_mfma_f32_16x16x32_bf16 v[228:231], v[188:191], v[128:131], v[228:231]\n"
"ds_read_u16_d16 v248, v248 offset:18432\n"
"v_lshlrev_b32_sdwa v249, 1, v249 dst_sel:DWORD dst_unused:UNUSED_PAD src0_sel:DWORD src1_sel:WORD_1\n"
"v_mfma_f32_16x16x32_bf16 v[232:235], v[192:195], v[120:123], 0\n"
"ds_read_u16_d16_hi v249, v249 offset:18432\n"
"v_or_b32 v238, v240, v241\n"
"v_mfma_f32_16x16x32_bf16 v[232:235], v[196:199], v[124:127], v[232:235]\n"
"v_or_b32 v239, v242, v243\n"
"v_lshlrev_b32_sdwa v250, 1, v250 dst_sel:DWORD dst_unused:UNUSED_PAD src0_sel:DWORD src1_sel:WORD_1\n"
"v_mfma_f32_16x16x32_bf16 v[232:235], v[200:203], v[128:131], v[232:235]\n"
"ds_read_u16_d16 v250, v250 offset:18432\n"
"v_lshlrev_b32_sdwa v251, 1, v251 dst_sel:DWORD dst_unused:UNUSED_PAD src0_sel:DWORD src1_sel:WORD_1\n"
"v_mfma_f32_16x16x32_bf16 v[72:75], v[204:207], v[236:239], v[72:75]\n"
"ds_read_u16_d16_hi v251, v251 offset:18432\n"
"v_mfma_f32_16x16x32_bf16 v[76:79], v[208:211], v[236:239], v[76:79]\n"
"v_lshlrev_b32_sdwa v228, 1, v228 dst_sel:DWORD dst_unused:UNUSED_PAD src0_sel:DWORD src1_sel:WORD_1\n"
"v_mfma_f32_16x16x32_bf16 v[80:83], v[212:215], v[236:239], v[80:83]\n"
"ds_read_u16_d16 v228, v228 offset:18432\n"
"v_lshlrev_b32_sdwa v229, 1, v229 dst_sel:DWORD dst_unused:UNUSED_PAD src0_sel:DWORD src1_sel:WORD_1\n"
"v_mfma_f32_16x16x32_bf16 v[84:87], v[216:219], v[236:239], v[84:87]\n"
"ds_read_u16_d16_hi v229, v229 offset:18432\n"
"v_mfma_f32_16x16x32_bf16 v[88:91], v[220:223], v[236:239], v[88:91]\n"
"v_lshlrev_b32_sdwa v230, 1, v230 dst_sel:DWORD dst_unused:UNUSED_PAD src0_sel:DWORD src1_sel:WORD_1\n"
"v_mfma_f32_16x16x32_bf16 v[92:95], v[224:227], v[236:239], v[92:95]\n"
"ds_read_u16_d16 v230, v230 offset:18432\n"
"v_lshlrev_b32_sdwa v231, 1, v231 dst_sel:DWORD dst_unused:UNUSED_PAD src0_sel:DWORD src1_sel:WORD_1\n"
"v_mfma_f32_16x16x32_bf16 v[236:239], v[180:183], v[132:135], 0\n"
"s_waitcnt lgkmcnt(3)\n"
"ds_read_u16_d16_hi v231, v231 offset:18432\n"
"v_or_b32 v244, v244, v245\n"
"v_mfma_f32_16x16x32_bf16 v[236:239], v[184:187], v[136:139], v[236:239]\n"
"v_or_b32 v245, v246, v247\n"
"v_lshlrev_b32_sdwa v232, 1, v232 dst_sel:DWORD dst_unused:UNUSED_PAD src0_sel:DWORD src1_sel:WORD_1\n"
"v_mfma_f32_16x16x32_bf16 v[236:239], v[188:191], v[140:143], v[236:239]\n"
"ds_read_u16_d16 v232, v232 offset:18432\n"
"v_lshlrev_b32_sdwa v233, 1, v233 dst_sel:DWORD dst_unused:UNUSED_PAD src0_sel:DWORD src1_sel:WORD_1\n"
"v_mfma_f32_16x16x32_bf16 v[240:243], v[192:195], v[132:135], 0\n"
"ds_read_u16_d16_hi v233, v233 offset:18432\n"
"v_or_b32 v246, v248, v249\n"
"v_mfma_f32_16x16x32_bf16 v[240:243], v[196:199], v[136:139], v[240:243]\n"
"v_or_b32 v247, v250, v251\n"
"v_lshlrev_b32_sdwa v234, 1, v234 dst_sel:DWORD dst_unused:UNUSED_PAD src0_sel:DWORD src1_sel:WORD_1\n"
"v_mfma_f32_16x16x32_bf16 v[240:243], v[200:203], v[140:143], v[240:243]\n"
"ds_read_u16_d16 v234, v234 offset:18432\n"
"v_lshlrev_b32_sdwa v235, 1, v235 dst_sel:DWORD dst_unused:UNUSED_PAD src0_sel:DWORD src1_sel:WORD_1\n"
"v_mfma_f32_16x16x32_bf16 v[96:99], v[204:207], v[244:247], v[96:99]\n"
"ds_read_b64_tr_b16 v[204:205], v253 offset:30800\n"
"ds_read_b64_tr_b16 v[206:207], v253 offset:32336\n"
"ds_read_u16_d16_hi v235, v235 offset:18432\n"
"v_mfma_f32_16x16x32_bf16 v[100:103], v[208:211], v[244:247], v[100:103]\n"
"ds_read_b64_tr_b16 v[208:209], v253 offset:30808\n"
"ds_read_b64_tr_b16 v[210:211], v253 offset:32344\n"
"v_lshlrev_b32_sdwa v236, 1, v236 dst_sel:DWORD dst_unused:UNUSED_PAD src0_sel:DWORD src1_sel:WORD_1\n"
"v_mfma_f32_16x16x32_bf16 v[104:107], v[212:215], v[244:247], v[104:107]\n"
"ds_read_b64_tr_b16 v[212:213], v253 offset:30864\n"
"ds_read_b64_tr_b16 v[214:215], v253 offset:32400\n"
"ds_read_u16_d16 v236, v236 offset:18432\n"
"v_lshlrev_b32_sdwa v237, 1, v237 dst_sel:DWORD dst_unused:UNUSED_PAD src0_sel:DWORD src1_sel:WORD_1\n"
"v_mfma_f32_16x16x32_bf16 v[108:111], v[216:219], v[244:247], v[108:111]\n"
"ds_read_b64_tr_b16 v[216:217], v253 offset:30872\n"
"ds_read_b64_tr_b16 v[218:219], v253 offset:32408\n"
"ds_read_u16_d16_hi v237, v237 offset:18432\n"
"v_mfma_f32_16x16x32_bf16 v[112:115], v[220:223], v[244:247], v[112:115]\n"
"ds_read_b64_tr_b16 v[220:221], v253 offset:30928\n"
"ds_read_b64_tr_b16 v[222:223], v253 offset:32464\n"
"v_lshlrev_b32_sdwa v238, 1, v238 dst_sel:DWORD dst_unused:UNUSED_PAD src0_sel:DWORD src1_sel:WORD_1\n"
"v_mfma_f32_16x16x32_bf16 v[116:119], v[224:227], v[244:247], v[116:119]\n"
"ds_read_b64_tr_b16 v[224:225], v253 offset:30936\n"
"ds_read_b64_tr_b16 v[226:227], v253 offset:32472\n"
"ds_read_u16_d16 v238, v238 offset:18432\n"
"v_lshlrev_b32_sdwa v239, 1, v239 dst_sel:DWORD dst_unused:UNUSED_PAD src0_sel:DWORD src1_sel:WORD_1\n"
"v_mfma_f32_16x16x32_bf16 v[244:247], v[180:183], v[144:147], 0\n"
"s_waitcnt lgkmcnt(13)\n"
"ds_read_u16_d16_hi v239, v239 offset:18432\n"
"v_or_b32 v228, v228, v229\n"
"v_mfma_f32_16x16x32_bf16 v[244:247], v[184:187], v[148:151], v[244:247]\n"
"v_or_b32 v229, v230, v231\n"
"v_lshlrev_b32_sdwa v240, 1, v240 dst_sel:DWORD dst_unused:UNUSED_PAD src0_sel:DWORD src1_sel:WORD_1\n"
"v_mfma_f32_16x16x32_bf16 v[244:247], v[188:191], v[152:155], v[244:247]\n"
"ds_read_u16_d16 v240, v240 offset:18432\n"
"v_lshlrev_b32_sdwa v241, 1, v241 dst_sel:DWORD dst_unused:UNUSED_PAD src0_sel:DWORD src1_sel:WORD_1\n"
"v_mfma_f32_16x16x32_bf16 v[248:251], v[192:195], v[144:147], 0\n"
"ds_read_u16_d16_hi v241, v241 offset:18432\n"
"v_or_b32 v230, v232, v233\n"
"v_mfma_f32_16x16x32_bf16 v[248:251], v[196:199], v[148:151], v[248:251]\n"
"v_or_b32 v231, v234, v235\n"
"v_lshlrev_b32_sdwa v242, 1, v242 dst_sel:DWORD dst_unused:UNUSED_PAD src0_sel:DWORD src1_sel:WORD_1\n"
"v_mfma_f32_16x16x32_bf16 v[248:251], v[200:203], v[152:155], v[248:251]\n"
"ds_read_u16_d16 v242, v242 offset:18432\n"
"v_lshlrev_b32_sdwa v243, 1, v243 dst_sel:DWORD dst_unused:UNUSED_PAD src0_sel:DWORD src1_sel:WORD_1\n"
"s_waitcnt lgkmcnt(13)\n"
"v_mfma_f32_16x16x32_bf16 v[0:3], v[204:207], v[228:231], v[0:3]\n"
"ds_read_u16_d16_hi v243, v243 offset:18432\n"
"v_mfma_f32_16x16x32_bf16 v[4:7], v[208:211], v[228:231], v[4:7]\n"
"v_lshlrev_b32_sdwa v244, 1, v244 dst_sel:DWORD dst_unused:UNUSED_PAD src0_sel:DWORD src1_sel:WORD_1\n"
"v_mfma_f32_16x16x32_bf16 v[8:11], v[212:215], v[228:231], v[8:11]\n"
"ds_read_u16_d16 v244, v244 offset:18432\n"
"v_lshlrev_b32_sdwa v245, 1, v245 dst_sel:DWORD dst_unused:UNUSED_PAD src0_sel:DWORD src1_sel:WORD_1\n"
"s_waitcnt lgkmcnt(7)\n"
"v_mfma_f32_16x16x32_bf16 v[12:15], v[216:219], v[228:231], v[12:15]\n"
"ds_read_u16_d16_hi v245, v245 offset:18432\n"
"v_mfma_f32_16x16x32_bf16 v[16:19], v[220:223], v[228:231], v[16:19]\n"
"v_lshlrev_b32_sdwa v246, 1, v246 dst_sel:DWORD dst_unused:UNUSED_PAD src0_sel:DWORD src1_sel:WORD_1\n"
"v_mfma_f32_16x16x32_bf16 v[20:23], v[224:227], v[228:231], v[20:23]\n"
"ds_read_u16_d16 v246, v246 offset:18432\n"
"v_lshlrev_b32_sdwa v247, 1, v247 dst_sel:DWORD dst_unused:UNUSED_PAD src0_sel:DWORD src1_sel:WORD_1\n"
"v_mfma_f32_16x16x32_bf16 v[228:231], v[180:183], v[156:159], 0\n"
"s_waitcnt lgkmcnt(3)\n"
"ds_read_u16_d16_hi v247, v247 offset:18432\n"
"v_or_b32 v236, v236, v237\n"
"v_mfma_f32_16x16x32_bf16 v[228:231], v[184:187], v[160:163], v[228:231]\n"
"v_or_b32 v237, v238, v239\n"
"v_lshlrev_b32_sdwa v248, 1, v248 dst_sel:DWORD dst_unused:UNUSED_PAD src0_sel:DWORD src1_sel:WORD_1\n"
"v_mfma_f32_16x16x32_bf16 v[228:231], v[188:191], v[164:167], v[228:231]\n"
"ds_read_u16_d16 v248, v248 offset:18432\n"
"v_lshlrev_b32_sdwa v249, 1, v249 dst_sel:DWORD dst_unused:UNUSED_PAD src0_sel:DWORD src1_sel:WORD_1\n"
"v_mfma_f32_16x16x32_bf16 v[232:235], v[192:195], v[156:159], 0\n"
"ds_read_u16_d16_hi v249, v249 offset:18432\n"
"v_or_b32 v238, v240, v241\n"
"v_mfma_f32_16x16x32_bf16 v[232:235], v[196:199], v[160:163], v[232:235]\n"
"v_or_b32 v239, v242, v243\n"
"v_lshlrev_b32_sdwa v250, 1, v250 dst_sel:DWORD dst_unused:UNUSED_PAD src0_sel:DWORD src1_sel:WORD_1\n"
"v_mfma_f32_16x16x32_bf16 v[232:235], v[200:203], v[164:167], v[232:235]\n"
"ds_read_u16_d16 v250, v250 offset:18432\n"
"v_lshlrev_b32_sdwa v251, 1, v251 dst_sel:DWORD dst_unused:UNUSED_PAD src0_sel:DWORD src1_sel:WORD_1\n"
"v_mfma_f32_16x16x32_bf16 v[24:27], v[204:207], v[236:239], v[24:27]\n"
"ds_read_u16_d16_hi v251, v251 offset:18432\n"
"v_mfma_f32_16x16x32_bf16 v[28:31], v[208:211], v[236:239], v[28:31]\n"
"v_lshlrev_b32_sdwa v228, 1, v228 dst_sel:DWORD dst_unused:UNUSED_PAD src0_sel:DWORD src1_sel:WORD_1\n"
"v_mfma_f32_16x16x32_bf16 v[32:35], v[212:215], v[236:239], v[32:35]\n"
"ds_read_u16_d16 v228, v228 offset:18432\n"
"v_lshlrev_b32_sdwa v229, 1, v229 dst_sel:DWORD dst_unused:UNUSED_PAD src0_sel:DWORD src1_sel:WORD_1\n"
"v_mfma_f32_16x16x32_bf16 v[36:39], v[216:219], v[236:239], v[36:39]\n"
"ds_read_u16_d16_hi v229, v229 offset:18432\n"
"v_mfma_f32_16x16x32_bf16 v[40:43], v[220:223], v[236:239], v[40:43]\n"
"v_lshlrev_b32_sdwa v230, 1, v230 dst_sel:DWORD dst_unused:UNUSED_PAD src0_sel:DWORD src1_sel:WORD_1\n"
"v_mfma_f32_16x16x32_bf16 v[44:47], v[224:227], v[236:239], v[44:47]\n"
"ds_read_u16_d16 v230, v230 offset:18432\n"
"v_lshlrev_b32_sdwa v231, 1, v231 dst_sel:DWORD dst_unused:UNUSED_PAD src0_sel:DWORD src1_sel:WORD_1\n"
"v_mov_b32 v236, 163784\n"
"s_setprio 0\n"
".Lqa_%=:\n"
"ds_read_b32 v237, v236\n"
"s_waitcnt lgkmcnt(0)\n"
"v_readfirstlane_b32 s66, v237\n"
"s_nop 1\n"
"s_cmp_lt_u32 s66, 4\n"
"s_cbranch_scc1 .Lqa_%=\n"
"v_mfma_f32_16x16x32_bf16 v[236:239], v[180:183], v[168:171], 0\n"
"ds_read_b64_tr_b16 v[180:181], v252 offset:36864\n"
"ds_read_b64_tr_b16 v[182:183], v252 offset:39936\n"
"ds_read_u16_d16_hi v231, v231 offset:18432\n"
"v_or_b32 v244, v244, v245\n"
"v_mfma_f32_16x16x32_bf16 v[236:239], v[184:187], v[172:175], v[236:239]\n"
"ds_read_b64_tr_b16 v[184:185], v252 offset:37888\n"
"ds_read_b64_tr_b16 v[186:187], v252 offset:40960\n"
"v_or_b32 v245, v246, v247\n"
"v_lshlrev_b32_sdwa v232, 1, v232 dst_sel:DWORD dst_unused:UNUSED_PAD src0_sel:DWORD src1_sel:WORD_1\n"
"v_mfma_f32_16x16x32_bf16 v[236:239], v[188:191], v[176:179], v[236:239]\n"
"ds_read_b64_tr_b16 v[188:189], v252 offset:38912\n"
"ds_read_b64_tr_b16 v[190:191], v252 offset:41984\n"
"ds_read_u16_d16 v232, v232 offset:18432\n"
"v_lshlrev_b32_sdwa v233, 1, v233 dst_sel:DWORD dst_unused:UNUSED_PAD src0_sel:DWORD src1_sel:WORD_1\n"
"v_mfma_f32_16x16x32_bf16 v[240:243], v[192:195], v[168:171], 0\n"
"ds_read_b64_tr_b16 v[192:193], v252 offset:37376\n"
"ds_read_b64_tr_b16 v[194:195], v252 offset:40448\n"
"ds_read_u16_d16_hi v233, v233 offset:18432\n"
"v_or_b32 v246, v248, v249\n"
"v_mfma_f32_16x16x32_bf16 v[240:243], v[196:199], v[172:175], v[240:243]\n"
"ds_read_b64_tr_b16 v[196:197], v252 offset:38400\n"
"ds_read_b64_tr_b16 v[198:199], v252 offset:41472\n"
"v_or_b32 v247, v250, v251\n"
"v_lshlrev_b32_sdwa v234, 1, v234 dst_sel:DWORD dst_unused:UNUSED_PAD src0_sel:DWORD src1_sel:WORD_1\n"
"v_mfma_f32_16x16x32_bf16 v[240:243], v[200:203], v[176:179], v[240:243]\n"
"ds_read_b64_tr_b16 v[200:201], v252 offset:39424\n"
"ds_read_b64_tr_b16 v[202:203], v252 offset:42496\n"
"ds_read_u16_d16 v234, v234 offset:18432\n"
"v_lshlrev_b32_sdwa v235, 1, v235 dst_sel:DWORD dst_unused:UNUSED_PAD src0_sel:DWORD src1_sel:WORD_1\n"
"v_mfma_f32_16x16x32_bf16 v[48:51], v[204:207], v[244:247], v[48:51]\n"
"ds_read_u16_d16_hi v235, v235 offset:18432\n"
"v_mfma_f32_16x16x32_bf16 v[52:55], v[208:211], v[244:247], v[52:55]\n"
"v_lshlrev_b32_sdwa v236, 1, v236 dst_sel:DWORD dst_unused:UNUSED_PAD src0_sel:DWORD src1_sel:WORD_1\n"
"v_mfma_f32_16x16x32_bf16 v[56:59], v[212:215], v[244:247], v[56:59]\n"
"ds_read_u16_d16 v236, v236 offset:18432\n"
"v_lshlrev_b32_sdwa v237, 1, v237 dst_sel:DWORD dst_unused:UNUSED_PAD src0_sel:DWORD src1_sel:WORD_1\n"
"v_mfma_f32_16x16x32_bf16 v[60:63], v[216:219], v[244:247], v[60:63]\n"
"ds_read_u16_d16_hi v237, v237 offset:18432\n"
"v_mfma_f32_16x16x32_bf16 v[64:67], v[220:223], v[244:247], v[64:67]\n"
"v_lshlrev_b32_sdwa v238, 1, v238 dst_sel:DWORD dst_unused:UNUSED_PAD src0_sel:DWORD src1_sel:WORD_1\n"
"v_mfma_f32_16x16x32_bf16 v[68:71], v[224:227], v[244:247], v[68:71]\n"
"ds_read_u16_d16 v238, v238 offset:18432\n"
"v_lshlrev_b32_sdwa v239, 1, v239 dst_sel:DWORD dst_unused:UNUSED_PAD src0_sel:DWORD src1_sel:WORD_1\n"
"s_waitcnt lgkmcnt(13)\n"
"v_mfma_f32_16x16x32_bf16 v[244:247], v[180:183], v[120:123], 0\n"
"s_waitcnt lgkmcnt(3)\n"
"ds_read_u16_d16_hi v239, v239 offset:18432\n"
"v_or_b32 v228, v228, v229\n"
"v_mfma_f32_16x16x32_bf16 v[244:247], v[184:187], v[124:127], v[244:247]\n"
"v_or_b32 v229, v230, v231\n"
"v_lshlrev_b32_sdwa v240, 1, v240 dst_sel:DWORD dst_unused:UNUSED_PAD src0_sel:DWORD src1_sel:WORD_1\n"
"v_mfma_f32_16x16x32_bf16 v[244:247], v[188:191], v[128:131], v[244:247]\n"
"ds_read_u16_d16 v240, v240 offset:18432\n"
"v_lshlrev_b32_sdwa v241, 1, v241 dst_sel:DWORD dst_unused:UNUSED_PAD src0_sel:DWORD src1_sel:WORD_1\n"
"v_mfma_f32_16x16x32_bf16 v[248:251], v[192:195], v[120:123], 0\n"
"ds_read_u16_d16_hi v241, v241 offset:18432\n"
"v_or_b32 v230, v232, v233\n"
"v_mfma_f32_16x16x32_bf16 v[248:251], v[196:199], v[124:127], v[248:251]\n"
"v_or_b32 v231, v234, v235\n"
"v_lshlrev_b32_sdwa v242, 1, v242 dst_sel:DWORD dst_unused:UNUSED_PAD src0_sel:DWORD src1_sel:WORD_1\n"
"v_mfma_f32_16x16x32_bf16 v[248:251], v[200:203], v[128:131], v[248:251]\n"
"ds_read_u16_d16 v242, v242 offset:18432\n"
"v_lshlrev_b32_sdwa v243, 1, v243 dst_sel:DWORD dst_unused:UNUSED_PAD src0_sel:DWORD src1_sel:WORD_1\n"
"v_mfma_f32_16x16x32_bf16 v[72:75], v[204:207], v[228:231], v[72:75]\n"
"ds_read_u16_d16_hi v243, v243 offset:18432\n"
"v_mfma_f32_16x16x32_bf16 v[76:79], v[208:211], v[228:231], v[76:79]\n"
"v_lshlrev_b32_sdwa v244, 1, v244 dst_sel:DWORD dst_unused:UNUSED_PAD src0_sel:DWORD src1_sel:WORD_1\n"
"v_mfma_f32_16x16x32_bf16 v[80:83], v[212:215], v[228:231], v[80:83]\n"
"ds_read_u16_d16 v244, v244 offset:18432\n"
"v_lshlrev_b32_sdwa v245, 1, v245 dst_sel:DWORD dst_unused:UNUSED_PAD src0_sel:DWORD src1_sel:WORD_1\n"
"v_mfma_f32_16x16x32_bf16 v[84:87], v[216:219], v[228:231], v[84:87]\n"
"ds_read_u16_d16_hi v245, v245 offset:18432\n"
"v_mfma_f32_16x16x32_bf16 v[88:91], v[220:223], v[228:231], v[88:91]\n"
"v_lshlrev_b32_sdwa v246, 1, v246 dst_sel:DWORD dst_unused:UNUSED_PAD src0_sel:DWORD src1_sel:WORD_1\n"
"v_mfma_f32_16x16x32_bf16 v[92:95], v[224:227], v[228:231], v[92:95]\n"
"ds_read_u16_d16 v246, v246 offset:18432\n"
"v_lshlrev_b32_sdwa v247, 1, v247 dst_sel:DWORD dst_unused:UNUSED_PAD src0_sel:DWORD src1_sel:WORD_1\n"
"v_mfma_f32_16x16x32_bf16 v[228:231], v[180:183], v[132:135], 0\n"
"s_waitcnt lgkmcnt(3)\n"
"ds_read_u16_d16_hi v247, v247 offset:18432\n"
"v_or_b32 v236, v236, v237\n"
"v_mfma_f32_16x16x32_bf16 v[228:231], v[184:187], v[136:139], v[228:231]\n"
"v_or_b32 v237, v238, v239\n"
"v_lshlrev_b32_sdwa v248, 1, v248 dst_sel:DWORD dst_unused:UNUSED_PAD src0_sel:DWORD src1_sel:WORD_1\n"
"v_mfma_f32_16x16x32_bf16 v[228:231], v[188:191], v[140:143], v[228:231]\n"
"ds_read_u16_d16 v248, v248 offset:18432\n"
"v_lshlrev_b32_sdwa v249, 1, v249 dst_sel:DWORD dst_unused:UNUSED_PAD src0_sel:DWORD src1_sel:WORD_1\n"
"v_mfma_f32_16x16x32_bf16 v[232:235], v[192:195], v[132:135], 0\n"
"ds_read_u16_d16_hi v249, v249 offset:18432\n"
"v_or_b32 v238, v240, v241\n"
"v_mfma_f32_16x16x32_bf16 v[232:235], v[196:199], v[136:139], v[232:235]\n"
"v_or_b32 v239, v242, v243\n"
"v_lshlrev_b32_sdwa v250, 1, v250 dst_sel:DWORD dst_unused:UNUSED_PAD src0_sel:DWORD src1_sel:WORD_1\n"
"v_mfma_f32_16x16x32_bf16 v[232:235], v[200:203], v[140:143], v[232:235]\n"
"ds_read_u16_d16 v250, v250 offset:18432\n"
"v_lshlrev_b32_sdwa v251, 1, v251 dst_sel:DWORD dst_unused:UNUSED_PAD src0_sel:DWORD src1_sel:WORD_1\n"
"v_mfma_f32_16x16x32_bf16 v[96:99], v[204:207], v[236:239], v[96:99]\n"
"ds_read_b64_tr_b16 v[204:205], v253 offset:36960\n"
"ds_read_b64_tr_b16 v[206:207], v253 offset:38496\n"
"ds_read_u16_d16_hi v251, v251 offset:18432\n"
"v_mfma_f32_16x16x32_bf16 v[100:103], v[208:211], v[236:239], v[100:103]\n"
"ds_read_b64_tr_b16 v[208:209], v253 offset:36968\n"
"ds_read_b64_tr_b16 v[210:211], v253 offset:38504\n"
"v_lshlrev_b32_sdwa v228, 1, v228 dst_sel:DWORD dst_unused:UNUSED_PAD src0_sel:DWORD src1_sel:WORD_1\n"
"v_mfma_f32_16x16x32_bf16 v[104:107], v[212:215], v[236:239], v[104:107]\n"
"ds_read_b64_tr_b16 v[212:213], v253 offset:37024\n"
"ds_read_b64_tr_b16 v[214:215], v253 offset:38560\n"
"ds_read_u16_d16 v228, v228 offset:18432\n"
"v_lshlrev_b32_sdwa v229, 1, v229 dst_sel:DWORD dst_unused:UNUSED_PAD src0_sel:DWORD src1_sel:WORD_1\n"
"v_mfma_f32_16x16x32_bf16 v[108:111], v[216:219], v[236:239], v[108:111]\n"
"ds_read_b64_tr_b16 v[216:217], v253 offset:37032\n"
"ds_read_b64_tr_b16 v[218:219], v253 offset:38568\n"
"ds_read_u16_d16_hi v229, v229 offset:18432\n"
"v_mfma_f32_16x16x32_bf16 v[112:115], v[220:223], v[236:239], v[112:115]\n"
"ds_read_b64_tr_b16 v[220:221], v253 offset:37088\n"
"ds_read_b64_tr_b16 v[222:223], v253 offset:38624\n"
"v_lshlrev_b32_sdwa v230, 1, v230 dst_sel:DWORD dst_unused:UNUSED_PAD src0_sel:DWORD src1_sel:WORD_1\n"
"v_mfma_f32_16x16x32_bf16 v[116:119], v[224:227], v[236:239], v[116:119]\n"
"ds_read_b64_tr_b16 v[224:225], v253 offset:37096\n"
"ds_read_b64_tr_b16 v[226:227], v253 offset:38632\n"
"ds_read_u16_d16 v230, v230 offset:18432\n"
"v_lshlrev_b32_sdwa v231, 1, v231 dst_sel:DWORD dst_unused:UNUSED_PAD src0_sel:DWORD src1_sel:WORD_1\n"
"v_mfma_f32_16x16x32_bf16 v[236:239], v[180:183], v[144:147], 0\n"
"s_waitcnt lgkmcnt(13)\n"
"ds_read_u16_d16_hi v231, v231 offset:18432\n"
"v_or_b32 v244, v244, v245\n"
"v_mfma_f32_16x16x32_bf16 v[236:239], v[184:187], v[148:151], v[236:239]\n"
"v_or_b32 v245, v246, v247\n"
"v_lshlrev_b32_sdwa v232, 1, v232 dst_sel:DWORD dst_unused:UNUSED_PAD src0_sel:DWORD src1_sel:WORD_1\n"
"v_mfma_f32_16x16x32_bf16 v[236:239], v[188:191], v[152:155], v[236:239]\n"
"ds_read_u16_d16 v232, v232 offset:18432\n"
"v_lshlrev_b32_sdwa v233, 1, v233 dst_sel:DWORD dst_unused:UNUSED_PAD src0_sel:DWORD src1_sel:WORD_1\n"
"v_mfma_f32_16x16x32_bf16 v[240:243], v[192:195], v[144:147], 0\n"
"ds_read_u16_d16_hi v233, v233 offset:18432\n"
"v_or_b32 v246, v248, v249\n"
"v_mfma_f32_16x16x32_bf16 v[240:243], v[196:199], v[148:151], v[240:243]\n"
"v_or_b32 v247, v250, v251\n"
"v_lshlrev_b32_sdwa v234, 1, v234 dst_sel:DWORD dst_unused:UNUSED_PAD src0_sel:DWORD src1_sel:WORD_1\n"
"v_mfma_f32_16x16x32_bf16 v[240:243], v[200:203], v[152:155], v[240:243]\n"
"ds_read_u16_d16 v234, v234 offset:18432\n"
"v_lshlrev_b32_sdwa v235, 1, v235 dst_sel:DWORD dst_unused:UNUSED_PAD src0_sel:DWORD src1_sel:WORD_1\n"
"s_waitcnt lgkmcnt(13)\n"
"v_mfma_f32_16x16x32_bf16 v[0:3], v[204:207], v[244:247], v[0:3]\n"
"ds_read_u16_d16_hi v235, v235 offset:18432\n"
"v_mfma_f32_16x16x32_bf16 v[4:7], v[208:211], v[244:247], v[4:7]\n"
"v_lshlrev_b32_sdwa v236, 1, v236 dst_sel:DWORD dst_unused:UNUSED_PAD src0_sel:DWORD src1_sel:WORD_1\n"
"v_mfma_f32_16x16x32_bf16 v[8:11], v[212:215], v[244:247], v[8:11]\n"
"ds_read_u16_d16 v236, v236 offset:18432\n"
"v_lshlrev_b32_sdwa v237, 1, v237 dst_sel:DWORD dst_unused:UNUSED_PAD src0_sel:DWORD src1_sel:WORD_1\n"
"s_waitcnt lgkmcnt(7)\n"
"v_mfma_f32_16x16x32_bf16 v[12:15], v[216:219], v[244:247], v[12:15]\n"
"ds_read_u16_d16_hi v237, v237 offset:18432\n"
"v_mfma_f32_16x16x32_bf16 v[16:19], v[220:223], v[244:247], v[16:19]\n"
"v_lshlrev_b32_sdwa v238, 1, v238 dst_sel:DWORD dst_unused:UNUSED_PAD src0_sel:DWORD src1_sel:WORD_1\n"
"v_mfma_f32_16x16x32_bf16 v[20:23], v[224:227], v[244:247], v[20:23]\n"
"ds_read_u16_d16 v238, v238 offset:18432\n"
"v_lshlrev_b32_sdwa v239, 1, v239 dst_sel:DWORD dst_unused:UNUSED_PAD src0_sel:DWORD src1_sel:WORD_1\n"
"v_mfma_f32_16x16x32_bf16 v[244:247], v[180:183], v[156:159], 0\n"
"s_waitcnt lgkmcnt(3)\n"
"ds_read_u16_d16_hi v239, v239 offset:18432\n"
"v_or_b32 v228, v228, v229\n"
"v_mfma_f32_16x16x32_bf16 v[244:247], v[184:187], v[160:163], v[244:247]\n"
"v_or_b32 v229, v230, v231\n"
"v_lshlrev_b32_sdwa v240, 1, v240 dst_sel:DWORD dst_unused:UNUSED_PAD src0_sel:DWORD src1_sel:WORD_1\n"
"v_mfma_f32_16x16x32_bf16 v[244:247], v[188:191], v[164:167], v[244:247]\n"
"ds_read_u16_d16 v240, v240 offset:18432\n"
"v_lshlrev_b32_sdwa v241, 1, v241 dst_sel:DWORD dst_unused:UNUSED_PAD src0_sel:DWORD src1_sel:WORD_1\n"
"v_mfma_f32_16x16x32_bf16 v[248:251], v[192:195], v[156:159], 0\n"
"ds_read_u16_d16_hi v241, v241 offset:18432\n"
"v_or_b32 v230, v232, v233\n"
"v_mfma_f32_16x16x32_bf16 v[248:251], v[196:199], v[160:163], v[248:251]\n"
"v_or_b32 v231, v234, v235\n"
"v_lshlrev_b32_sdwa v242, 1, v242 dst_sel:DWORD dst_unused:UNUSED_PAD src0_sel:DWORD src1_sel:WORD_1\n"
"v_mfma_f32_16x16x32_bf16 v[248:251], v[200:203], v[164:167], v[248:251]\n"
"ds_read_u16_d16 v242, v242 offset:18432\n"
"v_lshlrev_b32_sdwa v243, 1, v243 dst_sel:DWORD dst_unused:UNUSED_PAD src0_sel:DWORD src1_sel:WORD_1\n"
"v_mfma_f32_16x16x32_bf16 v[24:27], v[204:207], v[228:231], v[24:27]\n"
"ds_read_u16_d16_hi v243, v243 offset:18432\n"
"v_mfma_f32_16x16x32_bf16 v[28:31], v[208:211], v[228:231], v[28:31]\n"
"v_lshlrev_b32_sdwa v244, 1, v244 dst_sel:DWORD dst_unused:UNUSED_PAD src0_sel:DWORD src1_sel:WORD_1\n"
"v_mfma_f32_16x16x32_bf16 v[32:35], v[212:215], v[228:231], v[32:35]\n"
"ds_read_u16_d16 v244, v244 offset:18432\n"
"v_lshlrev_b32_sdwa v245, 1, v245 dst_sel:DWORD dst_unused:UNUSED_PAD src0_sel:DWORD src1_sel:WORD_1\n"
"v_mfma_f32_16x16x32_bf16 v[36:39], v[216:219], v[228:231], v[36:39]\n"
"ds_read_u16_d16_hi v245, v245 offset:18432\n"
"v_mfma_f32_16x16x32_bf16 v[40:43], v[220:223], v[228:231], v[40:43]\n"
"v_lshlrev_b32_sdwa v246, 1, v246 dst_sel:DWORD dst_unused:UNUSED_PAD src0_sel:DWORD src1_sel:WORD_1\n"
"v_mfma_f32_16x16x32_bf16 v[44:47], v[224:227], v[228:231], v[44:47]\n"
"ds_read_u16_d16 v246, v246 offset:18432\n"
"v_lshlrev_b32_sdwa v247, 1, v247 dst_sel:DWORD dst_unused:UNUSED_PAD src0_sel:DWORD src1_sel:WORD_1\n"
"v_mfma_f32_16x16x32_bf16 v[228:231], v[180:183], v[168:171], 0\n"
"ds_read_b64_tr_b16 v[180:181], v252 offset:43008\n"
"ds_read_b64_tr_b16 v[182:183], v252 offset:46080\n"
"s_waitcnt lgkmcnt(5)\n"
"ds_read_u16_d16_hi v247, v247 offset:18432\n"
"v_or_b32 v236, v236, v237\n"
"v_mfma_f32_16x16x32_bf16 v[228:231], v[184:187], v[172:175], v[228:231]\n"
"ds_read_b64_tr_b16 v[184:185], v252 offset:44032\n"
"ds_read_b64_tr_b16 v[186:187], v252 offset:47104\n"
"v_or_b32 v237, v238, v239\n"
"v_lshlrev_b32_sdwa v248, 1, v248 dst_sel:DWORD dst_unused:UNUSED_PAD src0_sel:DWORD src1_sel:WORD_1\n"
"v_mfma_f32_16x16x32_bf16 v[228:231], v[188:191], v[176:179], v[228:231]\n"
"ds_read_b64_tr_b16 v[188:189], v252 offset:45056\n"
"ds_read_b64_tr_b16 v[190:191], v252 offset:48128\n"
"ds_read_u16_d16 v248, v248 offset:18432\n"
"v_lshlrev_b32_sdwa v249, 1, v249 dst_sel:DWORD dst_unused:UNUSED_PAD src0_sel:DWORD src1_sel:WORD_1\n"
"v_mfma_f32_16x16x32_bf16 v[232:235], v[192:195], v[168:171], 0\n"
"ds_read_b64_tr_b16 v[192:193], v252 offset:43520\n"
"ds_read_b64_tr_b16 v[194:195], v252 offset:46592\n"
"ds_read_u16_d16_hi v249, v249 offset:18432\n"
"v_or_b32 v238, v240, v241\n"
"v_mfma_f32_16x16x32_bf16 v[232:235], v[196:199], v[172:175], v[232:235]\n"
"ds_read_b64_tr_b16 v[196:197], v252 offset:44544\n"
"ds_read_b64_tr_b16 v[198:199], v252 offset:47616\n"
"v_or_b32 v239, v242, v243\n"
"v_lshlrev_b32_sdwa v250, 1, v250 dst_sel:DWORD dst_unused:UNUSED_PAD src0_sel:DWORD src1_sel:WORD_1\n"
"v_mfma_f32_16x16x32_bf16 v[232:235], v[200:203], v[176:179], v[232:235]\n"
"ds_read_b64_tr_b16 v[200:201], v252 offset:45568\n"
"ds_read_b64_tr_b16 v[202:203], v252 offset:48640\n"
"ds_read_u16_d16 v250, v250 offset:18432\n"
"v_lshlrev_b32_sdwa v251, 1, v251 dst_sel:DWORD dst_unused:UNUSED_PAD src0_sel:DWORD src1_sel:WORD_1\n"
"v_mfma_f32_16x16x32_bf16 v[48:51], v[204:207], v[236:239], v[48:51]\n"
"ds_read_u16_d16_hi v251, v251 offset:18432\n"
"v_mfma_f32_16x16x32_bf16 v[52:55], v[208:211], v[236:239], v[52:55]\n"
"v_lshlrev_b32_sdwa v228, 1, v228 dst_sel:DWORD dst_unused:UNUSED_PAD src0_sel:DWORD src1_sel:WORD_1\n"
"v_mfma_f32_16x16x32_bf16 v[56:59], v[212:215], v[236:239], v[56:59]\n"
"ds_read_u16_d16 v228, v228 offset:18432\n"
"v_lshlrev_b32_sdwa v229, 1, v229 dst_sel:DWORD dst_unused:UNUSED_PAD src0_sel:DWORD src1_sel:WORD_1\n"
"v_mfma_f32_16x16x32_bf16 v[60:63], v[216:219], v[236:239], v[60:63]\n"
"ds_read_u16_d16_hi v229, v229 offset:18432\n"
"v_mfma_f32_16x16x32_bf16 v[64:67], v[220:223], v[236:239], v[64:67]\n"
"v_lshlrev_b32_sdwa v230, 1, v230 dst_sel:DWORD dst_unused:UNUSED_PAD src0_sel:DWORD src1_sel:WORD_1\n"
"v_mfma_f32_16x16x32_bf16 v[68:71], v[224:227], v[236:239], v[68:71]\n"
"ds_read_u16_d16 v230, v230 offset:18432\n"
"v_lshlrev_b32_sdwa v231, 1, v231 dst_sel:DWORD dst_unused:UNUSED_PAD src0_sel:DWORD src1_sel:WORD_1\n"
"s_waitcnt lgkmcnt(13)\n"
"v_mfma_f32_16x16x32_bf16 v[236:239], v[180:183], v[120:123], 0\n"
"s_waitcnt lgkmcnt(3)\n"
"ds_read_u16_d16_hi v231, v231 offset:18432\n"
"v_or_b32 v244, v244, v245\n"
"v_mfma_f32_16x16x32_bf16 v[236:239], v[184:187], v[124:127], v[236:239]\n"
"v_or_b32 v245, v246, v247\n"
"v_lshlrev_b32_sdwa v232, 1, v232 dst_sel:DWORD dst_unused:UNUSED_PAD src0_sel:DWORD src1_sel:WORD_1\n"
"v_mfma_f32_16x16x32_bf16 v[236:239], v[188:191], v[128:131], v[236:239]\n"
"ds_read_u16_d16 v232, v232 offset:18432\n"
"v_lshlrev_b32_sdwa v233, 1, v233 dst_sel:DWORD dst_unused:UNUSED_PAD src0_sel:DWORD src1_sel:WORD_1\n"
"v_mfma_f32_16x16x32_bf16 v[240:243], v[192:195], v[120:123], 0\n"
"ds_read_u16_d16_hi v233, v233 offset:18432\n"
"v_or_b32 v246, v248, v249\n"
"v_mfma_f32_16x16x32_bf16 v[240:243], v[196:199], v[124:127], v[240:243]\n"
"v_or_b32 v247, v250, v251\n"
"v_lshlrev_b32_sdwa v234, 1, v234 dst_sel:DWORD dst_unused:UNUSED_PAD src0_sel:DWORD src1_sel:WORD_1\n"
"v_mfma_f32_16x16x32_bf16 v[240:243], v[200:203], v[128:131], v[240:243]\n"
"ds_read_u16_d16 v234, v234 offset:18432\n"
"v_lshlrev_b32_sdwa v235, 1, v235 dst_sel:DWORD dst_unused:UNUSED_PAD src0_sel:DWORD src1_sel:WORD_1\n"
"v_mfma_f32_16x16x32_bf16 v[72:75], v[204:207], v[244:247], v[72:75]\n"
"ds_read_u16_d16_hi v235, v235 offset:18432\n"
"v_mfma_f32_16x16x32_bf16 v[76:79], v[208:211], v[244:247], v[76:79]\n"
"v_lshlrev_b32_sdwa v236, 1, v236 dst_sel:DWORD dst_unused:UNUSED_PAD src0_sel:DWORD src1_sel:WORD_1\n"
"v_mfma_f32_16x16x32_bf16 v[80:83], v[212:215], v[244:247], v[80:83]\n"
"ds_read_u16_d16 v236, v236 offset:18432\n"
"v_lshlrev_b32_sdwa v237, 1, v237 dst_sel:DWORD dst_unused:UNUSED_PAD src0_sel:DWORD src1_sel:WORD_1\n"
"v_mfma_f32_16x16x32_bf16 v[84:87], v[216:219], v[244:247], v[84:87]\n"
"ds_read_u16_d16_hi v237, v237 offset:18432\n"
"v_mfma_f32_16x16x32_bf16 v[88:91], v[220:223], v[244:247], v[88:91]\n"
"v_lshlrev_b32_sdwa v238, 1, v238 dst_sel:DWORD dst_unused:UNUSED_PAD src0_sel:DWORD src1_sel:WORD_1\n"
"v_mfma_f32_16x16x32_bf16 v[92:95], v[224:227], v[244:247], v[92:95]\n"
"ds_read_u16_d16 v238, v238 offset:18432\n"
"v_lshlrev_b32_sdwa v239, 1, v239 dst_sel:DWORD dst_unused:UNUSED_PAD src0_sel:DWORD src1_sel:WORD_1\n"
"v_mfma_f32_16x16x32_bf16 v[244:247], v[180:183], v[132:135], 0\n"
"s_waitcnt lgkmcnt(3)\n"
"ds_read_u16_d16_hi v239, v239 offset:18432\n"
"v_or_b32 v228, v228, v229\n"
"v_mfma_f32_16x16x32_bf16 v[244:247], v[184:187], v[136:139], v[244:247]\n"
"v_or_b32 v229, v230, v231\n"
"v_lshlrev_b32_sdwa v240, 1, v240 dst_sel:DWORD dst_unused:UNUSED_PAD src0_sel:DWORD src1_sel:WORD_1\n"
"v_mfma_f32_16x16x32_bf16 v[244:247], v[188:191], v[140:143], v[244:247]\n"
"ds_read_u16_d16 v240, v240 offset:18432\n"
"v_lshlrev_b32_sdwa v241, 1, v241 dst_sel:DWORD dst_unused:UNUSED_PAD src0_sel:DWORD src1_sel:WORD_1\n"
"v_mfma_f32_16x16x32_bf16 v[248:251], v[192:195], v[132:135], 0\n"
"ds_read_u16_d16_hi v241, v241 offset:18432\n"
"v_or_b32 v230, v232, v233\n"
"v_mfma_f32_16x16x32_bf16 v[248:251], v[196:199], v[136:139], v[248:251]\n"
"v_or_b32 v231, v234, v235\n"
"v_lshlrev_b32_sdwa v242, 1, v242 dst_sel:DWORD dst_unused:UNUSED_PAD src0_sel:DWORD src1_sel:WORD_1\n"
"v_mfma_f32_16x16x32_bf16 v[248:251], v[200:203], v[140:143], v[248:251]\n"
"ds_read_u16_d16 v242, v242 offset:18432\n"
"v_lshlrev_b32_sdwa v243, 1, v243 dst_sel:DWORD dst_unused:UNUSED_PAD src0_sel:DWORD src1_sel:WORD_1\n"
"v_mfma_f32_16x16x32_bf16 v[96:99], v[204:207], v[228:231], v[96:99]\n"
"ds_read_b64_tr_b16 v[204:205], v253 offset:43120\n"
"ds_read_b64_tr_b16 v[206:207], v253 offset:44656\n"
"ds_read_u16_d16_hi v243, v243 offset:18432\n"
"v_mfma_f32_16x16x32_bf16 v[100:103], v[208:211], v[228:231], v[100:103]\n"
"ds_read_b64_tr_b16 v[208:209], v253 offset:43128\n"
"ds_read_b64_tr_b16 v[210:211], v253 offset:44664\n"
"v_lshlrev_b32_sdwa v244, 1, v244 dst_sel:DWORD dst_unused:UNUSED_PAD src0_sel:DWORD src1_sel:WORD_1\n"
"v_mfma_f32_16x16x32_bf16 v[104:107], v[212:215], v[228:231], v[104:107]\n"
"ds_read_b64_tr_b16 v[212:213], v253 offset:43184\n"
"ds_read_b64_tr_b16 v[214:215], v253 offset:44720\n"
"ds_read_u16_d16 v244, v244 offset:18432\n"
"v_lshlrev_b32_sdwa v245, 1, v245 dst_sel:DWORD dst_unused:UNUSED_PAD src0_sel:DWORD src1_sel:WORD_1\n"
"v_mfma_f32_16x16x32_bf16 v[108:111], v[216:219], v[228:231], v[108:111]\n"
"ds_read_b64_tr_b16 v[216:217], v253 offset:43192\n"
"ds_read_b64_tr_b16 v[218:219], v253 offset:44728\n"
"ds_read_u16_d16_hi v245, v245 offset:18432\n"
"v_mfma_f32_16x16x32_bf16 v[112:115], v[220:223], v[228:231], v[112:115]\n"
"ds_read_b64_tr_b16 v[220:221], v253 offset:43248\n"
"ds_read_b64_tr_b16 v[222:223], v253 offset:44784\n"
"v_lshlrev_b32_sdwa v246, 1, v246 dst_sel:DWORD dst_unused:UNUSED_PAD src0_sel:DWORD src1_sel:WORD_1\n"
"v_mfma_f32_16x16x32_bf16 v[116:119], v[224:227], v[228:231], v[116:119]\n"
"ds_read_b64_tr_b16 v[224:225], v253 offset:43256\n"
"ds_read_b64_tr_b16 v[226:227], v253 offset:44792\n"
"ds_read_u16_d16 v246, v246 offset:18432\n"
"v_lshlrev_b32_sdwa v247, 1, v247 dst_sel:DWORD dst_unused:UNUSED_PAD src0_sel:DWORD src1_sel:WORD_1\n"
"v_mfma_f32_16x16x32_bf16 v[228:231], v[180:183], v[144:147], 0\n"
"s_waitcnt lgkmcnt(13)\n"
"ds_read_u16_d16_hi v247, v247 offset:18432\n"
"v_or_b32 v236, v236, v237\n"
"v_mfma_f32_16x16x32_bf16 v[228:231], v[184:187], v[148:151], v[228:231]\n"
"v_or_b32 v237, v238, v239\n"
"v_lshlrev_b32_sdwa v248, 1, v248 dst_sel:DWORD dst_unused:UNUSED_PAD src0_sel:DWORD src1_sel:WORD_1\n"
"v_mfma_f32_16x16x32_bf16 v[228:231], v[188:191], v[152:155], v[228:231]\n"
"ds_read_u16_d16 v248, v248 offset:18432\n"
"v_lshlrev_b32_sdwa v249, 1, v249 dst_sel:DWORD dst_unused:UNUSED_PAD src0_sel:DWORD src1_sel:WORD_1\n"
"v_mfma_f32_16x16x32_bf16 v[232:235], v[192:195], v[144:147], 0\n"
"ds_read_u16_d16_hi v249, v249 offset:18432\n"
"v_or_b32 v238, v240, v241\n"
"v_mfma_f32_16x16x32_bf16 v[232:235], v[196:199], v[148:151], v[232:235]\n"
"v_or_b32 v239, v242, v243\n"
"v_lshlrev_b32_sdwa v250, 1, v250 dst_sel:DWORD dst_unused:UNUSED_PAD src0_sel:DWORD src1_sel:WORD_1\n"
"v_mfma_f32_16x16x32_bf16 v[232:235], v[200:203], v[152:155], v[232:235]\n"
"ds_read_u16_d16 v250, v250 offset:18432\n"
"v_lshlrev_b32_sdwa v251, 1, v251 dst_sel:DWORD dst_unused:UNUSED_PAD src0_sel:DWORD src1_sel:WORD_1\n"
"s_waitcnt lgkmcnt(13)\n"
"v_mfma_f32_16x16x32_bf16 v[0:3], v[204:207], v[236:239], v[0:3]\n"
"ds_read_u16_d16_hi v251, v251 offset:18432\n"
"v_mfma_f32_16x16x32_bf16 v[4:7], v[208:211], v[236:239], v[4:7]\n"
"v_lshlrev_b32_sdwa v228, 1, v228 dst_sel:DWORD dst_unused:UNUSED_PAD src0_sel:DWORD src1_sel:WORD_1\n"
"v_mfma_f32_16x16x32_bf16 v[8:11], v[212:215], v[236:239], v[8:11]\n"
"ds_read_u16_d16 v228, v228 offset:18432\n"
"v_lshlrev_b32_sdwa v229, 1, v229 dst_sel:DWORD dst_unused:UNUSED_PAD src0_sel:DWORD src1_sel:WORD_1\n"
"s_waitcnt lgkmcnt(7)\n"
"v_mfma_f32_16x16x32_bf16 v[12:15], v[216:219], v[236:239], v[12:15]\n"
"ds_read_u16_d16_hi v229, v229 offset:18432\n"
"v_mfma_f32_16x16x32_bf16 v[16:19], v[220:223], v[236:239], v[16:19]\n"
"v_lshlrev_b32_sdwa v230, 1, v230 dst_sel:DWORD dst_unused:UNUSED_PAD src0_sel:DWORD src1_sel:WORD_1\n"
"v_mfma_f32_16x16x32_bf16 v[20:23], v[224:227], v[236:239], v[20:23]\n"
"ds_read_u16_d16 v230, v230 offset:18432\n"
"v_lshlrev_b32_sdwa v231, 1, v231 dst_sel:DWORD dst_unused:UNUSED_PAD src0_sel:DWORD src1_sel:WORD_1\n"
"v_mfma_f32_16x16x32_bf16 v[236:239], v[180:183], v[156:159], 0\n"
"s_waitcnt lgkmcnt(3)\n"
"ds_read_u16_d16_hi v231, v231 offset:18432\n"
"v_or_b32 v244, v244, v245\n"
"v_mfma_f32_16x16x32_bf16 v[236:239], v[184:187], v[160:163], v[236:239]\n"
"v_or_b32 v245, v246, v247\n"
"v_lshlrev_b32_sdwa v232, 1, v232 dst_sel:DWORD dst_unused:UNUSED_PAD src0_sel:DWORD src1_sel:WORD_1\n"
"v_mfma_f32_16x16x32_bf16 v[236:239], v[188:191], v[164:167], v[236:239]\n"
"ds_read_u16_d16 v232, v232 offset:18432\n"
"v_lshlrev_b32_sdwa v233, 1, v233 dst_sel:DWORD dst_unused:UNUSED_PAD src0_sel:DWORD src1_sel:WORD_1\n"
"v_mfma_f32_16x16x32_bf16 v[240:243], v[192:195], v[156:159], 0\n"
"ds_read_u16_d16_hi v233, v233 offset:18432\n"
"v_or_b32 v246, v248, v249\n"
"v_mfma_f32_16x16x32_bf16 v[240:243], v[196:199], v[160:163], v[240:243]\n"
"v_or_b32 v247, v250, v251\n"
"v_lshlrev_b32_sdwa v234, 1, v234 dst_sel:DWORD dst_unused:UNUSED_PAD src0_sel:DWORD src1_sel:WORD_1\n"
"v_mfma_f32_16x16x32_bf16 v[240:243], v[200:203], v[164:167], v[240:243]\n"
"ds_read_u16_d16 v234, v234 offset:18432\n"
"v_lshlrev_b32_sdwa v235, 1, v235 dst_sel:DWORD dst_unused:UNUSED_PAD src0_sel:DWORD src1_sel:WORD_1\n"
"v_mfma_f32_16x16x32_bf16 v[24:27], v[204:207], v[244:247], v[24:27]\n"
"ds_read_u16_d16_hi v235, v235 offset:18432\n"
"v_mfma_f32_16x16x32_bf16 v[28:31], v[208:211], v[244:247], v[28:31]\n"
"v_lshlrev_b32_sdwa v236, 1, v236 dst_sel:DWORD dst_unused:UNUSED_PAD src0_sel:DWORD src1_sel:WORD_1\n"
"v_mfma_f32_16x16x32_bf16 v[32:35], v[212:215], v[244:247], v[32:35]\n"
"ds_read_u16_d16 v236, v236 offset:18432\n"
"v_lshlrev_b32_sdwa v237, 1, v237 dst_sel:DWORD dst_unused:UNUSED_PAD src0_sel:DWORD src1_sel:WORD_1\n"
"v_mfma_f32_16x16x32_bf16 v[36:39], v[216:219], v[244:247], v[36:39]\n"
"ds_read_u16_d16_hi v237, v237 offset:18432\n"
"v_mfma_f32_16x16x32_bf16 v[40:43], v[220:223], v[244:247], v[40:43]\n"
"v_lshlrev_b32_sdwa v238, 1, v238 dst_sel:DWORD dst_unused:UNUSED_PAD src0_sel:DWORD src1_sel:WORD_1\n"
"v_mfma_f32_16x16x32_bf16 v[44:47], v[224:227], v[244:247], v[44:47]\n"
"ds_read_u16_d16 v238, v238 offset:18432\n"
"v_lshlrev_b32_sdwa v239, 1, v239 dst_sel:DWORD dst_unused:UNUSED_PAD src0_sel:DWORD src1_sel:WORD_1\n"
"v_mfma_f32_16x16x32_bf16 v[244:247], v[180:183], v[168:171], 0\n"
"ds_read_b64_tr_b16 v[180:181], v252 offset:49152\n"
"ds_read_b64_tr_b16 v[182:183], v252 offset:52224\n"
"s_waitcnt lgkmcnt(5)\n"
"ds_read_u16_d16_hi v239, v239 offset:18432\n"
"v_or_b32 v228, v228, v229\n"
"v_mfma_f32_16x16x32_bf16 v[244:247], v[184:187], v[172:175], v[244:247]\n"
"ds_read_b64_tr_b16 v[184:185], v252 offset:50176\n"
"ds_read_b64_tr_b16 v[186:187], v252 offset:53248\n"
"v_or_b32 v229, v230, v231\n"
"v_lshlrev_b32_sdwa v240, 1, v240 dst_sel:DWORD dst_unused:UNUSED_PAD src0_sel:DWORD src1_sel:WORD_1\n"
"v_mfma_f32_16x16x32_bf16 v[244:247], v[188:191], v[176:179], v[244:247]\n"
"ds_read_b64_tr_b16 v[188:189], v252 offset:51200\n"
"ds_read_b64_tr_b16 v[190:191], v252 offset:54272\n"
"ds_read_u16_d16 v240, v240 offset:18432\n"
"v_lshlrev_b32_sdwa v241, 1, v241 dst_sel:DWORD dst_unused:UNUSED_PAD src0_sel:DWORD src1_sel:WORD_1\n"
"v_mfma_f32_16x16x32_bf16 v[248:251], v[192:195], v[168:171], 0\n"
"ds_read_b64_tr_b16 v[192:193], v252 offset:49664\n"
"ds_read_b64_tr_b16 v[194:195], v252 offset:52736\n"
"ds_read_u16_d16_hi v241, v241 offset:18432\n"
"v_or_b32 v230, v232, v233\n"
"v_mfma_f32_16x16x32_bf16 v[248:251], v[196:199], v[172:175], v[248:251]\n"
"ds_read_b64_tr_b16 v[196:197], v252 offset:50688\n"
"ds_read_b64_tr_b16 v[198:199], v252 offset:53760\n"
"v_or_b32 v231, v234, v235\n"
"v_lshlrev_b32_sdwa v242, 1, v242 dst_sel:DWORD dst_unused:UNUSED_PAD src0_sel:DWORD src1_sel:WORD_1\n"
"v_mfma_f32_16x16x32_bf16 v[248:251], v[200:203], v[176:179], v[248:251]\n"
"ds_read_b64_tr_b16 v[200:201], v252 offset:51712\n"
"ds_read_b64_tr_b16 v[202:203], v252 offset:54784\n"
"ds_read_u16_d16 v242, v242 offset:18432\n"
"v_lshlrev_b32_sdwa v243, 1, v243 dst_sel:DWORD dst_unused:UNUSED_PAD src0_sel:DWORD src1_sel:WORD_1\n"
"v_mfma_f32_16x16x32_bf16 v[48:51], v[204:207], v[228:231], v[48:51]\n"
"ds_read_u16_d16_hi v243, v243 offset:18432\n"
"v_mfma_f32_16x16x32_bf16 v[52:55], v[208:211], v[228:231], v[52:55]\n"
"v_lshlrev_b32_sdwa v244, 1, v244 dst_sel:DWORD dst_unused:UNUSED_PAD src0_sel:DWORD src1_sel:WORD_1\n"
"v_mfma_f32_16x16x32_bf16 v[56:59], v[212:215], v[228:231], v[56:59]\n"
"ds_read_u16_d16 v244, v244 offset:18432\n"
"v_lshlrev_b32_sdwa v245, 1, v245 dst_sel:DWORD dst_unused:UNUSED_PAD src0_sel:DWORD src1_sel:WORD_1\n"
"v_mfma_f32_16x16x32_bf16 v[60:63], v[216:219], v[228:231], v[60:63]\n"
"ds_read_u16_d16_hi v245, v245 offset:18432\n"
"v_mfma_f32_16x16x32_bf16 v[64:67], v[220:223], v[228:231], v[64:67]\n"
"v_lshlrev_b32_sdwa v246, 1, v246 dst_sel:DWORD dst_unused:UNUSED_PAD src0_sel:DWORD src1_sel:WORD_1\n"
"v_mfma_f32_16x16x32_bf16 v[68:71], v[224:227], v[228:231], v[68:71]\n"
"ds_read_u16_d16 v246, v246 offset:18432\n"
"v_lshlrev_b32_sdwa v247, 1, v247 dst_sel:DWORD dst_unused:UNUSED_PAD src0_sel:DWORD src1_sel:WORD_1\n"
"s_waitcnt lgkmcnt(13)\n"
"v_mfma_f32_16x16x32_bf16 v[228:231], v[180:183], v[120:123], 0\n"
"s_waitcnt lgkmcnt(3)\n"
"ds_read_u16_d16_hi v247, v247 offset:18432\n"
"v_or_b32 v236, v236, v237\n"
"v_mfma_f32_16x16x32_bf16 v[228:231], v[184:187], v[124:127], v[228:231]\n"
"v_or_b32 v237, v238, v239\n"
"v_lshlrev_b32_sdwa v248, 1, v248 dst_sel:DWORD dst_unused:UNUSED_PAD src0_sel:DWORD src1_sel:WORD_1\n"
"v_mfma_f32_16x16x32_bf16 v[228:231], v[188:191], v[128:131], v[228:231]\n"
"ds_read_u16_d16 v248, v248 offset:18432\n"
"v_lshlrev_b32_sdwa v249, 1, v249 dst_sel:DWORD dst_unused:UNUSED_PAD src0_sel:DWORD src1_sel:WORD_1\n"
"v_mfma_f32_16x16x32_bf16 v[232:235], v[192:195], v[120:123], 0\n"
"ds_read_u16_d16_hi v249, v249 offset:18432\n"
"v_or_b32 v238, v240, v241\n"
"v_mfma_f32_16x16x32_bf16 v[232:235], v[196:199], v[124:127], v[232:235]\n"
"v_or_b32 v239, v242, v243\n"
"v_lshlrev_b32_sdwa v250, 1, v250 dst_sel:DWORD dst_unused:UNUSED_PAD src0_sel:DWORD src1_sel:WORD_1\n"
"v_mfma_f32_16x16x32_bf16 v[232:235], v[200:203], v[128:131], v[232:235]\n"
"ds_read_u16_d16 v250, v250 offset:18432\n"
"v_lshlrev_b32_sdwa v251, 1, v251 dst_sel:DWORD dst_unused:UNUSED_PAD src0_sel:DWORD src1_sel:WORD_1\n"
"v_mfma_f32_16x16x32_bf16 v[72:75], v[204:207], v[236:239], v[72:75]\n"
"ds_read_u16_d16_hi v251, v251 offset:18432\n"
"v_mfma_f32_16x16x32_bf16 v[76:79], v[208:211], v[236:239], v[76:79]\n"
"v_lshlrev_b32_sdwa v228, 1, v228 dst_sel:DWORD dst_unused:UNUSED_PAD src0_sel:DWORD src1_sel:WORD_1\n"
"v_mfma_f32_16x16x32_bf16 v[80:83], v[212:215], v[236:239], v[80:83]\n"
"ds_read_u16_d16 v228, v228 offset:18432\n"
"v_lshlrev_b32_sdwa v229, 1, v229 dst_sel:DWORD dst_unused:UNUSED_PAD src0_sel:DWORD src1_sel:WORD_1\n"
"v_mfma_f32_16x16x32_bf16 v[84:87], v[216:219], v[236:239], v[84:87]\n"
"ds_read_u16_d16_hi v229, v229 offset:18432\n"
"v_mfma_f32_16x16x32_bf16 v[88:91], v[220:223], v[236:239], v[88:91]\n"
"v_lshlrev_b32_sdwa v230, 1, v230 dst_sel:DWORD dst_unused:UNUSED_PAD src0_sel:DWORD src1_sel:WORD_1\n"
"v_mfma_f32_16x16x32_bf16 v[92:95], v[224:227], v[236:239], v[92:95]\n"
"ds_read_u16_d16 v230, v230 offset:18432\n"
"v_lshlrev_b32_sdwa v231, 1, v231 dst_sel:DWORD dst_unused:UNUSED_PAD src0_sel:DWORD src1_sel:WORD_1\n"
"v_mfma_f32_16x16x32_bf16 v[236:239], v[180:183], v[132:135], 0\n"
"s_waitcnt lgkmcnt(3)\n"
"ds_read_u16_d16_hi v231, v231 offset:18432\n"
"v_or_b32 v244, v244, v245\n"
"v_mfma_f32_16x16x32_bf16 v[236:239], v[184:187], v[136:139], v[236:239]\n"
"v_or_b32 v245, v246, v247\n"
"v_lshlrev_b32_sdwa v232, 1, v232 dst_sel:DWORD dst_unused:UNUSED_PAD src0_sel:DWORD src1_sel:WORD_1\n"
"v_mfma_f32_16x16x32_bf16 v[236:239], v[188:191], v[140:143], v[236:239]\n"
"ds_read_u16_d16 v232, v232 offset:18432\n"
"v_lshlrev_b32_sdwa v233, 1, v233 dst_sel:DWORD dst_unused:UNUSED_PAD src0_sel:DWORD src1_sel:WORD_1\n"
"v_mfma_f32_16x16x32_bf16 v[240:243], v[192:195], v[132:135], 0\n"
"ds_read_u16_d16_hi v233, v233 offset:18432\n"
"v_or_b32 v246, v248, v249\n"
"v_mfma_f32_16x16x32_bf16 v[240:243], v[196:199], v[136:139], v[240:243]\n"
"v_or_b32 v247, v250, v251\n"
"v_lshlrev_b32_sdwa v234, 1, v234 dst_sel:DWORD dst_unused:UNUSED_PAD src0_sel:DWORD src1_sel:WORD_1\n"
"v_mfma_f32_16x16x32_bf16 v[240:243], v[200:203], v[140:143], v[240:243]\n"
"ds_read_u16_d16 v234, v234 offset:18432\n"
"v_lshlrev_b32_sdwa v235, 1, v235 dst_sel:DWORD dst_unused:UNUSED_PAD src0_sel:DWORD src1_sel:WORD_1\n"
"v_mfma_f32_16x16x32_bf16 v[96:99], v[204:207], v[244:247], v[96:99]\n"
"ds_read_b64_tr_b16 v[204:205], v253 offset:49280\n"
"ds_read_b64_tr_b16 v[206:207], v253 offset:50816\n"
"ds_read_u16_d16_hi v235, v235 offset:18432\n"
"v_mfma_f32_16x16x32_bf16 v[100:103], v[208:211], v[244:247], v[100:103]\n"
"ds_read_b64_tr_b16 v[208:209], v253 offset:49288\n"
"ds_read_b64_tr_b16 v[210:211], v253 offset:50824\n"
"v_lshlrev_b32_sdwa v236, 1, v236 dst_sel:DWORD dst_unused:UNUSED_PAD src0_sel:DWORD src1_sel:WORD_1\n"
"v_mfma_f32_16x16x32_bf16 v[104:107], v[212:215], v[244:247], v[104:107]\n"
"ds_read_b64_tr_b16 v[212:213], v253 offset:49344\n"
"ds_read_b64_tr_b16 v[214:215], v253 offset:50880\n"
"ds_read_u16_d16 v236, v236 offset:18432\n"
"v_lshlrev_b32_sdwa v237, 1, v237 dst_sel:DWORD dst_unused:UNUSED_PAD src0_sel:DWORD src1_sel:WORD_1\n"
"v_mfma_f32_16x16x32_bf16 v[108:111], v[216:219], v[244:247], v[108:111]\n"
"ds_read_b64_tr_b16 v[216:217], v253 offset:49352\n"
"ds_read_b64_tr_b16 v[218:219], v253 offset:50888\n"
"ds_read_u16_d16_hi v237, v237 offset:18432\n"
"v_mfma_f32_16x16x32_bf16 v[112:115], v[220:223], v[244:247], v[112:115]\n"
"ds_read_b64_tr_b16 v[220:221], v253 offset:49408\n"
"ds_read_b64_tr_b16 v[222:223], v253 offset:50944\n"
"v_lshlrev_b32_sdwa v238, 1, v238 dst_sel:DWORD dst_unused:UNUSED_PAD src0_sel:DWORD src1_sel:WORD_1\n"
"v_mfma_f32_16x16x32_bf16 v[116:119], v[224:227], v[244:247], v[116:119]\n"
"ds_read_b64_tr_b16 v[224:225], v253 offset:49416\n"
"ds_read_b64_tr_b16 v[226:227], v253 offset:50952\n"
"ds_read_u16_d16 v238, v238 offset:18432\n"
"v_lshlrev_b32_sdwa v239, 1, v239 dst_sel:DWORD dst_unused:UNUSED_PAD src0_sel:DWORD src1_sel:WORD_1\n"
"v_mfma_f32_16x16x32_bf16 v[244:247], v[180:183], v[144:147], 0\n"
"s_waitcnt lgkmcnt(13)\n"
"ds_read_u16_d16_hi v239, v239 offset:18432\n"
"v_or_b32 v228, v228, v229\n"
"v_mfma_f32_16x16x32_bf16 v[244:247], v[184:187], v[148:151], v[244:247]\n"
"v_or_b32 v229, v230, v231\n"
"v_lshlrev_b32_sdwa v240, 1, v240 dst_sel:DWORD dst_unused:UNUSED_PAD src0_sel:DWORD src1_sel:WORD_1\n"
"v_mfma_f32_16x16x32_bf16 v[244:247], v[188:191], v[152:155], v[244:247]\n"
"ds_read_u16_d16 v240, v240 offset:18432\n"
"v_lshlrev_b32_sdwa v241, 1, v241 dst_sel:DWORD dst_unused:UNUSED_PAD src0_sel:DWORD src1_sel:WORD_1\n"
"v_mfma_f32_16x16x32_bf16 v[248:251], v[192:195], v[144:147], 0\n"
"ds_read_u16_d16_hi v241, v241 offset:18432\n"
"v_or_b32 v230, v232, v233\n"
"v_mfma_f32_16x16x32_bf16 v[248:251], v[196:199], v[148:151], v[248:251]\n"
"v_or_b32 v231, v234, v235\n"
"v_lshlrev_b32_sdwa v242, 1, v242 dst_sel:DWORD dst_unused:UNUSED_PAD src0_sel:DWORD src1_sel:WORD_1\n"
"v_mfma_f32_16x16x32_bf16 v[248:251], v[200:203], v[152:155], v[248:251]\n"
"ds_read_u16_d16 v242, v242 offset:18432\n"
"v_lshlrev_b32_sdwa v243, 1, v243 dst_sel:DWORD dst_unused:UNUSED_PAD src0_sel:DWORD src1_sel:WORD_1\n"
"s_waitcnt lgkmcnt(13)\n"
"v_mfma_f32_16x16x32_bf16 v[0:3], v[204:207], v[228:231], v[0:3]\n"
"ds_read_u16_d16_hi v243, v243 offset:18432\n"
"v_mfma_f32_16x16x32_bf16 v[4:7], v[208:211], v[228:231], v[4:7]\n"
"v_lshlrev_b32_sdwa v244, 1, v244 dst_sel:DWORD dst_unused:UNUSED_PAD src0_sel:DWORD src1_sel:WORD_1\n"
"v_mfma_f32_16x16x32_bf16 v[8:11], v[212:215], v[228:231], v[8:11]\n"
"ds_read_u16_d16 v244, v244 offset:18432\n"
"v_lshlrev_b32_sdwa v245, 1, v245 dst_sel:DWORD dst_unused:UNUSED_PAD src0_sel:DWORD src1_sel:WORD_1\n"
"s_waitcnt lgkmcnt(7)\n"
"v_mfma_f32_16x16x32_bf16 v[12:15], v[216:219], v[228:231], v[12:15]\n"
"ds_read_u16_d16_hi v245, v245 offset:18432\n"
"v_mfma_f32_16x16x32_bf16 v[16:19], v[220:223], v[228:231], v[16:19]\n"
"v_lshlrev_b32_sdwa v246, 1, v246 dst_sel:DWORD dst_unused:UNUSED_PAD src0_sel:DWORD src1_sel:WORD_1\n"
"v_mfma_f32_16x16x32_bf16 v[20:23], v[224:227], v[228:231], v[20:23]\n"
"ds_read_u16_d16 v246, v246 offset:18432\n"
"v_lshlrev_b32_sdwa v247, 1, v247 dst_sel:DWORD dst_unused:UNUSED_PAD src0_sel:DWORD src1_sel:WORD_1\n"
"v_mfma_f32_16x16x32_bf16 v[228:231], v[180:183], v[156:159], 0\n"
"s_waitcnt lgkmcnt(3)\n"
"ds_read_u16_d16_hi v247, v247 offset:18432\n"
"v_or_b32 v236, v236, v237\n"
"v_mfma_f32_16x16x32_bf16 v[228:231], v[184:187], v[160:163], v[228:231]\n"
"v_or_b32 v237, v238, v239\n"
"v_lshlrev_b32_sdwa v248, 1, v248 dst_sel:DWORD dst_unused:UNUSED_PAD src0_sel:DWORD src1_sel:WORD_1\n"
"v_mfma_f32_16x16x32_bf16 v[228:231], v[188:191], v[164:167], v[228:231]\n"
"ds_read_u16_d16 v248, v248 offset:18432\n"
"v_lshlrev_b32_sdwa v249, 1, v249 dst_sel:DWORD dst_unused:UNUSED_PAD src0_sel:DWORD src1_sel:WORD_1\n"
"v_mfma_f32_16x16x32_bf16 v[232:235], v[192:195], v[156:159], 0\n"
"ds_read_u16_d16_hi v249, v249 offset:18432\n"
"v_or_b32 v238, v240, v241\n"
"v_mfma_f32_16x16x32_bf16 v[232:235], v[196:199], v[160:163], v[232:235]\n"
"v_or_b32 v239, v242, v243\n"
"v_lshlrev_b32_sdwa v250, 1, v250 dst_sel:DWORD dst_unused:UNUSED_PAD src0_sel:DWORD src1_sel:WORD_1\n"
"v_mfma_f32_16x16x32_bf16 v[232:235], v[200:203], v[164:167], v[232:235]\n"
"ds_read_u16_d16 v250, v250 offset:18432\n"
"v_lshlrev_b32_sdwa v251, 1, v251 dst_sel:DWORD dst_unused:UNUSED_PAD src0_sel:DWORD src1_sel:WORD_1\n"
"v_mfma_f32_16x16x32_bf16 v[24:27], v[204:207], v[236:239], v[24:27]\n"
"ds_read_u16_d16_hi v251, v251 offset:18432\n"
"v_mfma_f32_16x16x32_bf16 v[28:31], v[208:211], v[236:239], v[28:31]\n"
"v_lshlrev_b32_sdwa v228, 1, v228 dst_sel:DWORD dst_unused:UNUSED_PAD src0_sel:DWORD src1_sel:WORD_1\n"
"v_mfma_f32_16x16x32_bf16 v[32:35], v[212:215], v[236:239], v[32:35]\n"
"ds_read_u16_d16 v228, v228 offset:18432\n"
"v_lshlrev_b32_sdwa v229, 1, v229 dst_sel:DWORD dst_unused:UNUSED_PAD src0_sel:DWORD src1_sel:WORD_1\n"
"v_mfma_f32_16x16x32_bf16 v[36:39], v[216:219], v[236:239], v[36:39]\n"
"ds_read_u16_d16_hi v229, v229 offset:18432\n"
"v_mfma_f32_16x16x32_bf16 v[40:43], v[220:223], v[236:239], v[40:43]\n"
"v_lshlrev_b32_sdwa v230, 1, v230 dst_sel:DWORD dst_unused:UNUSED_PAD src0_sel:DWORD src1_sel:WORD_1\n"
"v_mfma_f32_16x16x32_bf16 v[44:47], v[224:227], v[236:239], v[44:47]\n"
"ds_read_u16_d16 v230, v230 offset:18432\n"
"v_lshlrev_b32_sdwa v231, 1, v231 dst_sel:DWORD dst_unused:UNUSED_PAD src0_sel:DWORD src1_sel:WORD_1\n"
"v_mfma_f32_16x16x32_bf16 v[236:239], v[180:183], v[168:171], 0\n"
"v_add_u32 v252, 0xffffa100, v252\n"
"ds_read_b64_tr_b16 v[180:181], v252 offset:0\n"
"ds_read_b64_tr_b16 v[182:183], v252 offset:3072\n"
"s_waitcnt lgkmcnt(5)\n"
"ds_read_u16_d16_hi v231, v231 offset:18432\n"
"v_or_b32 v244, v244, v245\n"
"v_mfma_f32_16x16x32_bf16 v[236:239], v[184:187], v[172:175], v[236:239]\n"
"ds_read_b64_tr_b16 v[184:185], v252 offset:1024\n"
"ds_read_b64_tr_b16 v[186:187], v252 offset:4096\n"
"v_or_b32 v245, v246, v247\n"
"v_lshlrev_b32_sdwa v232, 1, v232 dst_sel:DWORD dst_unused:UNUSED_PAD src0_sel:DWORD src1_sel:WORD_1\n"
"v_mfma_f32_16x16x32_bf16 v[236:239], v[188:191], v[176:179], v[236:239]\n"
"ds_read_b64_tr_b16 v[188:189], v252 offset:2048\n"
"ds_read_b64_tr_b16 v[190:191], v252 offset:5120\n"
"ds_read_u16_d16 v232, v232 offset:18432\n"
"v_lshlrev_b32_sdwa v233, 1, v233 dst_sel:DWORD dst_unused:UNUSED_PAD src0_sel:DWORD src1_sel:WORD_1\n"
"v_mfma_f32_16x16x32_bf16 v[240:243], v[192:195], v[168:171], 0\n"
"ds_read_b64_tr_b16 v[192:193], v252 offset:512\n"
"ds_read_b64_tr_b16 v[194:195], v252 offset:3584\n"
"ds_read_u16_d16_hi v233, v233 offset:18432\n"
"v_or_b32 v246, v248, v249\n"
"v_mfma_f32_16x16x32_bf16 v[240:243], v[196:199], v[172:175], v[240:243]\n"
"ds_read_b64_tr_b16 v[196:197], v252 offset:1536\n"
"ds_read_b64_tr_b16 v[198:199], v252 offset:4608\n"
"v_or_b32 v247, v250, v251\n"
"v_lshlrev_b32_sdwa v234, 1, v234 dst_sel:DWORD dst_unused:UNUSED_PAD src0_sel:DWORD src1_sel:WORD_1\n"
"v_mfma_f32_16x16x32_bf16 v[240:243], v[200:203], v[176:179], v[240:243]\n"
"ds_read_b64_tr_b16 v[200:201], v252 offset:2560\n"
"ds_read_b64_tr_b16 v[202:203], v252 offset:5632\n"
"ds_read_u16_d16 v234, v234 offset:18432\n"
"v_lshlrev_b32_sdwa v235, 1, v235 dst_sel:DWORD dst_unused:UNUSED_PAD src0_sel:DWORD src1_sel:WORD_1\n"
"v_mfma_f32_16x16x32_bf16 v[48:51], v[204:207], v[244:247], v[48:51]\n"
"ds_read_u16_d16_hi v235, v235 offset:18432\n"
"v_mfma_f32_16x16x32_bf16 v[52:55], v[208:211], v[244:247], v[52:55]\n"
"v_lshlrev_b32_sdwa v236, 1, v236 dst_sel:DWORD dst_unused:UNUSED_PAD src0_sel:DWORD src1_sel:WORD_1\n"
"v_mfma_f32_16x16x32_bf16 v[56:59], v[212:215], v[244:247], v[56:59]\n"
"ds_read_u16_d16 v236, v236 offset:18432\n"
"v_lshlrev_b32_sdwa v237, 1, v237 dst_sel:DWORD dst_unused:UNUSED_PAD src0_sel:DWORD src1_sel:WORD_1\n"
"v_mfma_f32_16x16x32_bf16 v[60:63], v[216:219], v[244:247], v[60:63]\n"
"ds_read_u16_d16_hi v237, v237 offset:18432\n"
"v_mfma_f32_16x16x32_bf16 v[64:67], v[220:223], v[244:247], v[64:67]\n"
"v_lshlrev_b32_sdwa v238, 1, v238 dst_sel:DWORD dst_unused:UNUSED_PAD src0_sel:DWORD src1_sel:WORD_1\n"
"v_mfma_f32_16x16x32_bf16 v[68:71], v[224:227], v[244:247], v[68:71]\n"
"ds_read_u16_d16 v238, v238 offset:18432\n"
"v_lshlrev_b32_sdwa v239, 1, v239 dst_sel:DWORD dst_unused:UNUSED_PAD src0_sel:DWORD src1_sel:WORD_1\n"
"s_waitcnt lgkmcnt(13)\n"
"v_mfma_f32_16x16x32_bf16 v[244:247], v[180:183], v[120:123], 0\n"
"s_waitcnt lgkmcnt(3)\n"
"ds_read_u16_d16_hi v239, v239 offset:18432\n"
"v_or_b32 v228, v228, v229\n"
"v_mfma_f32_16x16x32_bf16 v[244:247], v[184:187], v[124:127], v[244:247]\n"
"v_or_b32 v229, v230, v231\n"
"v_lshlrev_b32_sdwa v240, 1, v240 dst_sel:DWORD dst_unused:UNUSED_PAD src0_sel:DWORD src1_sel:WORD_1\n"
"v_mfma_f32_16x16x32_bf16 v[244:247], v[188:191], v[128:131], v[244:247]\n"
"ds_read_u16_d16 v240, v240 offset:18432\n"
"v_lshlrev_b32_sdwa v241, 1, v241 dst_sel:DWORD dst_unused:UNUSED_PAD src0_sel:DWORD src1_sel:WORD_1\n"
"v_mfma_f32_16x16x32_bf16 v[248:251], v[192:195], v[120:123], 0\n"
"ds_read_u16_d16_hi v241, v241 offset:18432\n"
"v_or_b32 v230, v232, v233\n"
"v_mfma_f32_16x16x32_bf16 v[248:251], v[196:199], v[124:127], v[248:251]\n"
"v_or_b32 v231, v234, v235\n"
"v_lshlrev_b32_sdwa v242, 1, v242 dst_sel:DWORD dst_unused:UNUSED_PAD src0_sel:DWORD src1_sel:WORD_1\n"
"v_mfma_f32_16x16x32_bf16 v[248:251], v[200:203], v[128:131], v[248:251]\n"
"ds_read_u16_d16 v242, v242 offset:18432\n"
"v_lshlrev_b32_sdwa v243, 1, v243 dst_sel:DWORD dst_unused:UNUSED_PAD src0_sel:DWORD src1_sel:WORD_1\n"
"v_mfma_f32_16x16x32_bf16 v[72:75], v[204:207], v[228:231], v[72:75]\n"
"ds_read_u16_d16_hi v243, v243 offset:18432\n"
"v_mfma_f32_16x16x32_bf16 v[76:79], v[208:211], v[228:231], v[76:79]\n"
"v_lshlrev_b32_sdwa v244, 1, v244 dst_sel:DWORD dst_unused:UNUSED_PAD src0_sel:DWORD src1_sel:WORD_1\n"
"v_mfma_f32_16x16x32_bf16 v[80:83], v[212:215], v[228:231], v[80:83]\n"
"ds_read_u16_d16 v244, v244 offset:18432\n"
"v_lshlrev_b32_sdwa v245, 1, v245 dst_sel:DWORD dst_unused:UNUSED_PAD src0_sel:DWORD src1_sel:WORD_1\n"
"v_mfma_f32_16x16x32_bf16 v[84:87], v[216:219], v[228:231], v[84:87]\n"
"ds_read_u16_d16_hi v245, v245 offset:18432\n"
"v_mfma_f32_16x16x32_bf16 v[88:91], v[220:223], v[228:231], v[88:91]\n"
"v_lshlrev_b32_sdwa v246, 1, v246 dst_sel:DWORD dst_unused:UNUSED_PAD src0_sel:DWORD src1_sel:WORD_1\n"
"v_mfma_f32_16x16x32_bf16 v[92:95], v[224:227], v[228:231], v[92:95]\n"
"ds_read_u16_d16 v246, v246 offset:18432\n"
"v_lshlrev_b32_sdwa v247, 1, v247 dst_sel:DWORD dst_unused:UNUSED_PAD src0_sel:DWORD src1_sel:WORD_1\n"
"v_mfma_f32_16x16x32_bf16 v[228:231], v[180:183], v[132:135], 0\n"
"s_waitcnt lgkmcnt(3)\n"
"ds_read_u16_d16_hi v247, v247 offset:18432\n"
"v_or_b32 v236, v236, v237\n"
"v_mfma_f32_16x16x32_bf16 v[228:231], v[184:187], v[136:139], v[228:231]\n"
"v_or_b32 v237, v238, v239\n"
"v_lshlrev_b32_sdwa v248, 1, v248 dst_sel:DWORD dst_unused:UNUSED_PAD src0_sel:DWORD src1_sel:WORD_1\n"
"v_mfma_f32_16x16x32_bf16 v[228:231], v[188:191], v[140:143], v[228:231]\n"
"ds_read_u16_d16 v248, v248 offset:18432\n"
"v_lshlrev_b32_sdwa v249, 1, v249 dst_sel:DWORD dst_unused:UNUSED_PAD src0_sel:DWORD src1_sel:WORD_1\n"
"v_mfma_f32_16x16x32_bf16 v[232:235], v[192:195], v[132:135], 0\n"
"ds_read_u16_d16_hi v249, v249 offset:18432\n"
"v_or_b32 v238, v240, v241\n"
"v_mfma_f32_16x16x32_bf16 v[232:235], v[196:199], v[136:139], v[232:235]\n"
"v_or_b32 v239, v242, v243\n"
"v_lshlrev_b32_sdwa v250, 1, v250 dst_sel:DWORD dst_unused:UNUSED_PAD src0_sel:DWORD src1_sel:WORD_1\n"
"v_mfma_f32_16x16x32_bf16 v[232:235], v[200:203], v[140:143], v[232:235]\n"
"ds_read_u16_d16 v250, v250 offset:18432\n"
"v_lshlrev_b32_sdwa v251, 1, v251 dst_sel:DWORD dst_unused:UNUSED_PAD src0_sel:DWORD src1_sel:WORD_1\n"
"v_mfma_f32_16x16x32_bf16 v[96:99], v[204:207], v[236:239], v[96:99]\n"
"v_add_u32 v253, 0xffffb7d0, v253\n"
"ds_read_b64_tr_b16 v[204:205], v253 offset:0\n"
"ds_read_b64_tr_b16 v[206:207], v253 offset:1536\n"
"ds_read_u16_d16_hi v251, v251 offset:18432\n"
"v_mfma_f32_16x16x32_bf16 v[100:103], v[208:211], v[236:239], v[100:103]\n"
"ds_read_b64_tr_b16 v[208:209], v253 offset:8\n"
"ds_read_b64_tr_b16 v[210:211], v253 offset:1544\n"
"v_lshlrev_b32_sdwa v228, 1, v228 dst_sel:DWORD dst_unused:UNUSED_PAD src0_sel:DWORD src1_sel:WORD_1\n"
"v_mfma_f32_16x16x32_bf16 v[104:107], v[212:215], v[236:239], v[104:107]\n"
"ds_read_b64_tr_b16 v[212:213], v253 offset:64\n"
"ds_read_b64_tr_b16 v[214:215], v253 offset:1600\n"
"ds_read_u16_d16 v228, v228 offset:18432\n"
"v_lshlrev_b32_sdwa v229, 1, v229 dst_sel:DWORD dst_unused:UNUSED_PAD src0_sel:DWORD src1_sel:WORD_1\n"
"v_mfma_f32_16x16x32_bf16 v[108:111], v[216:219], v[236:239], v[108:111]\n"
"ds_read_b64_tr_b16 v[216:217], v253 offset:72\n"
"ds_read_b64_tr_b16 v[218:219], v253 offset:1608\n"
"ds_read_u16_d16_hi v229, v229 offset:18432\n"
"v_mfma_f32_16x16x32_bf16 v[112:115], v[220:223], v[236:239], v[112:115]\n"
"ds_read_b64_tr_b16 v[220:221], v253 offset:128\n"
"ds_read_b64_tr_b16 v[222:223], v253 offset:1664\n"
"v_lshlrev_b32_sdwa v230, 1, v230 dst_sel:DWORD dst_unused:UNUSED_PAD src0_sel:DWORD src1_sel:WORD_1\n"
"v_mfma_f32_16x16x32_bf16 v[116:119], v[224:227], v[236:239], v[116:119]\n"
"ds_read_b64_tr_b16 v[224:225], v253 offset:136\n"
"ds_read_b64_tr_b16 v[226:227], v253 offset:1672\n"
"ds_read_u16_d16 v230, v230 offset:18432\n"
"v_lshlrev_b32_sdwa v231, 1, v231 dst_sel:DWORD dst_unused:UNUSED_PAD src0_sel:DWORD src1_sel:WORD_1\n"
"v_mfma_f32_16x16x32_bf16 v[236:239], v[180:183], v[144:147], 0\n"
"s_waitcnt lgkmcnt(13)\n"
"ds_read_u16_d16_hi v231, v231 offset:18432\n"
"v_or_b32 v244, v244, v245\n"
"v_mfma_f32_16x16x32_bf16 v[236:239], v[184:187], v[148:151], v[236:239]\n"
"v_or_b32 v245, v246, v247\n"
"v_lshlrev_b32_sdwa v232, 1, v232 dst_sel:DWORD dst_unused:UNUSED_PAD src0_sel:DWORD src1_sel:WORD_1\n"
"v_mfma_f32_16x16x32_bf16 v[236:239], v[188:191], v[152:155], v[236:239]\n"
"ds_read_u16_d16 v232, v232 offset:18432\n"
"v_lshlrev_b32_sdwa v233, 1, v233 dst_sel:DWORD dst_unused:UNUSED_PAD src0_sel:DWORD src1_sel:WORD_1\n"
"v_mfma_f32_16x16x32_bf16 v[240:243], v[192:195], v[144:147], 0\n"
"ds_read_u16_d16_hi v233, v233 offset:18432\n"
"v_or_b32 v246, v248, v249\n"
"v_mfma_f32_16x16x32_bf16 v[240:243], v[196:199], v[148:151], v[240:243]\n"
"v_or_b32 v247, v250, v251\n"
"v_lshlrev_b32_sdwa v234, 1, v234 dst_sel:DWORD dst_unused:UNUSED_PAD src0_sel:DWORD src1_sel:WORD_1\n"
"v_mfma_f32_16x16x32_bf16 v[240:243], v[200:203], v[152:155], v[240:243]\n"
"ds_read_u16_d16 v234, v234 offset:18432\n"
"v_lshlrev_b32_sdwa v235, 1, v235 dst_sel:DWORD dst_unused:UNUSED_PAD src0_sel:DWORD src1_sel:WORD_1\n"
"s_waitcnt lgkmcnt(13)\n"
"v_mfma_f32_16x16x32_bf16 v[0:3], v[204:207], v[244:247], v[0:3]\n"
"ds_read_u16_d16_hi v235, v235 offset:18432\n"
"v_mfma_f32_16x16x32_bf16 v[4:7], v[208:211], v[244:247], v[4:7]\n"
"v_lshlrev_b32_sdwa v236, 1, v236 dst_sel:DWORD dst_unused:UNUSED_PAD src0_sel:DWORD src1_sel:WORD_1\n"
"v_mfma_f32_16x16x32_bf16 v[8:11], v[212:215], v[244:247], v[8:11]\n"
"ds_read_u16_d16 v236, v236 offset:18432\n"
"v_lshlrev_b32_sdwa v237, 1, v237 dst_sel:DWORD dst_unused:UNUSED_PAD src0_sel:DWORD src1_sel:WORD_1\n"
"s_waitcnt lgkmcnt(7)\n"
"v_mfma_f32_16x16x32_bf16 v[12:15], v[216:219], v[244:247], v[12:15]\n"
"ds_read_u16_d16_hi v237, v237 offset:18432\n"
"v_mfma_f32_16x16x32_bf16 v[16:19], v[220:223], v[244:247], v[16:19]\n"
"v_lshlrev_b32_sdwa v238, 1, v238 dst_sel:DWORD dst_unused:UNUSED_PAD src0_sel:DWORD src1_sel:WORD_1\n"
"v_mfma_f32_16x16x32_bf16 v[20:23], v[224:227], v[244:247], v[20:23]\n"
"ds_read_u16_d16 v238, v238 offset:18432\n"
"v_lshlrev_b32_sdwa v239, 1, v239 dst_sel:DWORD dst_unused:UNUSED_PAD src0_sel:DWORD src1_sel:WORD_1\n"
"v_mfma_f32_16x16x32_bf16 v[244:247], v[180:183], v[156:159], 0\n"
"s_waitcnt lgkmcnt(3)\n"
"ds_read_u16_d16_hi v239, v239 offset:18432\n"
"v_or_b32 v228, v228, v229\n"
"v_mfma_f32_16x16x32_bf16 v[244:247], v[184:187], v[160:163], v[244:247]\n"
"v_or_b32 v229, v230, v231\n"
"v_lshlrev_b32_sdwa v240, 1, v240 dst_sel:DWORD dst_unused:UNUSED_PAD src0_sel:DWORD src1_sel:WORD_1\n"
"v_mfma_f32_16x16x32_bf16 v[244:247], v[188:191], v[164:167], v[244:247]\n"
"ds_read_u16_d16 v240, v240 offset:18432\n"
"v_lshlrev_b32_sdwa v241, 1, v241 dst_sel:DWORD dst_unused:UNUSED_PAD src0_sel:DWORD src1_sel:WORD_1\n"
"v_mfma_f32_16x16x32_bf16 v[248:251], v[192:195], v[156:159], 0\n"
"ds_read_u16_d16_hi v241, v241 offset:18432\n"
"v_or_b32 v230, v232, v233\n"
"v_mfma_f32_16x16x32_bf16 v[248:251], v[196:199], v[160:163], v[248:251]\n"
"v_or_b32 v231, v234, v235\n"
"v_lshlrev_b32_sdwa v242, 1, v242 dst_sel:DWORD dst_unused:UNUSED_PAD src0_sel:DWORD src1_sel:WORD_1\n"
"v_mfma_f32_16x16x32_bf16 v[248:251], v[200:203], v[164:167], v[248:251]\n"
"ds_read_u16_d16 v242, v242 offset:18432\n"
"v_lshlrev_b32_sdwa v243, 1, v243 dst_sel:DWORD dst_unused:UNUSED_PAD src0_sel:DWORD src1_sel:WORD_1\n"
"v_mfma_f32_16x16x32_bf16 v[24:27], v[204:207], v[228:231], v[24:27]\n"
"ds_read_u16_d16_hi v243, v243 offset:18432\n"
"v_mfma_f32_16x16x32_bf16 v[28:31], v[208:211], v[228:231], v[28:31]\n"
"v_lshlrev_b32_sdwa v244, 1, v244 dst_sel:DWORD dst_unused:UNUSED_PAD src0_sel:DWORD src1_sel:WORD_1\n"
"v_mfma_f32_16x16x32_bf16 v[32:35], v[212:215], v[228:231], v[32:35]\n"
"ds_read_u16_d16 v244, v244 offset:18432\n"
"v_lshlrev_b32_sdwa v245, 1, v245 dst_sel:DWORD dst_unused:UNUSED_PAD src0_sel:DWORD src1_sel:WORD_1\n"
"v_mfma_f32_16x16x32_bf16 v[36:39], v[216:219], v[228:231], v[36:39]\n"
"ds_read_u16_d16_hi v245, v245 offset:18432\n"
"v_mfma_f32_16x16x32_bf16 v[40:43], v[220:223], v[228:231], v[40:43]\n"
"v_lshlrev_b32_sdwa v246, 1, v246 dst_sel:DWORD dst_unused:UNUSED_PAD src0_sel:DWORD src1_sel:WORD_1\n"
"v_mfma_f32_16x16x32_bf16 v[44:47], v[224:227], v[228:231], v[44:47]\n"
"ds_read_u16_d16 v246, v246 offset:18432\n"
"v_lshlrev_b32_sdwa v247, 1, v247 dst_sel:DWORD dst_unused:UNUSED_PAD src0_sel:DWORD src1_sel:WORD_1\n"
"v_mfma_f32_16x16x32_bf16 v[228:231], v[180:183], v[168:171], 0\n"
"ds_read_b64_tr_b16 v[180:181], v252 offset:6144\n"
"ds_read_b64_tr_b16 v[182:183], v252 offset:9216\n"
"s_waitcnt lgkmcnt(5)\n"
"ds_read_u16_d16_hi v247, v247 offset:18432\n"
"v_or_b32 v236, v236, v237\n"
"v_mfma_f32_16x16x32_bf16 v[228:231], v[184:187], v[172:175], v[228:231]\n"
"ds_read_b64_tr_b16 v[184:185], v252 offset:7168\n"
"ds_read_b64_tr_b16 v[186:187], v252 offset:10240\n"
"v_or_b32 v237, v238, v239\n"
"v_lshlrev_b32_sdwa v248, 1, v248 dst_sel:DWORD dst_unused:UNUSED_PAD src0_sel:DWORD src1_sel:WORD_1\n"
"v_mfma_f32_16x16x32_bf16 v[228:231], v[188:191], v[176:179], v[228:231]\n"
"ds_read_b64_tr_b16 v[188:189], v252 offset:8192\n"
"ds_read_b64_tr_b16 v[190:191], v252 offset:11264\n"
"ds_read_u16_d16 v248, v248 offset:18432\n"
"v_lshlrev_b32_sdwa v249, 1, v249 dst_sel:DWORD dst_unused:UNUSED_PAD src0_sel:DWORD src1_sel:WORD_1\n"
"v_mfma_f32_16x16x32_bf16 v[232:235], v[192:195], v[168:171], 0\n"
"ds_read_b64_tr_b16 v[192:193], v252 offset:6656\n"
"ds_read_b64_tr_b16 v[194:195], v252 offset:9728\n"
"ds_read_u16_d16_hi v249, v249 offset:18432\n"
"v_or_b32 v238, v240, v241\n"
"v_mfma_f32_16x16x32_bf16 v[232:235], v[196:199], v[172:175], v[232:235]\n"
"ds_read_b64_tr_b16 v[196:197], v252 offset:7680\n"
"ds_read_b64_tr_b16 v[198:199], v252 offset:10752\n"
"v_or_b32 v239, v242, v243\n"
"v_lshlrev_b32_sdwa v250, 1, v250 dst_sel:DWORD dst_unused:UNUSED_PAD src0_sel:DWORD src1_sel:WORD_1\n"
"v_mfma_f32_16x16x32_bf16 v[232:235], v[200:203], v[176:179], v[232:235]\n"
"ds_read_b64_tr_b16 v[200:201], v252 offset:8704\n"
"ds_read_b64_tr_b16 v[202:203], v252 offset:11776\n"
"ds_read_u16_d16 v250, v250 offset:18432\n"
"v_lshlrev_b32_sdwa v251, 1, v251 dst_sel:DWORD dst_unused:UNUSED_PAD src0_sel:DWORD src1_sel:WORD_1\n"
"v_mfma_f32_16x16x32_bf16 v[48:51], v[204:207], v[236:239], v[48:51]\n"
"ds_read_u16_d16_hi v251, v251 offset:18432\n"
"v_mfma_f32_16x16x32_bf16 v[52:55], v[208:211], v[236:239], v[52:55]\n"
"v_lshlrev_b32_sdwa v228, 1, v228 dst_sel:DWORD dst_unused:UNUSED_PAD src0_sel:DWORD src1_sel:WORD_1\n"
"v_mfma_f32_16x16x32_bf16 v[56:59], v[212:215], v[236:239], v[56:59]\n"
"ds_read_u16_d16 v228, v228 offset:18432\n"
"v_lshlrev_b32_sdwa v229, 1, v229 dst_sel:DWORD dst_unused:UNUSED_PAD src0_sel:DWORD src1_sel:WORD_1\n"
"v_mfma_f32_16x16x32_bf16 v[60:63], v[216:219], v[236:239], v[60:63]\n"
"ds_read_u16_d16_hi v229, v229 offset:18432\n"
"v_mfma_f32_16x16x32_bf16 v[64:67], v[220:223], v[236:239], v[64:67]\n"
"v_lshlrev_b32_sdwa v230, 1, v230 dst_sel:DWORD dst_unused:UNUSED_PAD src0_sel:DWORD src1_sel:WORD_1\n"
"v_mfma_f32_16x16x32_bf16 v[68:71], v[224:227], v[236:239], v[68:71]\n"
"ds_read_u16_d16 v230, v230 offset:18432\n"
"v_lshlrev_b32_sdwa v231, 1, v231 dst_sel:DWORD dst_unused:UNUSED_PAD src0_sel:DWORD src1_sel:WORD_1\n"
"s_waitcnt lgkmcnt(13)\n"
"v_mfma_f32_16x16x32_bf16 v[236:239], v[180:183], v[120:123], 0\n"
"s_waitcnt lgkmcnt(3)\n"
"ds_read_u16_d16_hi v231, v231 offset:18432\n"
"v_or_b32 v244, v244, v245\n"
"v_mfma_f32_16x16x32_bf16 v[236:239], v[184:187], v[124:127], v[236:239]\n"
"v_or_b32 v245, v246, v247\n"
"v_lshlrev_b32_sdwa v232, 1, v232 dst_sel:DWORD dst_unused:UNUSED_PAD src0_sel:DWORD src1_sel:WORD_1\n"
"v_mfma_f32_16x16x32_bf16 v[236:239], v[188:191], v[128:131], v[236:239]\n"
"ds_read_u16_d16 v232, v232 offset:18432\n"
"v_lshlrev_b32_sdwa v233, 1, v233 dst_sel:DWORD dst_unused:UNUSED_PAD src0_sel:DWORD src1_sel:WORD_1\n"
"v_mfma_f32_16x16x32_bf16 v[240:243], v[192:195], v[120:123], 0\n"
"ds_read_u16_d16_hi v233, v233 offset:18432\n"
"v_or_b32 v246, v248, v249\n"
"v_mfma_f32_16x16x32_bf16 v[240:243], v[196:199], v[124:127], v[240:243]\n"
"v_or_b32 v247, v250, v251\n"
"v_lshlrev_b32_sdwa v234, 1, v234 dst_sel:DWORD dst_unused:UNUSED_PAD src0_sel:DWORD src1_sel:WORD_1\n"
"v_mfma_f32_16x16x32_bf16 v[240:243], v[200:203], v[128:131], v[240:243]\n"
"ds_read_u16_d16 v234, v234 offset:18432\n"
"v_lshlrev_b32_sdwa v235, 1, v235 dst_sel:DWORD dst_unused:UNUSED_PAD src0_sel:DWORD src1_sel:WORD_1\n"
"v_mfma_f32_16x16x32_bf16 v[72:75], v[204:207], v[244:247], v[72:75]\n"
"ds_read_u16_d16_hi v235, v235 offset:18432\n"
"v_mfma_f32_16x16x32_bf16 v[76:79], v[208:211], v[244:247], v[76:79]\n"
"v_lshlrev_b32_sdwa v236, 1, v236 dst_sel:DWORD dst_unused:UNUSED_PAD src0_sel:DWORD src1_sel:WORD_1\n"
"v_mfma_f32_16x16x32_bf16 v[80:83], v[212:215], v[244:247], v[80:83]\n"
"ds_read_u16_d16 v236, v236 offset:18432\n"
"v_lshlrev_b32_sdwa v237, 1, v237 dst_sel:DWORD dst_unused:UNUSED_PAD src0_sel:DWORD src1_sel:WORD_1\n"
"v_mfma_f32_16x16x32_bf16 v[84:87], v[216:219], v[244:247], v[84:87]\n"
"ds_read_u16_d16_hi v237, v237 offset:18432\n"
"v_mfma_f32_16x16x32_bf16 v[88:91], v[220:223], v[244:247], v[88:91]\n"
"v_lshlrev_b32_sdwa v238, 1, v238 dst_sel:DWORD dst_unused:UNUSED_PAD src0_sel:DWORD src1_sel:WORD_1\n"
"v_mfma_f32_16x16x32_bf16 v[92:95], v[224:227], v[244:247], v[92:95]\n"
"ds_read_u16_d16 v238, v238 offset:18432\n"
"v_lshlrev_b32_sdwa v239, 1, v239 dst_sel:DWORD dst_unused:UNUSED_PAD src0_sel:DWORD src1_sel:WORD_1\n"
"v_mfma_f32_16x16x32_bf16 v[244:247], v[180:183], v[132:135], 0\n"
"s_waitcnt lgkmcnt(3)\n"
"ds_read_u16_d16_hi v239, v239 offset:18432\n"
"v_or_b32 v228, v228, v229\n"
"v_mfma_f32_16x16x32_bf16 v[244:247], v[184:187], v[136:139], v[244:247]\n"
"v_or_b32 v229, v230, v231\n"
"v_lshlrev_b32_sdwa v240, 1, v240 dst_sel:DWORD dst_unused:UNUSED_PAD src0_sel:DWORD src1_sel:WORD_1\n"
"v_mfma_f32_16x16x32_bf16 v[244:247], v[188:191], v[140:143], v[244:247]\n"
"ds_read_u16_d16 v240, v240 offset:18432\n"
"v_lshlrev_b32_sdwa v241, 1, v241 dst_sel:DWORD dst_unused:UNUSED_PAD src0_sel:DWORD src1_sel:WORD_1\n"
"v_mfma_f32_16x16x32_bf16 v[248:251], v[192:195], v[132:135], 0\n"
"ds_read_u16_d16_hi v241, v241 offset:18432\n"
"v_or_b32 v230, v232, v233\n"
"v_mfma_f32_16x16x32_bf16 v[248:251], v[196:199], v[136:139], v[248:251]\n"
"v_or_b32 v231, v234, v235\n"
"v_lshlrev_b32_sdwa v242, 1, v242 dst_sel:DWORD dst_unused:UNUSED_PAD src0_sel:DWORD src1_sel:WORD_1\n"
"v_mfma_f32_16x16x32_bf16 v[248:251], v[200:203], v[140:143], v[248:251]\n"
"ds_read_u16_d16 v242, v242 offset:18432\n"
"v_lshlrev_b32_sdwa v243, 1, v243 dst_sel:DWORD dst_unused:UNUSED_PAD src0_sel:DWORD src1_sel:WORD_1\n"
"v_mfma_f32_16x16x32_bf16 v[96:99], v[204:207], v[228:231], v[96:99]\n"
"ds_read_b64_tr_b16 v[204:205], v253 offset:6160\n"
"ds_read_b64_tr_b16 v[206:207], v253 offset:7696\n"
"ds_read_u16_d16_hi v243, v243 offset:18432\n"
"v_mfma_f32_16x16x32_bf16 v[100:103], v[208:211], v[228:231], v[100:103]\n"
"ds_read_b64_tr_b16 v[208:209], v253 offset:6168\n"
"ds_read_b64_tr_b16 v[210:211], v253 offset:7704\n"
"v_lshlrev_b32_sdwa v244, 1, v244 dst_sel:DWORD dst_unused:UNUSED_PAD src0_sel:DWORD src1_sel:WORD_1\n"
"v_mfma_f32_16x16x32_bf16 v[104:107], v[212:215], v[228:231], v[104:107]\n"
"ds_read_b64_tr_b16 v[212:213], v253 offset:6224\n"
"ds_read_b64_tr_b16 v[214:215], v253 offset:7760\n"
"ds_read_u16_d16 v244, v244 offset:18432\n"
"v_lshlrev_b32_sdwa v245, 1, v245 dst_sel:DWORD dst_unused:UNUSED_PAD src0_sel:DWORD src1_sel:WORD_1\n"
"v_mfma_f32_16x16x32_bf16 v[108:111], v[216:219], v[228:231], v[108:111]\n"
"ds_read_b64_tr_b16 v[216:217], v253 offset:6232\n"
"ds_read_b64_tr_b16 v[218:219], v253 offset:7768\n"
"ds_read_u16_d16_hi v245, v245 offset:18432\n"
"v_mfma_f32_16x16x32_bf16 v[112:115], v[220:223], v[228:231], v[112:115]\n"
"ds_read_b64_tr_b16 v[220:221], v253 offset:6288\n"
"ds_read_b64_tr_b16 v[222:223], v253 offset:7824\n"
"v_lshlrev_b32_sdwa v246, 1, v246 dst_sel:DWORD dst_unused:UNUSED_PAD src0_sel:DWORD src1_sel:WORD_1\n"
"v_mfma_f32_16x16x32_bf16 v[116:119], v[224:227], v[228:231], v[116:119]\n"
"ds_read_b64_tr_b16 v[224:225], v253 offset:6296\n"
"ds_read_b64_tr_b16 v[226:227], v253 offset:7832\n"
"ds_read_u16_d16 v246, v246 offset:18432\n"
"v_lshlrev_b32_sdwa v247, 1, v247 dst_sel:DWORD dst_unused:UNUSED_PAD src0_sel:DWORD src1_sel:WORD_1\n"
"v_mfma_f32_16x16x32_bf16 v[228:231], v[180:183], v[144:147], 0\n"
"s_waitcnt lgkmcnt(13)\n"
"ds_read_u16_d16_hi v247, v247 offset:18432\n"
"v_or_b32 v236, v236, v237\n"
"v_mfma_f32_16x16x32_bf16 v[228:231], v[184:187], v[148:151], v[228:231]\n"
"v_or_b32 v237, v238, v239\n"
"v_lshlrev_b32_sdwa v248, 1, v248 dst_sel:DWORD dst_unused:UNUSED_PAD src0_sel:DWORD src1_sel:WORD_1\n"
"v_mfma_f32_16x16x32_bf16 v[228:231], v[188:191], v[152:155], v[228:231]\n"
"ds_read_u16_d16 v248, v248 offset:18432\n"
"v_lshlrev_b32_sdwa v249, 1, v249 dst_sel:DWORD dst_unused:UNUSED_PAD src0_sel:DWORD src1_sel:WORD_1\n"
"v_mfma_f32_16x16x32_bf16 v[232:235], v[192:195], v[144:147], 0\n"
"ds_read_u16_d16_hi v249, v249 offset:18432\n"
"v_or_b32 v238, v240, v241\n"
"v_mfma_f32_16x16x32_bf16 v[232:235], v[196:199], v[148:151], v[232:235]\n"
"v_or_b32 v239, v242, v243\n"
"v_lshlrev_b32_sdwa v250, 1, v250 dst_sel:DWORD dst_unused:UNUSED_PAD src0_sel:DWORD src1_sel:WORD_1\n"
"v_mfma_f32_16x16x32_bf16 v[232:235], v[200:203], v[152:155], v[232:235]\n"
"ds_read_u16_d16 v250, v250 offset:18432\n"
"v_lshlrev_b32_sdwa v251, 1, v251 dst_sel:DWORD dst_unused:UNUSED_PAD src0_sel:DWORD src1_sel:WORD_1\n"
"s_waitcnt lgkmcnt(13)\n"
"v_mfma_f32_16x16x32_bf16 v[0:3], v[204:207], v[236:239], v[0:3]\n"
"ds_read_u16_d16_hi v251, v251 offset:18432\n"
"v_mfma_f32_16x16x32_bf16 v[4:7], v[208:211], v[236:239], v[4:7]\n"
"v_lshlrev_b32_sdwa v228, 1, v228 dst_sel:DWORD dst_unused:UNUSED_PAD src0_sel:DWORD src1_sel:WORD_1\n"
"v_mfma_f32_16x16x32_bf16 v[8:11], v[212:215], v[236:239], v[8:11]\n"
"ds_read_u16_d16 v228, v228 offset:18432\n"
"v_lshlrev_b32_sdwa v229, 1, v229 dst_sel:DWORD dst_unused:UNUSED_PAD src0_sel:DWORD src1_sel:WORD_1\n"
"s_waitcnt lgkmcnt(7)\n"
"v_mfma_f32_16x16x32_bf16 v[12:15], v[216:219], v[236:239], v[12:15]\n"
"ds_read_u16_d16_hi v229, v229 offset:18432\n"
"v_mfma_f32_16x16x32_bf16 v[16:19], v[220:223], v[236:239], v[16:19]\n"
"v_lshlrev_b32_sdwa v230, 1, v230 dst_sel:DWORD dst_unused:UNUSED_PAD src0_sel:DWORD src1_sel:WORD_1\n"
"v_mfma_f32_16x16x32_bf16 v[20:23], v[224:227], v[236:239], v[20:23]\n"
"ds_read_u16_d16 v230, v230 offset:18432\n"
"v_lshlrev_b32_sdwa v231, 1, v231 dst_sel:DWORD dst_unused:UNUSED_PAD src0_sel:DWORD src1_sel:WORD_1\n"
"v_mfma_f32_16x16x32_bf16 v[236:239], v[180:183], v[156:159], 0\n"
"s_waitcnt lgkmcnt(3)\n"
"ds_read_u16_d16_hi v231, v231 offset:18432\n"
"v_or_b32 v244, v244, v245\n"
"v_mfma_f32_16x16x32_bf16 v[236:239], v[184:187], v[160:163], v[236:239]\n"
"v_or_b32 v245, v246, v247\n"
"v_lshlrev_b32_sdwa v232, 1, v232 dst_sel:DWORD dst_unused:UNUSED_PAD src0_sel:DWORD src1_sel:WORD_1\n"
"v_mfma_f32_16x16x32_bf16 v[236:239], v[188:191], v[164:167], v[236:239]\n"
"ds_read_u16_d16 v232, v232 offset:18432\n"
"v_lshlrev_b32_sdwa v233, 1, v233 dst_sel:DWORD dst_unused:UNUSED_PAD src0_sel:DWORD src1_sel:WORD_1\n"
"v_mfma_f32_16x16x32_bf16 v[240:243], v[192:195], v[156:159], 0\n"
"ds_read_u16_d16_hi v233, v233 offset:18432\n"
"v_or_b32 v246, v248, v249\n"
"v_mfma_f32_16x16x32_bf16 v[240:243], v[196:199], v[160:163], v[240:243]\n"
"v_or_b32 v247, v250, v251\n"
"v_lshlrev_b32_sdwa v234, 1, v234 dst_sel:DWORD dst_unused:UNUSED_PAD src0_sel:DWORD src1_sel:WORD_1\n"
"v_mfma_f32_16x16x32_bf16 v[240:243], v[200:203], v[164:167], v[240:243]\n"
"ds_read_u16_d16 v234, v234 offset:18432\n"
"v_lshlrev_b32_sdwa v235, 1, v235 dst_sel:DWORD dst_unused:UNUSED_PAD src0_sel:DWORD src1_sel:WORD_1\n"
"v_mfma_f32_16x16x32_bf16 v[24:27], v[204:207], v[244:247], v[24:27]\n"
"ds_read_u16_d16_hi v235, v235 offset:18432\n"
"v_mfma_f32_16x16x32_bf16 v[28:31], v[208:211], v[244:247], v[28:31]\n"
"v_lshlrev_b32_sdwa v236, 1, v236 dst_sel:DWORD dst_unused:UNUSED_PAD src0_sel:DWORD src1_sel:WORD_1\n"
"v_mfma_f32_16x16x32_bf16 v[32:35], v[212:215], v[244:247], v[32:35]\n"
"ds_read_u16_d16 v236, v236 offset:18432\n"
"v_lshlrev_b32_sdwa v237, 1, v237 dst_sel:DWORD dst_unused:UNUSED_PAD src0_sel:DWORD src1_sel:WORD_1\n"
"v_mfma_f32_16x16x32_bf16 v[36:39], v[216:219], v[244:247], v[36:39]\n"
"ds_read_u16_d16_hi v237, v237 offset:18432\n"
"v_mfma_f32_16x16x32_bf16 v[40:43], v[220:223], v[244:247], v[40:43]\n"
"v_lshlrev_b32_sdwa v238, 1, v238 dst_sel:DWORD dst_unused:UNUSED_PAD src0_sel:DWORD src1_sel:WORD_1\n"
"v_mfma_f32_16x16x32_bf16 v[44:47], v[224:227], v[244:247], v[44:47]\n"
"ds_read_u16_d16 v238, v238 offset:18432\n"
"v_lshlrev_b32_sdwa v239, 1, v239 dst_sel:DWORD dst_unused:UNUSED_PAD src0_sel:DWORD src1_sel:WORD_1\n"
"v_mfma_f32_16x16x32_bf16 v[244:247], v[180:183], v[168:171], 0\n"
"ds_read_b64_tr_b16 v[180:181], v252 offset:12288\n"
"ds_read_b64_tr_b16 v[182:183], v252 offset:15360\n"
"s_waitcnt lgkmcnt(5)\n"
"ds_read_u16_d16_hi v239, v239 offset:18432\n"
"v_or_b32 v228, v228, v229\n"
"v_mfma_f32_16x16x32_bf16 v[244:247], v[184:187], v[172:175], v[244:247]\n"
"ds_read_b64_tr_b16 v[184:185], v252 offset:13312\n"
"ds_read_b64_tr_b16 v[186:187], v252 offset:16384\n"
"v_or_b32 v229, v230, v231\n"
"v_lshlrev_b32_sdwa v240, 1, v240 dst_sel:DWORD dst_unused:UNUSED_PAD src0_sel:DWORD src1_sel:WORD_1\n"
"v_mfma_f32_16x16x32_bf16 v[244:247], v[188:191], v[176:179], v[244:247]\n"
"ds_read_b64_tr_b16 v[188:189], v252 offset:14336\n"
"ds_read_b64_tr_b16 v[190:191], v252 offset:17408\n"
"ds_read_u16_d16 v240, v240 offset:18432\n"
"v_lshlrev_b32_sdwa v241, 1, v241 dst_sel:DWORD dst_unused:UNUSED_PAD src0_sel:DWORD src1_sel:WORD_1\n"
"v_mfma_f32_16x16x32_bf16 v[248:251], v[192:195], v[168:171], 0\n"
"ds_read_b64_tr_b16 v[192:193], v252 offset:12800\n"
"ds_read_b64_tr_b16 v[194:195], v252 offset:15872\n"
"ds_read_u16_d16_hi v241, v241 offset:18432\n"
"v_or_b32 v230, v232, v233\n"
"v_mfma_f32_16x16x32_bf16 v[248:251], v[196:199], v[172:175], v[248:251]\n"
"ds_read_b64_tr_b16 v[196:197], v252 offset:13824\n"
"ds_read_b64_tr_b16 v[198:199], v252 offset:16896\n"
"v_or_b32 v231, v234, v235\n"
"v_lshlrev_b32_sdwa v242, 1, v242 dst_sel:DWORD dst_unused:UNUSED_PAD src0_sel:DWORD src1_sel:WORD_1\n"
"v_mfma_f32_16x16x32_bf16 v[248:251], v[200:203], v[176:179], v[248:251]\n"
"ds_read_b64_tr_b16 v[200:201], v252 offset:14848\n"
"ds_read_b64_tr_b16 v[202:203], v252 offset:17920\n"
"ds_read_u16_d16 v242, v242 offset:18432\n"
"v_lshlrev_b32_sdwa v243, 1, v243 dst_sel:DWORD dst_unused:UNUSED_PAD src0_sel:DWORD src1_sel:WORD_1\n"
"v_mfma_f32_16x16x32_bf16 v[48:51], v[204:207], v[228:231], v[48:51]\n"
"ds_read_u16_d16_hi v243, v243 offset:18432\n"
"v_mfma_f32_16x16x32_bf16 v[52:55], v[208:211], v[228:231], v[52:55]\n"
"v_lshlrev_b32_sdwa v244, 1, v244 dst_sel:DWORD dst_unused:UNUSED_PAD src0_sel:DWORD src1_sel:WORD_1\n"
"v_mfma_f32_16x16x32_bf16 v[56:59], v[212:215], v[228:231], v[56:59]\n"
"ds_read_u16_d16 v244, v244 offset:18432\n"
"v_lshlrev_b32_sdwa v245, 1, v245 dst_sel:DWORD dst_unused:UNUSED_PAD src0_sel:DWORD src1_sel:WORD_1\n"
"v_mfma_f32_16x16x32_bf16 v[60:63], v[216:219], v[228:231], v[60:63]\n"
"ds_read_u16_d16_hi v245, v245 offset:18432\n"
"v_mfma_f32_16x16x32_bf16 v[64:67], v[220:223], v[228:231], v[64:67]\n"
"v_lshlrev_b32_sdwa v246, 1, v246 dst_sel:DWORD dst_unused:UNUSED_PAD src0_sel:DWORD src1_sel:WORD_1\n"
"v_mfma_f32_16x16x32_bf16 v[68:71], v[224:227], v[228:231], v[68:71]\n"
"ds_read_u16_d16 v246, v246 offset:18432\n"
"v_lshlrev_b32_sdwa v247, 1, v247 dst_sel:DWORD dst_unused:UNUSED_PAD src0_sel:DWORD src1_sel:WORD_1\n"
"s_waitcnt lgkmcnt(13)\n"
"v_mfma_f32_16x16x32_bf16 v[228:231], v[180:183], v[120:123], 0\n"
"s_waitcnt lgkmcnt(3)\n"
"ds_read_u16_d16_hi v247, v247 offset:18432\n"
"v_or_b32 v236, v236, v237\n"
"v_mfma_f32_16x16x32_bf16 v[228:231], v[184:187], v[124:127], v[228:231]\n"
"v_or_b32 v237, v238, v239\n"
"v_lshlrev_b32_sdwa v248, 1, v248 dst_sel:DWORD dst_unused:UNUSED_PAD src0_sel:DWORD src1_sel:WORD_1\n"
"v_mfma_f32_16x16x32_bf16 v[228:231], v[188:191], v[128:131], v[228:231]\n"
"ds_read_u16_d16 v248, v248 offset:18432\n"
"v_lshlrev_b32_sdwa v249, 1, v249 dst_sel:DWORD dst_unused:UNUSED_PAD src0_sel:DWORD src1_sel:WORD_1\n"
"v_mfma_f32_16x16x32_bf16 v[232:235], v[192:195], v[120:123], 0\n"
"ds_read_u16_d16_hi v249, v249 offset:18432\n"
"v_or_b32 v238, v240, v241\n"
"v_mfma_f32_16x16x32_bf16 v[232:235], v[196:199], v[124:127], v[232:235]\n"
"v_or_b32 v239, v242, v243\n"
"v_lshlrev_b32_sdwa v250, 1, v250 dst_sel:DWORD dst_unused:UNUSED_PAD src0_sel:DWORD src1_sel:WORD_1\n"
"v_mfma_f32_16x16x32_bf16 v[232:235], v[200:203], v[128:131], v[232:235]\n"
"ds_read_u16_d16 v250, v250 offset:18432\n"
"v_lshlrev_b32_sdwa v251, 1, v251 dst_sel:DWORD dst_unused:UNUSED_PAD src0_sel:DWORD src1_sel:WORD_1\n"
"v_mfma_f32_16x16x32_bf16 v[72:75], v[204:207], v[236:239], v[72:75]\n"
"ds_read_u16_d16_hi v251, v251 offset:18432\n"
"v_mfma_f32_16x16x32_bf16 v[76:79], v[208:211], v[236:239], v[76:79]\n"
"v_lshlrev_b32_sdwa v228, 1, v228 dst_sel:DWORD dst_unused:UNUSED_PAD src0_sel:DWORD src1_sel:WORD_1\n"
"v_mfma_f32_16x16x32_bf16 v[80:83], v[212:215], v[236:239], v[80:83]\n"
"ds_read_u16_d16 v228, v228 offset:18432\n"
"v_lshlrev_b32_sdwa v229, 1, v229 dst_sel:DWORD dst_unused:UNUSED_PAD src0_sel:DWORD src1_sel:WORD_1\n"
"v_mfma_f32_16x16x32_bf16 v[84:87], v[216:219], v[236:239], v[84:87]\n"
"ds_read_u16_d16_hi v229, v229 offset:18432\n"
"v_mfma_f32_16x16x32_bf16 v[88:91], v[220:223], v[236:239], v[88:91]\n"
"v_lshlrev_b32_sdwa v230, 1, v230 dst_sel:DWORD dst_unused:UNUSED_PAD src0_sel:DWORD src1_sel:WORD_1\n"
"v_mfma_f32_16x16x32_bf16 v[92:95], v[224:227], v[236:239], v[92:95]\n"
"ds_read_u16_d16 v230, v230 offset:18432\n"
"v_lshlrev_b32_sdwa v231, 1, v231 dst_sel:DWORD dst_unused:UNUSED_PAD src0_sel:DWORD src1_sel:WORD_1\n"
"v_mfma_f32_16x16x32_bf16 v[236:239], v[180:183], v[132:135], 0\n"
"s_waitcnt lgkmcnt(3)\n"
"ds_read_u16_d16_hi v231, v231 offset:18432\n"
"v_or_b32 v244, v244, v245\n"
"v_mbcnt_lo_u32_b32 v123, -1, 0\n"
"v_mbcnt_hi_u32_b32 v123, -1, v123\n"
"v_lshrrev_b32 v121, 2, v123\n"
"v_xor_b32 v121, s59, v121\n"
"v_bfe_u32 v122, v121, 1, 1\n"
"v_mul_i32_i24 v120, s96, v122\n"
"v_and_b32 v121, 1, v121\n"
"v_mfma_f32_16x16x32_bf16 v[236:239], v[184:187], v[136:139], v[236:239]\n"
"v_or_b32 v245, v246, v247\n"
"v_lshlrev_b32_sdwa v232, 1, v232 dst_sel:DWORD dst_unused:UNUSED_PAD src0_sel:DWORD src1_sel:WORD_1\n"
"v_mul_u32_u24 v121, 192, v121\n"
"v_add3_u32 v120, v120, v121, s56\n"
"v_and_b32 v121, 3, v123\n"
"v_mul_u32_u24 v121, 1152, v121\n"
"v_and_b32 v122, 48, v123\n"
"v_add3_u32 v120, v120, v121, v122\n"
"s_mov_b64 s[48:49], s[34:35]\n"
"s_and_b32 s49, s49, 0xffff\n"
"s_mov_b32 s50, 28311552\n"
"s_mov_b32 s51, 0x20000\n"
"v_mfma_f32_16x16x32_bf16 v[236:239], v[188:191], v[140:143], v[236:239]\n"
"ds_read_u16_d16 v232, v232 offset:18432\n"
"v_lshlrev_b32_sdwa v233, 1, v233 dst_sel:DWORD dst_unused:UNUSED_PAD src0_sel:DWORD src1_sel:WORD_1\n"
"v_mfma_f32_16x16x32_bf16 v[240:243], v[192:195], v[132:135], 0\n"
"ds_read_u16_d16_hi v233, v233 offset:18432\n"
"v_or_b32 v246, v248, v249\n"
"v_mfma_f32_16x16x32_bf16 v[240:243], v[196:199], v[136:139], v[240:243]\n"
"v_or_b32 v247, v250, v251\n"
"v_lshlrev_b32_sdwa v234, 1, v234 dst_sel:DWORD dst_unused:UNUSED_PAD src0_sel:DWORD src1_sel:WORD_1\n"
"v_mfma_f32_16x16x32_bf16 v[240:243], v[200:203], v[140:143], v[240:243]\n"
"ds_read_u16_d16 v234, v234 offset:18432\n"
"v_lshlrev_b32_sdwa v235, 1, v235 dst_sel:DWORD dst_unused:UNUSED_PAD src0_sel:DWORD src1_sel:WORD_1\n"
"v_mfma_f32_16x16x32_bf16 v[96:99], v[204:207], v[244:247], v[96:99]\n"
"ds_read_b64_tr_b16 v[204:205], v253 offset:12320\n"
"ds_read_b64_tr_b16 v[206:207], v253 offset:13856\n"
"ds_read_u16_d16_hi v235, v235 offset:18432\n"
"v_mfma_f32_16x16x32_bf16 v[100:103], v[208:211], v[244:247], v[100:103]\n"
"ds_read_b64_tr_b16 v[208:209], v253 offset:12328\n"
"ds_read_b64_tr_b16 v[210:211], v253 offset:13864\n"
"v_lshlrev_b32_sdwa v236, 1, v236 dst_sel:DWORD dst_unused:UNUSED_PAD src0_sel:DWORD src1_sel:WORD_1\n"
"v_mfma_f32_16x16x32_bf16 v[104:107], v[212:215], v[244:247], v[104:107]\n"
"ds_read_b64_tr_b16 v[212:213], v253 offset:12384\n"
"ds_read_b64_tr_b16 v[214:215], v253 offset:13920\n"
"ds_read_u16_d16 v236, v236 offset:18432\n"
"v_lshlrev_b32_sdwa v237, 1, v237 dst_sel:DWORD dst_unused:UNUSED_PAD src0_sel:DWORD src1_sel:WORD_1\n"
"v_mfma_f32_16x16x32_bf16 v[108:111], v[216:219], v[244:247], v[108:111]\n"
"ds_read_b64_tr_b16 v[216:217], v253 offset:12392\n"
"ds_read_b64_tr_b16 v[218:219], v253 offset:13928\n"
"ds_read_u16_d16_hi v237, v237 offset:18432\n"
"v_mfma_f32_16x16x32_bf16 v[112:115], v[220:223], v[244:247], v[112:115]\n"
"ds_read_b64_tr_b16 v[220:221], v253 offset:12448\n"
"ds_read_b64_tr_b16 v[222:223], v253 offset:13984\n"
"v_lshlrev_b32_sdwa v238, 1, v238 dst_sel:DWORD dst_unused:UNUSED_PAD src0_sel:DWORD src1_sel:WORD_1\n"
"v_mfma_f32_16x16x32_bf16 v[116:119], v[224:227], v[244:247], v[116:119]\n"
"ds_read_b64_tr_b16 v[224:225], v253 offset:12456\n"
"ds_read_b64_tr_b16 v[226:227], v253 offset:13992\n"
"ds_read_u16_d16 v238, v238 offset:18432\n"
"v_lshlrev_b32_sdwa v239, 1, v239 dst_sel:DWORD dst_unused:UNUSED_PAD src0_sel:DWORD src1_sel:WORD_1\n"
"v_mfma_f32_16x16x32_bf16 v[244:247], v[180:183], v[144:147], 0\n"
"s_waitcnt lgkmcnt(13)\n"
"ds_read_u16_d16_hi v239, v239 offset:18432\n"
"v_or_b32 v228, v228, v229\n"
"v_mfma_f32_16x16x32_bf16 v[244:247], v[184:187], v[148:151], v[244:247]\n"
"v_or_b32 v229, v230, v231\n"
"v_lshlrev_b32_sdwa v240, 1, v240 dst_sel:DWORD dst_unused:UNUSED_PAD src0_sel:DWORD src1_sel:WORD_1\n"
"v_mfma_f32_16x16x32_bf16 v[244:247], v[188:191], v[152:155], v[244:247]\n"
"ds_read_u16_d16 v240, v240 offset:18432\n"
"v_lshlrev_b32_sdwa v241, 1, v241 dst_sel:DWORD dst_unused:UNUSED_PAD src0_sel:DWORD src1_sel:WORD_1\n"
"v_mfma_f32_16x16x32_bf16 v[248:251], v[192:195], v[144:147], 0\n"
"ds_read_u16_d16_hi v241, v241 offset:18432\n"
"v_or_b32 v230, v232, v233\n"
"v_mfma_f32_16x16x32_bf16 v[248:251], v[196:199], v[148:151], v[248:251]\n"
"v_or_b32 v231, v234, v235\n"
"v_lshlrev_b32_sdwa v242, 1, v242 dst_sel:DWORD dst_unused:UNUSED_PAD src0_sel:DWORD src1_sel:WORD_1\n"
"v_mfma_f32_16x16x32_bf16 v[248:251], v[200:203], v[152:155], v[248:251]\n"
"ds_read_u16_d16 v242, v242 offset:18432\n"
"v_lshlrev_b32_sdwa v243, 1, v243 dst_sel:DWORD dst_unused:UNUSED_PAD src0_sel:DWORD src1_sel:WORD_1\n"
"s_waitcnt lgkmcnt(13)\n"
"v_mfma_f32_16x16x32_bf16 v[0:3], v[204:207], v[228:231], v[0:3]\n"
"ds_read_u16_d16_hi v243, v243 offset:18432\n"
"v_mfma_f32_16x16x32_bf16 v[4:7], v[208:211], v[228:231], v[4:7]\n"
"v_lshlrev_b32_sdwa v244, 1, v244 dst_sel:DWORD dst_unused:UNUSED_PAD src0_sel:DWORD src1_sel:WORD_1\n"
"v_mfma_f32_16x16x32_bf16 v[8:11], v[212:215], v[228:231], v[8:11]\n"
"ds_read_u16_d16 v244, v244 offset:18432\n"
"v_lshlrev_b32_sdwa v245, 1, v245 dst_sel:DWORD dst_unused:UNUSED_PAD src0_sel:DWORD src1_sel:WORD_1\n"
"s_waitcnt lgkmcnt(7)\n"
"v_mfma_f32_16x16x32_bf16 v[12:15], v[216:219], v[228:231], v[12:15]\n"
"ds_read_u16_d16_hi v245, v245 offset:18432\n"
"v_mfma_f32_16x16x32_bf16 v[16:19], v[220:223], v[228:231], v[16:19]\n"
"v_lshlrev_b32_sdwa v246, 1, v246 dst_sel:DWORD dst_unused:UNUSED_PAD src0_sel:DWORD src1_sel:WORD_1\n"
"s_add_u32 s68, s57, 0\n"
"v_cvt_pk_bf16_f32 v0, v0, v1\n"
"v_cvt_pk_bf16_f32 v1, v2, v3\n"
"v_cvt_pk_bf16_f32 v2, v4, v5\n"
"v_cvt_pk_bf16_f32 v3, v6, v7\n"
"v_mfma_f32_16x16x32_bf16 v[20:23], v[224:227], v[228:231], v[20:23]\n"
"buffer_store_dwordx4 v[0:3], v120, s[48:51], s68 offen offset:0 sc1\n"
"ds_read_u16_d16 v246, v246 offset:18432\n"
"v_lshlrev_b32_sdwa v247, 1, v247 dst_sel:DWORD dst_unused:UNUSED_PAD src0_sel:DWORD src1_sel:WORD_1\n"
"v_mfma_f32_16x16x32_bf16 v[228:231], v[180:183], v[156:159], 0\n"
"s_waitcnt lgkmcnt(3)\n"
"ds_read_u16_d16_hi v247, v247 offset:18432\n"
"v_or_b32 v236, v236, v237\n"
"v_mfma_f32_16x16x32_bf16 v[228:231], v[184:187], v[160:163], v[228:231]\n"
"v_or_b32 v237, v238, v239\n"
"v_lshlrev_b32_sdwa v248, 1, v248 dst_sel:DWORD dst_unused:UNUSED_PAD src0_sel:DWORD src1_sel:WORD_1\n"
"v_mfma_f32_16x16x32_bf16 v[228:231], v[188:191], v[164:167], v[228:231]\n"
"ds_read_u16_d16 v248, v248 offset:18432\n"
"v_lshlrev_b32_sdwa v249, 1, v249 dst_sel:DWORD dst_unused:UNUSED_PAD src0_sel:DWORD src1_sel:WORD_1\n"
"v_mfma_f32_16x16x32_bf16 v[232:235], v[192:195], v[156:159], 0\n"
"ds_read_u16_d16_hi v249, v249 offset:18432\n"
"v_or_b32 v238, v240, v241\n"
"v_mfma_f32_16x16x32_bf16 v[232:235], v[196:199], v[160:163], v[232:235]\n"
"v_or_b32 v239, v242, v243\n"
"v_lshlrev_b32_sdwa v250, 1, v250 dst_sel:DWORD dst_unused:UNUSED_PAD src0_sel:DWORD src1_sel:WORD_1\n"
"v_cvt_pk_bf16_f32 v8, v8, v9\n"
"v_cvt_pk_bf16_f32 v9, v10, v11\n"
"v_cvt_pk_bf16_f32 v10, v12, v13\n"
"v_cvt_pk_bf16_f32 v11, v14, v15\n"
"v_mfma_f32_16x16x32_bf16 v[232:235], v[200:203], v[164:167], v[232:235]\n"
"buffer_store_dwordx4 v[8:11], v120, s[48:51], s68 offen offset:64 sc1\n"
"ds_read_u16_d16 v250, v250 offset:18432\n"
"v_lshlrev_b32_sdwa v251, 1, v251 dst_sel:DWORD dst_unused:UNUSED_PAD src0_sel:DWORD src1_sel:WORD_1\n"
"v_mfma_f32_16x16x32_bf16 v[24:27], v[204:207], v[236:239], v[24:27]\n"
"ds_read_u16_d16_hi v251, v251 offset:18432\n"
"v_mfma_f32_16x16x32_bf16 v[28:31], v[208:211], v[236:239], v[28:31]\n"
"v_lshlrev_b32_sdwa v228, 1, v228 dst_sel:DWORD dst_unused:UNUSED_PAD src0_sel:DWORD src1_sel:WORD_1\n"
"v_mfma_f32_16x16x32_bf16 v[32:35], v[212:215], v[236:239], v[32:35]\n"
"ds_read_u16_d16 v228, v228 offset:18432\n"
"v_lshlrev_b32_sdwa v229, 1, v229 dst_sel:DWORD dst_unused:UNUSED_PAD src0_sel:DWORD src1_sel:WORD_1\n"
"v_mfma_f32_16x16x32_bf16 v[36:39], v[216:219], v[236:239], v[36:39]\n"
"ds_read_u16_d16_hi v229, v229 offset:18432\n"
"v_mfma_f32_16x16x32_bf16 v[40:43], v[220:223], v[236:239], v[40:43]\n"
"v_lshlrev_b32_sdwa v230, 1, v230 dst_sel:DWORD dst_unused:UNUSED_PAD src0_sel:DWORD src1_sel:WORD_1\n"
"v_cvt_pk_bf16_f32 v16, v16, v17\n"
"v_cvt_pk_bf16_f32 v17, v18, v19\n"
"v_cvt_pk_bf16_f32 v18, v20, v21\n"
"v_cvt_pk_bf16_f32 v19, v22, v23\n"
"v_mfma_f32_16x16x32_bf16 v[44:47], v[224:227], v[236:239], v[44:47]\n"
"buffer_store_dwordx4 v[16:19], v120, s[48:51], s68 offen offset:128 sc1\n"
"ds_read_u16_d16 v230, v230 offset:18432\n"
"v_lshlrev_b32_sdwa v231, 1, v231 dst_sel:DWORD dst_unused:UNUSED_PAD src0_sel:DWORD src1_sel:WORD_1\n"
"v_mfma_f32_16x16x32_bf16 v[236:239], v[180:183], v[168:171], 0\n"
"ds_read_b64_tr_b16 v[180:181], v252 offset:24320\n"
"ds_read_b64_tr_b16 v[182:183], v252 offset:27392\n"
"s_waitcnt lgkmcnt(5)\n"
"ds_read_u16_d16_hi v231, v231 offset:18432\n"
"v_or_b32 v244, v244, v245\n"
"v_mfma_f32_16x16x32_bf16 v[236:239], v[184:187], v[172:175], v[236:239]\n"
"ds_read_b64_tr_b16 v[184:185], v252 offset:25344\n"
"ds_read_b64_tr_b16 v[186:187], v252 offset:28416\n"
"v_or_b32 v245, v246, v247\n"
"v_lshlrev_b32_sdwa v232, 1, v232 dst_sel:DWORD dst_unused:UNUSED_PAD src0_sel:DWORD src1_sel:WORD_1\n"
"v_mfma_f32_16x16x32_bf16 v[236:239], v[188:191], v[176:179], v[236:239]\n"
"ds_read_b64_tr_b16 v[188:189], v252 offset:26368\n"
"ds_read_b64_tr_b16 v[190:191], v252 offset:29440\n"
"ds_read_u16_d16 v232, v232 offset:18432\n"
"v_lshlrev_b32_sdwa v233, 1, v233 dst_sel:DWORD dst_unused:UNUSED_PAD src0_sel:DWORD src1_sel:WORD_1\n"
"v_mfma_f32_16x16x32_bf16 v[240:243], v[192:195], v[168:171], 0\n"
"ds_read_b64_tr_b16 v[192:193], v252 offset:24832\n"
"ds_read_b64_tr_b16 v[194:195], v252 offset:27904\n"
"ds_read_u16_d16_hi v233, v233 offset:18432\n"
"v_or_b32 v246, v248, v249\n"
"v_mfma_f32_16x16x32_bf16 v[240:243], v[196:199], v[172:175], v[240:243]\n"
"ds_read_b64_tr_b16 v[196:197], v252 offset:25856\n"
"ds_read_b64_tr_b16 v[198:199], v252 offset:28928\n"
"v_or_b32 v247, v250, v251\n"
"v_lshlrev_b32_sdwa v234, 1, v234 dst_sel:DWORD dst_unused:UNUSED_PAD src0_sel:DWORD src1_sel:WORD_1\n"
"s_add_u32 s68, s57, 384\n"
"v_cvt_pk_bf16_f32 v24, v24, v25\n"
"v_cvt_pk_bf16_f32 v25, v26, v27\n"
"v_cvt_pk_bf16_f32 v26, v28, v29\n"
"v_cvt_pk_bf16_f32 v27, v30, v31\n"
"v_mfma_f32_16x16x32_bf16 v[240:243], v[200:203], v[176:179], v[240:243]\n"
"buffer_store_dwordx4 v[24:27], v120, s[48:51], s68 offen offset:0 sc1\n"
"ds_read_b64_tr_b16 v[200:201], v252 offset:26880\n"
"ds_read_b64_tr_b16 v[202:203], v252 offset:29952\n"
"ds_read_u16_d16 v234, v234 offset:18432\n"
"v_lshlrev_b32_sdwa v235, 1, v235 dst_sel:DWORD dst_unused:UNUSED_PAD src0_sel:DWORD src1_sel:WORD_1\n"
"v_mfma_f32_16x16x32_bf16 v[48:51], v[204:207], v[244:247], v[48:51]\n"
"ds_read_u16_d16_hi v235, v235 offset:18432\n"
"v_mfma_f32_16x16x32_bf16 v[52:55], v[208:211], v[244:247], v[52:55]\n"
"v_lshlrev_b32_sdwa v236, 1, v236 dst_sel:DWORD dst_unused:UNUSED_PAD src0_sel:DWORD src1_sel:WORD_1\n"
"v_mfma_f32_16x16x32_bf16 v[56:59], v[212:215], v[244:247], v[56:59]\n"
"ds_read_u16_d16 v236, v236 offset:18432\n"
"v_lshlrev_b32_sdwa v237, 1, v237 dst_sel:DWORD dst_unused:UNUSED_PAD src0_sel:DWORD src1_sel:WORD_1\n"
"v_mfma_f32_16x16x32_bf16 v[60:63], v[216:219], v[244:247], v[60:63]\n"
"ds_read_u16_d16_hi v237, v237 offset:18432\n"
"v_mfma_f32_16x16x32_bf16 v[64:67], v[220:223], v[244:247], v[64:67]\n"
"v_lshlrev_b32_sdwa v238, 1, v238 dst_sel:DWORD dst_unused:UNUSED_PAD src0_sel:DWORD src1_sel:WORD_1\n"
"v_cvt_pk_bf16_f32 v32, v32, v33\n"
"v_cvt_pk_bf16_f32 v33, v34, v35\n"
"v_cvt_pk_bf16_f32 v34, v36, v37\n"
"v_cvt_pk_bf16_f32 v35, v38, v39\n"
"v_mfma_f32_16x16x32_bf16 v[68:71], v[224:227], v[244:247], v[68:71]\n"
"buffer_store_dwordx4 v[32:35], v120, s[48:51], s68 offen offset:64 sc1\n"
"ds_read_u16_d16 v238, v238 offset:18432\n"
"v_lshlrev_b32_sdwa v239, 1, v239 dst_sel:DWORD dst_unused:UNUSED_PAD src0_sel:DWORD src1_sel:WORD_1\n"
"s_waitcnt lgkmcnt(13)\n"
"v_mfma_f32_16x16x32_bf16 v[244:247], v[180:183], v[144:147], 0\n"
"s_waitcnt lgkmcnt(3)\n"
"ds_read_u16_d16_hi v239, v239 offset:18432\n"
"v_or_b32 v228, v228, v229\n"
"v_mfma_f32_16x16x32_bf16 v[244:247], v[184:187], v[148:151], v[244:247]\n"
"v_or_b32 v229, v230, v231\n"
"v_lshlrev_b32_sdwa v240, 1, v240 dst_sel:DWORD dst_unused:UNUSED_PAD src0_sel:DWORD src1_sel:WORD_1\n"
"v_mfma_f32_16x16x32_bf16 v[244:247], v[188:191], v[152:155], v[244:247]\n"
"ds_read_u16_d16 v240, v240 offset:18432\n"
"v_lshlrev_b32_sdwa v241, 1, v241 dst_sel:DWORD dst_unused:UNUSED_PAD src0_sel:DWORD src1_sel:WORD_1\n"
"v_mfma_f32_16x16x32_bf16 v[248:251], v[192:195], v[144:147], 0\n"
"ds_read_u16_d16_hi v241, v241 offset:18432\n"
"v_or_b32 v230, v232, v233\n"
"v_mfma_f32_16x16x32_bf16 v[248:251], v[196:199], v[148:151], v[248:251]\n"
"v_or_b32 v231, v234, v235\n"
"v_lshlrev_b32_sdwa v242, 1, v242 dst_sel:DWORD dst_unused:UNUSED_PAD src0_sel:DWORD src1_sel:WORD_1\n"
"v_cvt_pk_bf16_f32 v40, v40, v41\n"
"v_cvt_pk_bf16_f32 v41, v42, v43\n"
"v_cvt_pk_bf16_f32 v42, v44, v45\n"
"v_cvt_pk_bf16_f32 v43, v46, v47\n"
"v_mfma_f32_16x16x32_bf16 v[248:251], v[200:203], v[152:155], v[248:251]\n"
"buffer_store_dwordx4 v[40:43], v120, s[48:51], s68 offen offset:128 sc1\n"
"ds_read_u16_d16 v242, v242 offset:18432\n"
"v_lshlrev_b32_sdwa v243, 1, v243 dst_sel:DWORD dst_unused:UNUSED_PAD src0_sel:DWORD src1_sel:WORD_1\n"
"v_mfma_f32_16x16x32_bf16 v[72:75], v[204:207], v[228:231], v[72:75]\n"
"ds_read_u16_d16_hi v243, v243 offset:18432\n"
"v_mfma_f32_16x16x32_bf16 v[76:79], v[208:211], v[228:231], v[76:79]\n"
"v_lshlrev_b32_sdwa v244, 1, v244 dst_sel:DWORD dst_unused:UNUSED_PAD src0_sel:DWORD src1_sel:WORD_1\n"
"v_mfma_f32_16x16x32_bf16 v[80:83], v[212:215], v[228:231], v[80:83]\n"
"ds_read_u16_d16 v244, v244 offset:18432\n"
"v_lshlrev_b32_sdwa v245, 1, v245 dst_sel:DWORD dst_unused:UNUSED_PAD src0_sel:DWORD src1_sel:WORD_1\n"
"v_mfma_f32_16x16x32_bf16 v[84:87], v[216:219], v[228:231], v[84:87]\n"
"ds_read_u16_d16_hi v245, v245 offset:18432\n"
"v_mfma_f32_16x16x32_bf16 v[88:91], v[220:223], v[228:231], v[88:91]\n"
"v_lshlrev_b32_sdwa v246, 1, v246 dst_sel:DWORD dst_unused:UNUSED_PAD src0_sel:DWORD src1_sel:WORD_1\n"
"v_mfma_f32_16x16x32_bf16 v[92:95], v[224:227], v[228:231], v[92:95]\n"
"ds_read_u16_d16 v246, v246 offset:18432\n"
"v_lshlrev_b32_sdwa v247, 1, v247 dst_sel:DWORD dst_unused:UNUSED_PAD src0_sel:DWORD src1_sel:WORD_1\n"
"v_mfma_f32_16x16x32_bf16 v[228:231], v[180:183], v[156:159], 0\n"
"s_waitcnt lgkmcnt(3)\n"
"ds_read_u16_d16_hi v247, v247 offset:18432\n"
"v_or_b32 v236, v236, v237\n"
"v_mfma_f32_16x16x32_bf16 v[228:231], v[184:187], v[160:163], v[228:231]\n"
"v_or_b32 v237, v238, v239\n"
"v_lshlrev_b32_sdwa v248, 1, v248 dst_sel:DWORD dst_unused:UNUSED_PAD src0_sel:DWORD src1_sel:WORD_1\n"
"v_mfma_f32_16x16x32_bf16 v[228:231], v[188:191], v[164:167], v[228:231]\n"
"ds_read_u16_d16 v248, v248 offset:18432\n"
"v_lshlrev_b32_sdwa v249, 1, v249 dst_sel:DWORD dst_unused:UNUSED_PAD src0_sel:DWORD src1_sel:WORD_1\n"
"v_mfma_f32_16x16x32_bf16 v[232:235], v[192:195], v[156:159], 0\n"
"ds_read_u16_d16_hi v249, v249 offset:18432\n"
"v_or_b32 v238, v240, v241\n"
"v_mfma_f32_16x16x32_bf16 v[232:235], v[196:199], v[160:163], v[232:235]\n"
"v_or_b32 v239, v242, v243\n"
"v_lshlrev_b32_sdwa v250, 1, v250 dst_sel:DWORD dst_unused:UNUSED_PAD src0_sel:DWORD src1_sel:WORD_1\n"
"v_mfma_f32_16x16x32_bf16 v[232:235], v[200:203], v[164:167], v[232:235]\n"
"ds_read_u16_d16 v250, v250 offset:18432\n"
"v_lshlrev_b32_sdwa v251, 1, v251 dst_sel:DWORD dst_unused:UNUSED_PAD src0_sel:DWORD src1_sel:WORD_1\n"
"v_mfma_f32_16x16x32_bf16 v[96:99], v[204:207], v[236:239], v[96:99]\n"
"ds_read_b64_tr_b16 v[204:205], v253 offset:18480\n"
"ds_read_b64_tr_b16 v[206:207], v253 offset:20016\n"
"ds_read_u16_d16_hi v251, v251 offset:18432\n"
"v_mfma_f32_16x16x32_bf16 v[100:103], v[208:211], v[236:239], v[100:103]\n"
"ds_read_b64_tr_b16 v[208:209], v253 offset:18488\n"
"ds_read_b64_tr_b16 v[210:211], v253 offset:20024\n"
"v_lshlrev_b32_sdwa v228, 1, v228 dst_sel:DWORD dst_unused:UNUSED_PAD src0_sel:DWORD src1_sel:WORD_1\n"
"v_mfma_f32_16x16x32_bf16 v[104:107], v[212:215], v[236:239], v[104:107]\n"
"ds_read_b64_tr_b16 v[212:213], v253 offset:18544\n"
"ds_read_b64_tr_b16 v[214:215], v253 offset:20080\n"
"ds_read_u16_d16 v228, v228 offset:18432\n"
"v_lshlrev_b32_sdwa v229, 1, v229 dst_sel:DWORD dst_unused:UNUSED_PAD src0_sel:DWORD src1_sel:WORD_1\n"
"v_mfma_f32_16x16x32_bf16 v[108:111], v[216:219], v[236:239], v[108:111]\n"
"ds_read_b64_tr_b16 v[216:217], v253 offset:18552\n"
"ds_read_b64_tr_b16 v[218:219], v253 offset:20088\n"
"ds_read_u16_d16_hi v229, v229 offset:18432\n"
"v_mfma_f32_16x16x32_bf16 v[112:115], v[220:223], v[236:239], v[112:115]\n"
"ds_read_b64_tr_b16 v[220:221], v253 offset:18608\n"
"ds_read_b64_tr_b16 v[222:223], v253 offset:20144\n"
"v_lshlrev_b32_sdwa v230, 1, v230 dst_sel:DWORD dst_unused:UNUSED_PAD src0_sel:DWORD src1_sel:WORD_1\n"
"v_mfma_f32_16x16x32_bf16 v[116:119], v[224:227], v[236:239], v[116:119]\n"
"ds_read_b64_tr_b16 v[224:225], v253 offset:18616\n"
"ds_read_b64_tr_b16 v[226:227], v253 offset:20152\n"
"ds_read_u16_d16 v230, v230 offset:18432\n"
"v_lshlrev_b32_sdwa v231, 1, v231 dst_sel:DWORD dst_unused:UNUSED_PAD src0_sel:DWORD src1_sel:WORD_1\n"
"v_mfma_f32_16x16x32_bf16 v[236:239], v[180:183], v[168:171], 0\n"
"ds_read_b64_tr_b16 v[180:181], v252 offset:30464\n"
"ds_read_b64_tr_b16 v[182:183], v252 offset:33536\n"
"s_waitcnt lgkmcnt(14)\n"
"ds_read_u16_d16_hi v231, v231 offset:18432\n"
"v_or_b32 v244, v244, v245\n"
"v_mfma_f32_16x16x32_bf16 v[236:239], v[184:187], v[172:175], v[236:239]\n"
"ds_read_b64_tr_b16 v[184:185], v252 offset:31488\n"
"ds_read_b64_tr_b16 v[186:187], v252 offset:34560\n"
"v_or_b32 v245, v246, v247\n"
"v_lshlrev_b32_sdwa v232, 1, v232 dst_sel:DWORD dst_unused:UNUSED_PAD src0_sel:DWORD src1_sel:WORD_1\n"
"v_mfma_f32_16x16x32_bf16 v[236:239], v[188:191], v[176:179], v[236:239]\n"
"ds_read_b64_tr_b16 v[188:189], v252 offset:32512\n"
"ds_read_b64_tr_b16 v[190:191], v252 offset:35584\n"
"ds_read_u16_d16 v232, v232 offset:18432\n"
"v_lshlrev_b32_sdwa v233, 1, v233 dst_sel:DWORD dst_unused:UNUSED_PAD src0_sel:DWORD src1_sel:WORD_1\n"
"v_mfma_f32_16x16x32_bf16 v[240:243], v[192:195], v[168:171], 0\n"
"ds_read_b64_tr_b16 v[192:193], v252 offset:30976\n"
"ds_read_b64_tr_b16 v[194:195], v252 offset:34048\n"
"ds_read_u16_d16_hi v233, v233 offset:18432\n"
"v_or_b32 v246, v248, v249\n"
"v_mfma_f32_16x16x32_bf16 v[240:243], v[196:199], v[172:175], v[240:243]\n"
"ds_read_b64_tr_b16 v[196:197], v252 offset:32000\n"
"ds_read_b64_tr_b16 v[198:199], v252 offset:35072\n"
"v_or_b32 v247, v250, v251\n"
"v_lshlrev_b32_sdwa v234, 1, v234 dst_sel:DWORD dst_unused:UNUSED_PAD src0_sel:DWORD src1_sel:WORD_1\n"
"v_mfma_f32_16x16x32_bf16 v[240:243], v[200:203], v[176:179], v[240:243]\n"
"ds_read_b64_tr_b16 v[200:201], v252 offset:33024\n"
"ds_read_b64_tr_b16 v[202:203], v252 offset:36096\n"
"ds_read_u16_d16 v234, v234 offset:18432\n"
"v_lshlrev_b32_sdwa v235, 1, v235 dst_sel:DWORD dst_unused:UNUSED_PAD src0_sel:DWORD src1_sel:WORD_1\n"
"s_waitcnt lgkmcnt(14)\n"
"v_mfma_f32_16x16x32_bf16 v[48:51], v[204:207], v[244:247], v[48:51]\n"
"ds_read_u16_d16_hi v235, v235 offset:18432\n"
"v_mfma_f32_16x16x32_bf16 v[52:55], v[208:211], v[244:247], v[52:55]\n"
"v_lshlrev_b32_sdwa v236, 1, v236 dst_sel:DWORD dst_unused:UNUSED_PAD src0_sel:DWORD src1_sel:WORD_1\n"
"v_mfma_f32_16x16x32_bf16 v[56:59], v[212:215], v[244:247], v[56:59]\n"
"ds_read_u16_d16 v236, v236 offset:18432\n"
"v_lshlrev_b32_sdwa v237, 1, v237 dst_sel:DWORD dst_unused:UNUSED_PAD src0_sel:DWORD src1_sel:WORD_1\n"
"v_mfma_f32_16x16x32_bf16 v[60:63], v[216:219], v[244:247], v[60:63]\n"
"ds_read_u16_d16_hi v237, v237 offset:18432\n"
"v_mfma_f32_16x16x32_bf16 v[64:67], v[220:223], v[244:247], v[64:67]\n"
"v_lshlrev_b32_sdwa v238, 1, v238 dst_sel:DWORD dst_unused:UNUSED_PAD src0_sel:DWORD src1_sel:WORD_1\n"
"s_mov_b32 s68, 589824\n"
"v_cvt_pk_bf16_f32 v48, v48, v49\n"
"v_cvt_pk_bf16_f32 v49, v50, v51\n"
"v_cvt_pk_bf16_f32 v50, v52, v53\n"
"v_cvt_pk_bf16_f32 v51, v54, v55\n"
"v_mfma_f32_16x16x32_bf16 v[68:71], v[224:227], v[244:247], v[68:71]\n"
"buffer_store_dwordx4 v[48:51], v120, s[48:51], s68 offen offset:0 sc1\n"
"ds_read_u16_d16 v238, v238 offset:18432\n"
"v_lshlrev_b32_sdwa v239, 1, v239 dst_sel:DWORD dst_unused:UNUSED_PAD src0_sel:DWORD src1_sel:WORD_1\n"
"s_waitcnt lgkmcnt(13)\n"
"v_mfma_f32_16x16x32_bf16 v[244:247], v[180:183], v[156:159], 0\n"
"s_waitcnt lgkmcnt(3)\n"
"ds_read_u16_d16_hi v239, v239 offset:18432\n"
"v_or_b32 v228, v228, v229\n"
"v_mfma_f32_16x16x32_bf16 v[244:247], v[184:187], v[160:163], v[244:247]\n"
"v_or_b32 v229, v230, v231\n"
"v_lshlrev_b32_sdwa v240, 1, v240 dst_sel:DWORD dst_unused:UNUSED_PAD src0_sel:DWORD src1_sel:WORD_1\n"
"v_mfma_f32_16x16x32_bf16 v[244:247], v[188:191], v[164:167], v[244:247]\n"
"ds_read_u16_d16 v240, v240 offset:18432\n"
"v_lshlrev_b32_sdwa v241, 1, v241 dst_sel:DWORD dst_unused:UNUSED_PAD src0_sel:DWORD src1_sel:WORD_1\n"
"v_mfma_f32_16x16x32_bf16 v[248:251], v[192:195], v[156:159], 0\n"
"ds_read_u16_d16_hi v241, v241 offset:18432\n"
"v_or_b32 v230, v232, v233\n"
"v_mfma_f32_16x16x32_bf16 v[248:251], v[196:199], v[160:163], v[248:251]\n"
"v_or_b32 v231, v234, v235\n"
"v_lshlrev_b32_sdwa v242, 1, v242 dst_sel:DWORD dst_unused:UNUSED_PAD src0_sel:DWORD src1_sel:WORD_1\n"
"v_cvt_pk_bf16_f32 v56, v56, v57\n"
"v_cvt_pk_bf16_f32 v57, v58, v59\n"
"v_cvt_pk_bf16_f32 v58, v60, v61\n"
"v_cvt_pk_bf16_f32 v59, v62, v63\n"
"v_mfma_f32_16x16x32_bf16 v[248:251], v[200:203], v[164:167], v[248:251]\n"
"buffer_store_dwordx4 v[56:59], v120, s[48:51], s68 offen offset:64 sc1\n"
"ds_read_u16_d16 v242, v242 offset:18432\n"
"v_lshlrev_b32_sdwa v243, 1, v243 dst_sel:DWORD dst_unused:UNUSED_PAD src0_sel:DWORD src1_sel:WORD_1\n"
"v_mfma_f32_16x16x32_bf16 v[72:75], v[204:207], v[228:231], v[72:75]\n"
"ds_read_u16_d16_hi v243, v243 offset:18432\n"
"v_mfma_f32_16x16x32_bf16 v[76:79], v[208:211], v[228:231], v[76:79]\n"
"v_lshlrev_b32_sdwa v244, 1, v244 dst_sel:DWORD dst_unused:UNUSED_PAD src0_sel:DWORD src1_sel:WORD_1\n"
"v_mfma_f32_16x16x32_bf16 v[80:83], v[212:215], v[228:231], v[80:83]\n"
"ds_read_u16_d16 v244, v244 offset:18432\n"
"v_lshlrev_b32_sdwa v245, 1, v245 dst_sel:DWORD dst_unused:UNUSED_PAD src0_sel:DWORD src1_sel:WORD_1\n"
"v_mfma_f32_16x16x32_bf16 v[84:87], v[216:219], v[228:231], v[84:87]\n"
"ds_read_u16_d16_hi v245, v245 offset:18432\n"
"v_mfma_f32_16x16x32_bf16 v[88:91], v[220:223], v[228:231], v[88:91]\n"
"v_lshlrev_b32_sdwa v246, 1, v246 dst_sel:DWORD dst_unused:UNUSED_PAD src0_sel:DWORD src1_sel:WORD_1\n"
"v_cvt_pk_bf16_f32 v64, v64, v65\n"
"v_cvt_pk_bf16_f32 v65, v66, v67\n"
"v_cvt_pk_bf16_f32 v66, v68, v69\n"
"v_cvt_pk_bf16_f32 v67, v70, v71\n"
"v_mfma_f32_16x16x32_bf16 v[92:95], v[224:227], v[228:231], v[92:95]\n"
"buffer_store_dwordx4 v[64:67], v120, s[48:51], s68 offen offset:128 sc1\n"
"ds_read_u16_d16 v246, v246 offset:18432\n"
"v_lshlrev_b32_sdwa v247, 1, v247 dst_sel:DWORD dst_unused:UNUSED_PAD src0_sel:DWORD src1_sel:WORD_1\n"
"v_mfma_f32_16x16x32_bf16 v[228:231], v[180:183], v[168:171], 0\n"
"ds_read_b64_tr_b16 v[180:181], v252 offset:36608\n"
"ds_read_b64_tr_b16 v[182:183], v252 offset:39680\n"
"s_waitcnt lgkmcnt(5)\n"
"ds_read_u16_d16_hi v247, v247 offset:18432\n"
"v_or_b32 v236, v236, v237\n"
"v_mfma_f32_16x16x32_bf16 v[228:231], v[184:187], v[172:175], v[228:231]\n"
"ds_read_b64_tr_b16 v[184:185], v252 offset:37632\n"
"ds_read_b64_tr_b16 v[186:187], v252 offset:40704\n"
"v_or_b32 v237, v238, v239\n"
"v_lshlrev_b32_sdwa v248, 1, v248 dst_sel:DWORD dst_unused:UNUSED_PAD src0_sel:DWORD src1_sel:WORD_1\n"
"v_mfma_f32_16x16x32_bf16 v[228:231], v[188:191], v[176:179], v[228:231]\n"
"ds_read_b64_tr_b16 v[188:189], v252 offset:38656\n"
"ds_read_b64_tr_b16 v[190:191], v252 offset:41728\n"
"ds_read_u16_d16 v248, v248 offset:18432\n"
"v_lshlrev_b32_sdwa v249, 1, v249 dst_sel:DWORD dst_unused:UNUSED_PAD src0_sel:DWORD src1_sel:WORD_1\n"
"v_mfma_f32_16x16x32_bf16 v[232:235], v[192:195], v[168:171], 0\n"
"ds_read_b64_tr_b16 v[192:193], v252 offset:37120\n"
"ds_read_b64_tr_b16 v[194:195], v252 offset:40192\n"
"ds_read_u16_d16_hi v249, v249 offset:18432\n"
"v_or_b32 v238, v240, v241\n"
"v_mfma_f32_16x16x32_bf16 v[232:235], v[196:199], v[172:175], v[232:235]\n"
"ds_read_b64_tr_b16 v[196:197], v252 offset:38144\n"
"ds_read_b64_tr_b16 v[198:199], v252 offset:41216\n"
"v_or_b32 v239, v242, v243\n"
"v_lshlrev_b32_sdwa v250, 1, v250 dst_sel:DWORD dst_unused:UNUSED_PAD src0_sel:DWORD src1_sel:WORD_1\n"
"v_mfma_f32_16x16x32_bf16 v[232:235], v[200:203], v[176:179], v[232:235]\n"
"ds_read_b64_tr_b16 v[200:201], v252 offset:39168\n"
"ds_read_b64_tr_b16 v[202:203], v252 offset:42240\n"
"ds_read_u16_d16 v250, v250 offset:18432\n"
"v_lshlrev_b32_sdwa v251, 1, v251 dst_sel:DWORD dst_unused:UNUSED_PAD src0_sel:DWORD src1_sel:WORD_1\n"
"v_mfma_f32_16x16x32_bf16 v[96:99], v[204:207], v[236:239], v[96:99]\n"
"ds_read_b64_tr_b16 v[204:205], v253 offset:24640\n"
"ds_read_b64_tr_b16 v[206:207], v253 offset:26176\n"
"ds_read_u16_d16_hi v251, v251 offset:18432\n"
"v_mfma_f32_16x16x32_bf16 v[100:103], v[208:211], v[236:239], v[100:103]\n"
"ds_read_b64_tr_b16 v[208:209], v253 offset:24648\n"
"ds_read_b64_tr_b16 v[210:211], v253 offset:26184\n"
"v_lshlrev_b32_sdwa v228, 1, v228 dst_sel:DWORD dst_unused:UNUSED_PAD src0_sel:DWORD src1_sel:WORD_1\n"
"v_mfma_f32_16x16x32_bf16 v[104:107], v[212:215], v[236:239], v[104:107]\n"
"ds_read_b64_tr_b16 v[212:213], v253 offset:24704\n"
"ds_read_b64_tr_b16 v[214:215], v253 offset:26240\n"
"ds_read_u16_d16 v228, v228 offset:18432\n"
"v_lshlrev_b32_sdwa v229, 1, v229 dst_sel:DWORD dst_unused:UNUSED_PAD src0_sel:DWORD src1_sel:WORD_1\n"
"v_mfma_f32_16x16x32_bf16 v[108:111], v[216:219], v[236:239], v[108:111]\n"
"ds_read_b64_tr_b16 v[216:217], v253 offset:24712\n"
"ds_read_b64_tr_b16 v[218:219], v253 offset:26248\n"
"ds_read_u16_d16_hi v229, v229 offset:18432\n"
"v_mfma_f32_16x16x32_bf16 v[112:115], v[220:223], v[236:239], v[112:115]\n"
"ds_read_b64_tr_b16 v[220:221], v253 offset:24768\n"
"ds_read_b64_tr_b16 v[222:223], v253 offset:26304\n"
"v_lshlrev_b32_sdwa v230, 1, v230 dst_sel:DWORD dst_unused:UNUSED_PAD src0_sel:DWORD src1_sel:WORD_1\n"
"v_mfma_f32_16x16x32_bf16 v[116:119], v[224:227], v[236:239], v[116:119]\n"
"ds_read_b64_tr_b16 v[224:225], v253 offset:24776\n"
"ds_read_b64_tr_b16 v[226:227], v253 offset:26312\n"
"ds_read_u16_d16 v230, v230 offset:18432\n"
"v_lshlrev_b32_sdwa v231, 1, v231 dst_sel:DWORD dst_unused:UNUSED_PAD src0_sel:DWORD src1_sel:WORD_1\n"
"s_waitcnt lgkmcnt(14)\n"
"v_mfma_f32_16x16x32_bf16 v[236:239], v[180:183], v[168:171], 0\n"
"ds_read_b64_tr_b16 v[180:181], v252 offset:42752\n"
"ds_read_b64_tr_b16 v[182:183], v252 offset:45824\n"
"s_waitcnt lgkmcnt(14)\n"
"ds_read_u16_d16_hi v231, v231 offset:18432\n"
"v_or_b32 v244, v244, v245\n"
"v_mfma_f32_16x16x32_bf16 v[236:239], v[184:187], v[172:175], v[236:239]\n"
"ds_read_b64_tr_b16 v[184:185], v252 offset:43776\n"
"ds_read_b64_tr_b16 v[186:187], v252 offset:46848\n"
"v_or_b32 v245, v246, v247\n"
"v_lshlrev_b32_sdwa v232, 1, v232 dst_sel:DWORD dst_unused:UNUSED_PAD src0_sel:DWORD src1_sel:WORD_1\n"
"v_mfma_f32_16x16x32_bf16 v[236:239], v[188:191], v[176:179], v[236:239]\n"
"ds_read_b64_tr_b16 v[188:189], v252 offset:44800\n"
"ds_read_b64_tr_b16 v[190:191], v252 offset:47872\n"
"ds_read_u16_d16 v232, v232 offset:18432\n"
"v_lshlrev_b32_sdwa v233, 1, v233 dst_sel:DWORD dst_unused:UNUSED_PAD src0_sel:DWORD src1_sel:WORD_1\n"
"v_mfma_f32_16x16x32_bf16 v[240:243], v[192:195], v[168:171], 0\n"
"ds_read_b64_tr_b16 v[192:193], v252 offset:43264\n"
"ds_read_b64_tr_b16 v[194:195], v252 offset:46336\n"
"ds_read_u16_d16_hi v233, v233 offset:18432\n"
"v_or_b32 v246, v248, v249\n"
"v_mfma_f32_16x16x32_bf16 v[240:243], v[196:199], v[172:175], v[240:243]\n"
"ds_read_b64_tr_b16 v[196:197], v252 offset:44288\n"
"ds_read_b64_tr_b16 v[198:199], v252 offset:47360\n"
"v_or_b32 v247, v250, v251\n"
"v_lshlrev_b32_sdwa v234, 1, v234 dst_sel:DWORD dst_unused:UNUSED_PAD src0_sel:DWORD src1_sel:WORD_1\n"
"v_mfma_f32_16x16x32_bf16 v[240:243], v[200:203], v[176:179], v[240:243]\n"
"ds_read_b64_tr_b16 v[200:201], v252 offset:45312\n"
"ds_read_b64_tr_b16 v[202:203], v252 offset:48384\n"
"ds_read_u16_d16 v234, v234 offset:18432\n"
"v_lshlrev_b32_sdwa v235, 1, v235 dst_sel:DWORD dst_unused:UNUSED_PAD src0_sel:DWORD src1_sel:WORD_1\n"
"s_waitcnt lgkmcnt(14)\n"
"v_mfma_f32_16x16x32_bf16 v[72:75], v[204:207], v[244:247], v[72:75]\n"
"ds_read_u16_d16_hi v235, v235 offset:18432\n"
"v_mfma_f32_16x16x32_bf16 v[76:79], v[208:211], v[244:247], v[76:79]\n"
"v_lshlrev_b32_sdwa v236, 1, v236 dst_sel:DWORD dst_unused:UNUSED_PAD src0_sel:DWORD src1_sel:WORD_1\n"
"v_mfma_f32_16x16x32_bf16 v[80:83], v[212:215], v[244:247], v[80:83]\n"
"ds_read_u16_d16 v236, v236 offset:18432\n"
"v_lshlrev_b32_sdwa v237, 1, v237 dst_sel:DWORD dst_unused:UNUSED_PAD src0_sel:DWORD src1_sel:WORD_1\n"
"v_mfma_f32_16x16x32_bf16 v[84:87], v[216:219], v[244:247], v[84:87]\n"
"ds_read_u16_d16_hi v237, v237 offset:18432\n"
"v_mfma_f32_16x16x32_bf16 v[88:91], v[220:223], v[244:247], v[88:91]\n"
"v_lshlrev_b32_sdwa v238, 1, v238 dst_sel:DWORD dst_unused:UNUSED_PAD src0_sel:DWORD src1_sel:WORD_1\n"
"s_mov_b32 s68, 590208\n"
"v_cvt_pk_bf16_f32 v72, v72, v73\n"
"v_cvt_pk_bf16_f32 v73, v74, v75\n"
"v_cvt_pk_bf16_f32 v74, v76, v77\n"
"v_cvt_pk_bf16_f32 v75, v78, v79\n"
"v_mfma_f32_16x16x32_bf16 v[92:95], v[224:227], v[244:247], v[92:95]\n"
"buffer_store_dwordx4 v[72:75], v120, s[48:51], s68 offen offset:0 sc1\n"
"ds_read_u16_d16 v238, v238 offset:18432\n"
"v_lshlrev_b32_sdwa v239, 1, v239 dst_sel:DWORD dst_unused:UNUSED_PAD src0_sel:DWORD src1_sel:WORD_1\n"
"s_waitcnt lgkmcnt(13)\n"
"v_mfma_f32_16x16x32_bf16 v[244:247], v[180:183], v[168:171], 0\n"
"s_waitcnt lgkmcnt(3)\n"
"ds_read_u16_d16_hi v239, v239 offset:18432\n"
"v_or_b32 v228, v228, v229\n"
"v_mfma_f32_16x16x32_bf16 v[244:247], v[184:187], v[172:175], v[244:247]\n"
"v_or_b32 v229, v230, v231\n"
"v_lshlrev_b32_sdwa v240, 1, v240 dst_sel:DWORD dst_unused:UNUSED_PAD src0_sel:DWORD src1_sel:WORD_1\n"
"v_mfma_f32_16x16x32_bf16 v[244:247], v[188:191], v[176:179], v[244:247]\n"
"ds_read_u16_d16 v240, v240 offset:18432\n"
"v_lshlrev_b32_sdwa v241, 1, v241 dst_sel:DWORD dst_unused:UNUSED_PAD src0_sel:DWORD src1_sel:WORD_1\n"
"v_mfma_f32_16x16x32_bf16 v[248:251], v[192:195], v[168:171], 0\n"
"ds_read_u16_d16_hi v241, v241 offset:18432\n"
"v_or_b32 v230, v232, v233\n"
"v_mfma_f32_16x16x32_bf16 v[248:251], v[196:199], v[172:175], v[248:251]\n"
"v_or_b32 v231, v234, v235\n"
"v_lshlrev_b32_sdwa v242, 1, v242 dst_sel:DWORD dst_unused:UNUSED_PAD src0_sel:DWORD src1_sel:WORD_1\n"
"v_cvt_pk_bf16_f32 v80, v80, v81\n"
"v_cvt_pk_bf16_f32 v81, v82, v83\n"
"v_cvt_pk_bf16_f32 v82, v84, v85\n"
"v_cvt_pk_bf16_f32 v83, v86, v87\n"
"v_mfma_f32_16x16x32_bf16 v[248:251], v[200:203], v[176:179], v[248:251]\n"
"buffer_store_dwordx4 v[80:83], v120, s[48:51], s68 offen offset:64 sc1\n"
"ds_read_u16_d16 v242, v242 offset:18432\n"
"v_lshlrev_b32_sdwa v243, 1, v243 dst_sel:DWORD dst_unused:UNUSED_PAD src0_sel:DWORD src1_sel:WORD_1\n"
"v_mfma_f32_16x16x32_bf16 v[96:99], v[204:207], v[228:231], v[96:99]\n"
"ds_read_b64_tr_b16 v[204:205], v253 offset:30800\n"
"ds_read_b64_tr_b16 v[206:207], v253 offset:32336\n"
"ds_read_u16_d16_hi v243, v243 offset:18432\n"
"v_mfma_f32_16x16x32_bf16 v[100:103], v[208:211], v[228:231], v[100:103]\n"
"ds_read_b64_tr_b16 v[208:209], v253 offset:30808\n"
"ds_read_b64_tr_b16 v[210:211], v253 offset:32344\n"
"v_lshlrev_b32_sdwa v244, 1, v244 dst_sel:DWORD dst_unused:UNUSED_PAD src0_sel:DWORD src1_sel:WORD_1\n"
"v_mfma_f32_16x16x32_bf16 v[104:107], v[212:215], v[228:231], v[104:107]\n"
"ds_read_b64_tr_b16 v[212:213], v253 offset:30864\n"
"ds_read_b64_tr_b16 v[214:215], v253 offset:32400\n"
"ds_read_u16_d16 v244, v244 offset:18432\n"
"v_lshlrev_b32_sdwa v245, 1, v245 dst_sel:DWORD dst_unused:UNUSED_PAD src0_sel:DWORD src1_sel:WORD_1\n"
"v_mfma_f32_16x16x32_bf16 v[108:111], v[216:219], v[228:231], v[108:111]\n"
"ds_read_b64_tr_b16 v[216:217], v253 offset:30872\n"
"ds_read_b64_tr_b16 v[218:219], v253 offset:32408\n"
"ds_read_u16_d16_hi v245, v245 offset:18432\n"
"v_mfma_f32_16x16x32_bf16 v[112:115], v[220:223], v[228:231], v[112:115]\n"
"ds_read_b64_tr_b16 v[220:221], v253 offset:30928\n"
"ds_read_b64_tr_b16 v[222:223], v253 offset:32464\n"
"v_lshlrev_b32_sdwa v246, 1, v246 dst_sel:DWORD dst_unused:UNUSED_PAD src0_sel:DWORD src1_sel:WORD_1\n"
"v_cvt_pk_bf16_f32 v88, v88, v89\n"
"v_cvt_pk_bf16_f32 v89, v90, v91\n"
"v_cvt_pk_bf16_f32 v90, v92, v93\n"
"v_cvt_pk_bf16_f32 v91, v94, v95\n"
"v_mfma_f32_16x16x32_bf16 v[116:119], v[224:227], v[228:231], v[116:119]\n"
"buffer_store_dwordx4 v[88:91], v120, s[48:51], s68 offen offset:128 sc1\n"
"ds_read_b64_tr_b16 v[224:225], v253 offset:30936\n"
"ds_read_b64_tr_b16 v[226:227], v253 offset:32472\n"
"ds_read_u16_d16 v246, v246 offset:18432\n"
"v_lshlrev_b32_sdwa v247, 1, v247 dst_sel:DWORD dst_unused:UNUSED_PAD src0_sel:DWORD src1_sel:WORD_1\n"
"s_waitcnt lgkmcnt(13)\n"
"ds_read_u16_d16_hi v247, v247 offset:18432\n"
"v_or_b32 v236, v236, v237\n"
"v_or_b32 v237, v238, v239\n"
"v_lshlrev_b32_sdwa v248, 1, v248 dst_sel:DWORD dst_unused:UNUSED_PAD src0_sel:DWORD src1_sel:WORD_1\n"
"ds_read_u16_d16 v248, v248 offset:18432\n"
"v_lshlrev_b32_sdwa v249, 1, v249 dst_sel:DWORD dst_unused:UNUSED_PAD src0_sel:DWORD src1_sel:WORD_1\n"
"ds_read_u16_d16_hi v249, v249 offset:18432\n"
"v_or_b32 v238, v240, v241\n"
"v_or_b32 v239, v242, v243\n"
"v_lshlrev_b32_sdwa v250, 1, v250 dst_sel:DWORD dst_unused:UNUSED_PAD src0_sel:DWORD src1_sel:WORD_1\n"
"ds_read_u16_d16 v250, v250 offset:18432\n"
"v_lshlrev_b32_sdwa v251, 1, v251 dst_sel:DWORD dst_unused:UNUSED_PAD src0_sel:DWORD src1_sel:WORD_1\n"
"s_waitcnt lgkmcnt(13)\n"
"v_mfma_f32_16x16x32_bf16 v[96:99], v[204:207], v[236:239], v[96:99]\n"
"ds_read_b64_tr_b16 v[204:205], v253 offset:36960\n"
"ds_read_b64_tr_b16 v[206:207], v253 offset:38496\n"
"ds_read_u16_d16_hi v251, v251 offset:18432\n"
"v_mfma_f32_16x16x32_bf16 v[100:103], v[208:211], v[236:239], v[100:103]\n"
"ds_read_b64_tr_b16 v[208:209], v253 offset:36968\n"
"ds_read_b64_tr_b16 v[210:211], v253 offset:38504\n"
"v_mfma_f32_16x16x32_bf16 v[104:107], v[212:215], v[236:239], v[104:107]\n"
"ds_read_b64_tr_b16 v[212:213], v253 offset:37024\n"
"ds_read_b64_tr_b16 v[214:215], v253 offset:38560\n"
"s_waitcnt lgkmcnt(12)\n"
"v_mfma_f32_16x16x32_bf16 v[108:111], v[216:219], v[236:239], v[108:111]\n"
"ds_read_b64_tr_b16 v[216:217], v253 offset:37032\n"
"ds_read_b64_tr_b16 v[218:219], v253 offset:38568\n"
"v_mfma_f32_16x16x32_bf16 v[112:115], v[220:223], v[236:239], v[112:115]\n"
"ds_read_b64_tr_b16 v[220:221], v253 offset:37088\n"
"ds_read_b64_tr_b16 v[222:223], v253 offset:38624\n"
"v_mfma_f32_16x16x32_bf16 v[116:119], v[224:227], v[236:239], v[116:119]\n"
"ds_read_b64_tr_b16 v[224:225], v253 offset:37096\n"
"ds_read_b64_tr_b16 v[226:227], v253 offset:38632\n"
"s_waitcnt lgkmcnt(10)\n"
"v_or_b32 v244, v244, v245\n"
"v_or_b32 v245, v246, v247\n"
"v_or_b32 v246, v248, v249\n"
"v_or_b32 v247, v250, v251\n"
"s_waitcnt lgkmcnt(6)\n"
"s_nop 0\n"
"v_mfma_f32_16x16x32_bf16 v[96:99], v[204:207], v[244:247], v[96:99]\n"
"v_mfma_f32_16x16x32_bf16 v[100:103], v[208:211], v[244:247], v[100:103]\n"
"v_mfma_f32_16x16x32_bf16 v[104:107], v[212:215], v[244:247], v[104:107]\n"
"s_waitcnt lgkmcnt(0)\n"
"v_mfma_f32_16x16x32_bf16 v[108:111], v[216:219], v[244:247], v[108:111]\n"
"v_mfma_f32_16x16x32_bf16 v[112:115], v[220:223], v[244:247], v[112:115]\n"
"s_add_u32 s68, s69, 0\n"
"s_nop 1\n"
"v_cvt_pk_bf16_f32 v96, v96, v97\n"
"v_cvt_pk_bf16_f32 v97, v98, v99\n"
"v_cvt_pk_bf16_f32 v98, v100, v101\n"
"v_cvt_pk_bf16_f32 v99, v102, v103\n"
"v_mfma_f32_16x16x32_bf16 v[116:119], v[224:227], v[244:247], v[116:119]\n"
"buffer_store_dwordx4 v[96:99], v120, s[48:51], s68 offen offset:0 sc1\n"
"v_cvt_pk_bf16_f32 v104, v104, v105\n"
"v_cvt_pk_bf16_f32 v105, v106, v107\n"
"v_cvt_pk_bf16_f32 v106, v108, v109\n"
"v_cvt_pk_bf16_f32 v107, v110, v111\n"
"buffer_store_dwordx4 v[104:107], v120, s[48:51], s68 offen offset:64 sc1\n"
"v_cvt_pk_bf16_f32 v112, v112, v113\n"
"v_cvt_pk_bf16_f32 v113, v114, v115\n"
"v_cvt_pk_bf16_f32 v114, v116, v117\n"
"v_cvt_pk_bf16_f32 v115, v118, v119\n"
"buffer_store_dwordx4 v[112:115], v120, s[48:51], s68 offen offset:128 sc1\n"
"s_mov_b32 s67, 0\n"
"s_branch .Le_%=\n"
".Lb_%=:\n"
"s_add_u32 m0, s86, 14464\n"
"s_mul_i32 s68, s65, 4\n"
"s_add_u32 s68, s85, s68\n"
"s_mul_i32 s68, s68, 73728\n"
"buffer_load_dwordx4 v166, s[36:39], s68 offen lds\n"
"s_add_u32 m0, s86, 15488\n"
"s_nop 0\n"
"buffer_load_dwordx4 v167, s[36:39], s68 offen lds\n"
"s_add_u32 m0, s86, 16512\n"
"s_mov_b64 exec, 0x1fffffff\n"
"buffer_load_dwordx4 v178, s[36:39], s68 offen lds\n"
"s_mov_b64 exec, -1\n"
"v_mov_b32 v174, v255\n"
"s_waitcnt lgkmcnt(0)\n"
"s_barrier\n"
"ds_read_b32 v0, v62 offset:0\n"
"ds_read_b32 v10, v62 offset:128\n"
"ds_read_b32 v20, v62 offset:256\n"
"ds_read_b32 v2, v63 offset:2400\n"
"ds_read_b32 v3, v64 offset:7200\n"
"ds_read_b32 v4, v65 offset:4800\n"
"ds_read_b32 v5, v64 offset:9600\n"
"ds_read_b32 v6, v64 offset:8000\n"
"ds_read_b32 v7, v66 offset:1600\n"
"ds_read_b32 v8, v64 offset:10400\n"
"ds_read_b32 v9, v67 offset:13600\n"
"ds_read_b32 v12, v63 offset:2528\n"
"ds_read_b32 v13, v64 offset:7328\n"
"ds_read_b32 v14, v65 offset:4928\n"
"ds_read_b32 v15, v64 offset:9728\n"
"ds_read_b32 v16, v64 offset:8128\n"
"ds_read_b32 v17, v66 offset:1728\n"
"ds_read_b32 v18, v64 offset:10528\n"
"ds_read_b32 v19, v67 offset:13728\n"
"ds_read_b32 v22, v63 offset:2656\n"
"ds_read_b32 v23, v64 offset:7456\n"
"ds_read_b32 v24, v65 offset:5056\n"
"ds_read_b32 v25, v64 offset:9856\n"
"ds_read_b32 v26, v64 offset:8256\n"
"ds_read_b32 v27, v66 offset:1856\n"
"ds_read_b32 v28, v64 offset:10656\n"
"ds_read_b32 v29, v67 offset:13856\n"
"s_mov_b64 s[34:35], s[46:47]\n"
"s_and_b32 s45, s45, 0xffff\n"
"s_mov_b32 s46, 73728\n"
"s_mov_b32 s47, 0x20000\n"
"s_waitcnt lgkmcnt(14)\n"
"v_mul_u32_u24 v1, v0, v69\n"
"v_mul_u32_u24 v0, v0, v68\n"
"v_mul_u32_u24 v11, v10, v69\n"
"v_mul_u32_u24 v10, v10, v68\n"
"v_mul_u32_u24 v21, v20, v69\n"
"v_mul_u32_u24 v20, v20, v68\n"
"s_lshl_b32 s66, s59, 6\n"
"v_add_u32 v76, s66, v251\n"
"v_lshlrev_b32 v76, 18, v76\n"
"v_or_b32 v76, 0x8000, v76\n"
"v_add_u32 v78, 0x0, v76\n"
"v_add_u32 v79, 0x10000, v76\n"
"v_add_u32 v80, 0x20000, v76\n"
"v_add_u32 v81, 0x30000, v76\n"
"v_cmp_ne_u32 vcc, 0x8000, v78\n"
"s_nop 1\n"
"v_cndmask_b32 v78, 0, v78, vcc\n"
"v_mul_f32 v78, s64, v78\n"
"v_mul_f32 v79, s64, v79\n"
"v_mul_f32 v80, s64, v80\n"
"v_mul_f32 v81, s64, v81\n"
"v_fma_f32 v82, -v78, v78, s62\n"
"v_fma_f32 v83, -v79, v79, s62\n"
"v_fma_f32 v84, -v80, v80, s62\n"
"v_fma_f32 v85, -v81, v81, s62\n"
"v_mul_f32 v82, v82, v78\n"
"v_mul_f32 v83, v83, v79\n"
"v_mul_f32 v84, v84, v80\n"
"v_mul_f32 v85, v85, v81\n"
"v_exp_f32 v82, v82\n"
"v_exp_f32 v83, v83\n"
"v_exp_f32 v84, v84\n"
"v_exp_f32 v85, v85\n"
"v_fma_f32 v86, v82, s63, s63\n"
"v_fma_f32 v87, v83, s63, s63\n"
"v_fma_f32 v88, v84, s63, s63\n"
"v_fma_f32 v89, v85, s63, s63\n"
"v_rcp_f32 v86, v86\n"
"v_rcp_f32 v87, v87\n"
"v_rcp_f32 v88, v88\n"
"v_rcp_f32 v89, v89\n"
"v_mul_f32 v86, v86, v78\n"
"v_mul_f32 v87, v87, v79\n"
"v_mul_f32 v88, v88, v80\n"
"v_mul_f32 v89, v89, v81\n"
"v_mul_f32 v82, -v86, v82\n"
"v_mul_f32 v83, -v87, v83\n"
"v_mul_f32 v84, -v88, v84\n"
"v_mul_f32 v85, -v89, v85\n"
"v_cvt_pk_bf16_f32 v228, v86, v87\n"
"v_cvt_pk_bf16_f32 v229, v88, v89\n"
"v_cvt_pk_bf16_f32 v230, v82, v83\n"
"v_cvt_pk_bf16_f32 v231, v84, v85\n"
"v_add_u32 v78, 0x4000000, v76\n"
"v_add_u32 v79, 0x4010000, v76\n"
"v_add_u32 v80, 0x4020000, v76\n"
"v_add_u32 v81, 0x4030000, v76\n"
"v_mul_f32 v78, s64, v78\n"
"v_mul_f32 v79, s64, v79\n"
"v_mul_f32 v80, s64, v80\n"
"v_mul_f32 v81, s64, v81\n"
"v_fma_f32 v82, -v78, v78, s62\n"
"v_fma_f32 v83, -v79, v79, s62\n"
"v_fma_f32 v84, -v80, v80, s62\n"
"v_fma_f32 v85, -v81, v81, s62\n"
"v_mul_f32 v82, v82, v78\n"
"v_mul_f32 v83, v83, v79\n"
"v_mul_f32 v84, v84, v80\n"
"v_mul_f32 v85, v85, v81\n"
"v_exp_f32 v82, v82\n"
"v_exp_f32 v83, v83\n"
"v_exp_f32 v84, v84\n"
"v_exp_f32 v85, v85\n"
"v_fma_f32 v86, v82, s63, s63\n"
"v_fma_f32 v87, v83, s63, s63\n"
"v_fma_f32 v88, v84, s63, s63\n"
"v_fma_f32 v89, v85, s63, s63\n"
"v_rcp_f32 v86, v86\n"
"v_rcp_f32 v87, v87\n"
"v_rcp_f32 v88, v88\n"
"v_rcp_f32 v89, v89\n"
"v_mul_f32 v86, v86, v78\n"
"v_mul_f32 v87, v87, v79\n"
"v_mul_f32 v88, v88, v80\n"
"v_mul_f32 v89, v89, v81\n"
"v_mul_f32 v82, -v86, v82\n"
"v_mul_f32 v83, -v87, v83\n"
"v_mul_f32 v84, -v88, v84\n"
"v_mul_f32 v85, -v89, v85\n"
"v_cvt_pk_bf16_f32 v232, v86, v87\n"
"v_cvt_pk_bf16_f32 v233, v88, v89\n"
"v_cvt_pk_bf16_f32 v234, v82, v83\n"
"v_cvt_pk_bf16_f32 v235, v84, v85\n"
"v_add_u32 v78, 0x8000000, v76\n"
"v_add_u32 v79, 0x8010000, v76\n"
"v_add_u32 v80, 0x8020000, v76\n"
"v_add_u32 v81, 0x8030000, v76\n"
"v_mul_f32 v78, s64, v78\n"
"v_mul_f32 v79, s64, v79\n"
"v_mul_f32 v80, s64, v80\n"
"v_mul_f32 v81, s64, v81\n"
"v_fma_f32 v82, -v78, v78, s62\n"
"v_fma_f32 v83, -v79, v79, s62\n"
"v_fma_f32 v84, -v80, v80, s62\n"
"v_fma_f32 v85, -v81, v81, s62\n"
"v_mul_f32 v82, v82, v78\n"
"v_mul_f32 v83, v83, v79\n"
"v_mul_f32 v84, v84, v80\n"
"v_mul_f32 v85, v85, v81\n"
"v_exp_f32 v82, v82\n"
"v_exp_f32 v83, v83\n"
"v_exp_f32 v84, v84\n"
"v_exp_f32 v85, v85\n"
"v_fma_f32 v86, v82, s63, s63\n"
"v_fma_f32 v87, v83, s63, s63\n"
"v_fma_f32 v88, v84, s63, s63\n"
"v_fma_f32 v89, v85, s63, s63\n"
"v_rcp_f32 v86, v86\n"
"v_rcp_f32 v87, v87\n"
"v_rcp_f32 v88, v88\n"
"v_rcp_f32 v89, v89\n"
"v_mul_f32 v86, v86, v78\n"
"v_mul_f32 v87, v87, v79\n"
"v_mul_f32 v88, v88, v80\n"
"v_mul_f32 v89, v89, v81\n"
"v_mul_f32 v82, -v86, v82\n"
"v_mul_f32 v83, -v87, v83\n"
"v_mul_f32 v84, -v88, v84\n"
"v_mul_f32 v85, -v89, v85\n"
"v_cvt_pk_bf16_f32 v236, v86, v87\n"
"v_cvt_pk_bf16_f32 v237, v88, v89\n"
"v_cvt_pk_bf16_f32 v238, v82, v83\n"
"v_cvt_pk_bf16_f32 v239, v84, v85\n"
"s_add_u32 m0, s86, 34176\n"
"s_mul_i32 s68, s65, 12\n"
"s_add_u32 s68, s85, s68\n"
"s_mul_i32 s68, s68, 73728\n"
"buffer_load_dwordx4 v252, s[36:39], s68 offen lds\n"
"s_add_u32 m0, s86, 35200\n"
"s_nop 0\n"
"buffer_load_dwordx4 v253, s[36:39], s68 offen lds\n"
"s_waitcnt vmcnt(2)\n"
"s_waitcnt lgkmcnt(0)\n"
"s_barrier\n"
"ds_read_b64 v[30:31], v116 offset:0\n"
"ds_read_b64 v[36:37], v116 offset:400\n"
"ds_read_b64 v[42:43], v116 offset:800\n"
"ds_read_b64 v[32:33], v115 offset:0\n"
"ds_read_b64 v[34:35], v115 offset:5696\n"
"ds_read_b64 v[38:39], v115 offset:400\n"
"ds_read_b64 v[40:41], v115 offset:6096\n"
"ds_read_b64 v[44:45], v115 offset:800\n"
"ds_read_b64 v[46:47], v115 offset:6496\n"
"ds_read_b64 v[48:49], v116 offset:1200\n"
"ds_read_b64 v[50:51], v115 offset:1200\n"
"s_waitcnt lgkmcnt(8)\n"
"v_mfma_f32_16x16x16_bf16 v[90:93], v[0:1], v[30:31], 0\n"
"s_add_u32 m0, s86, 36224\n"
"s_mul_i32 s68, s65, 12\n"
"s_add_u32 s68, s85, s68\n"
"s_mul_i32 s68, s68, 73728\n"
"buffer_load_dwordx4 v254, s[36:39], s68 offen lds\n"
"s_add_u32 m0, s86, 37248\n"
"ds_read_b64 v[52:53], v115 offset:6896\n"
"ds_read_b64 v[54:55], v116 offset:1600\n"
"v_mfma_f32_16x16x16_bf16 v[94:97], v[0:1], v[36:37], 0\n"
"ds_read_b64 v[56:57], v115 offset:1600\n"
"ds_read_b64 v[58:59], v115 offset:7296\n"
"v_mfma_f32_16x16x16_bf16 v[98:101], v[0:1], v[42:43], 0\n"
"ds_read_b64 v[60:61], v116 offset:64\n"
"s_waitcnt lgkmcnt(10)\n"
"v_smfmac_f32_16x16x64_bf16 v[90:93], v[2:5], v[32:39], v114\n"
"ds_read_b64 v[66:67], v116 offset:464\n"
"ds_read_b64 v[72:73], v116 offset:864\n"
"s_waitcnt lgkmcnt(10)\n"
"v_smfmac_f32_16x16x64_bf16 v[94:97], v[2:5], v[38:45], v114\n"
"ds_read_b64 v[62:63], v115 offset:64\n"
"ds_read_b64 v[64:65], v115 offset:5760\n"
"s_waitcnt lgkmcnt(11)\n"
"v_smfmac_f32_16x16x64_bf16 v[90:93], v[6:9], v[40:47], v114\n"
"ds_read_b64 v[68:69], v115 offset:464\n"
"s_waitcnt lgkmcnt(10)\n"
"v_smfmac_f32_16x16x64_bf16 v[98:101], v[2:5], v[44:51], v114\n"
"buffer_load_dwordx4 v166, s[36:39], s68 offen lds\n"
"s_add_u32 m0, s86, 38272\n"
"ds_read_b64 v[70:71], v115 offset:6160\n"
"ds_read_b64 v[74:75], v115 offset:864\n"
"s_waitcnt lgkmcnt(11)\n"
"v_smfmac_f32_16x16x64_bf16 v[94:97], v[6:9], v[46:53], v114\n"
"ds_read_b64 v[76:77], v115 offset:6560\n"
"ds_read_b64 v[78:79], v116 offset:1264\n"
"s_waitcnt lgkmcnt(10)\n"
"v_smfmac_f32_16x16x64_bf16 v[98:101], v[6:9], v[52:59], v114\n"
"ds_read_b64 v[80:81], v115 offset:1264\n"
"v_cvt_pk_f16_f32 v90, v90, v91\n"
"v_cvt_pk_f16_f32 v91, v92, v93\n"
"s_waitcnt lgkmcnt(8)\n"
"v_mfma_f32_16x16x16_bf16 v[102:105], v[10:11], v[60:61], 0\n"
"ds_write_b64 v117, v[90:91] offset:0\n"
"ds_read_b64 v[82:83], v115 offset:6960\n"
"ds_read_b64 v[84:85], v116 offset:1664\n"
"v_mfma_f32_16x16x16_bf16 v[106:109], v[10:11], v[66:67], 0\n"
"ds_read_b64 v[86:87], v115 offset:1664\n"
"ds_read_b64 v[88:89], v115 offset:7360\n"
"v_mfma_f32_16x16x16_bf16 v[110:113], v[10:11], v[72:73], 0\n"
"ds_read_b64 v[30:31], v116 offset:128\n"
"v_cvt_pk_f16_f32 v94, v94, v95\n"
"v_cvt_pk_f16_f32 v95, v96, v97\n"
"s_waitcnt lgkmcnt(11)\n"
"v_smfmac_f32_16x16x64_bf16 v[102:105], v[12:15], v[62:69], v114\n"
"ds_write_b64 v117, v[94:95] offset:3072\n"
"ds_read_b64 v[36:37], v116 offset:528\n"
"ds_read_b64 v[42:43], v116 offset:928\n"
"s_waitcnt lgkmcnt(12)\n"
"v_smfmac_f32_16x16x64_bf16 v[106:109], v[12:15], v[68:75], v114\n"
"buffer_load_dwordx4 v167, s[36:39], s68 offen lds\n"
"s_add_u32 m0, s86, 39296\n"
"ds_read_b64 v[32:33], v115 offset:128\n"
"ds_read_b64 v[34:35], v115 offset:5824\n"
"v_cvt_pk_f16_f32 v98, v98, v99\n"
"v_cvt_pk_f16_f32 v99, v100, v101\n"
"s_waitcnt lgkmcnt(13)\n"
"v_smfmac_f32_16x16x64_bf16 v[102:105], v[16:19], v[70:77], v114\n"
"ds_write_b64 v117, v[98:99] offset:6144\n"
"ds_read_b64 v[38:39], v115 offset:528\n"
"s_waitcnt lgkmcnt(13)\n"
"v_smfmac_f32_16x16x64_bf16 v[110:113], v[12:15], v[74:81], v114\n"
"ds_read_b64 v[40:41], v115 offset:6224\n"
"ds_read_b64 v[44:45], v115 offset:928\n"
"s_waitcnt lgkmcnt(13)\n"
"v_smfmac_f32_16x16x64_bf16 v[106:109], v[16:19], v[76:83], v114\n"
"ds_read_b64 v[46:47], v115 offset:6624\n"
"ds_read_b64 v[48:49], v116 offset:1328\n"
"s_waitcnt lgkmcnt(12)\n"
"v_smfmac_f32_16x16x64_bf16 v[110:113], v[16:19], v[82:89], v114\n"
"ds_read_b64 v[50:51], v115 offset:1328\n"
"v_cvt_pk_f16_f32 v102, v102, v103\n"
"v_cvt_pk_f16_f32 v103, v104, v105\n"
"s_waitcnt lgkmcnt(9)\n"
"v_mfma_f32_16x16x16_bf16 v[90:93], v[20:21], v[30:31], 0\n"
"ds_write_b64 v117, v[102:103] offset:1024\n"
"ds_read_b64 v[52:53], v115 offset:7024\n"
"ds_read_b64 v[54:55], v116 offset:1728\n"
"v_mfma_f32_16x16x16_bf16 v[94:97], v[20:21], v[36:37], 0\n"
"s_mov_b64 exec, 0x1fffffff\n"
"buffer_load_dwordx4 v178, s[36:39], s68 offen lds\n"
"s_mov_b64 exec, -1\n"
"ds_read_b64 v[56:57], v115 offset:1728\n"
"ds_read_b64 v[58:59], v115 offset:7424\n"
"v_mfma_f32_16x16x16_bf16 v[98:101], v[20:21], v[42:43], 0\n"
"v_cvt_pk_f16_f32 v106, v106, v107\n"
"v_cvt_pk_f16_f32 v107, v108, v109\n"
"s_waitcnt lgkmcnt(10)\n"
"v_smfmac_f32_16x16x64_bf16 v[90:93], v[22:25], v[32:39], v114\n"
"ds_write_b64 v117, v[106:107] offset:4096\n"
"s_waitcnt lgkmcnt(9)\n"
"v_smfmac_f32_16x16x64_bf16 v[94:97], v[22:25], v[38:45], v114\n"
"v_cvt_pk_f16_f32 v110, v110, v111\n"
"v_cvt_pk_f16_f32 v111, v112, v113\n"
"s_waitcnt lgkmcnt(8)\n"
"v_smfmac_f32_16x16x64_bf16 v[90:93], v[26:29], v[40:47], v114\n"
"ds_write_b64 v117, v[110:111] offset:7168\n"
"s_waitcnt lgkmcnt(7)\n"
"v_smfmac_f32_16x16x64_bf16 v[98:101], v[22:25], v[44:51], v114\n"
"s_waitcnt lgkmcnt(5)\n"
"v_smfmac_f32_16x16x64_bf16 v[94:97], v[26:29], v[46:53], v114\n"
"s_waitcnt lgkmcnt(2)\n"
"v_smfmac_f32_16x16x64_bf16 v[98:101], v[26:29], v[52:59], v114\n"
"s_nop 0\n"
"v_cvt_pk_f16_f32 v90, v90, v91\n"
"v_cvt_pk_f16_f32 v91, v92, v93\n"
"s_nop 2\n"
"v_cvt_pk_f16_f32 v94, v94, v95\n"
"v_cvt_pk_f16_f32 v95, v96, v97\n"
"v_cvt_pk_f16_f32 v98, v98, v99\n"
"v_cvt_pk_f16_f32 v99, v100, v101\n"
"ds_write_b64 v117, v[90:91] offset:2048\n"
"ds_write_b64 v117, v[94:95] offset:5120\n"
"ds_write_b64 v117, v[98:99] offset:8192\n"
"s_waitcnt lgkmcnt(0)\n"
"s_barrier\n"
"ds_read_b64 v[120:121], v118 offset:6144\n"
"ds_read_b64 v[122:123], v118 offset:6656\n"
"ds_read_b64 v[124:125], v118 offset:7168\n"
"ds_read_b64 v[126:127], v118 offset:7680\n"
"ds_read_b64 v[128:129], v118 offset:8192\n"
"ds_read_b64 v[130:131], v118 offset:8704\n"
"v_add_u32 v117, 0xffff2180, v117\n"
"v_add_u32 v118, 0xffff2180, v118\n"
"s_waitcnt lgkmcnt(4)\n"
"v_mfma_f32_16x16x32_f16 v[156:159], v[120:123], v[120:123], 0\n"
"s_waitcnt lgkmcnt(2)\n"
"v_mfma_f32_16x16x32_f16 v[156:159], v[124:127], v[124:127], v[156:159]\n"
"s_waitcnt lgkmcnt(0)\n"
"v_mfma_f32_16x16x32_f16 v[156:159], v[128:131], v[128:131], v[156:159]\n"
"v_mfma_f32_16x16x32_f16 v[160:163], v[240:243], v[120:123], 0\n"
"v_mfma_f32_16x16x32_f16 v[160:163], v[240:243], v[124:127], v[160:163]\n"
"v_mfma_f32_16x16x32_f16 v[160:163], v[240:243], v[128:131], v[160:163]\n"
"s_waitcnt lgkmcnt(0)\n"
"s_barrier\n"
"s_setprio 1\n"
"s_nop 1\n"
"v_cndmask_b32 v164, 0, v156, s[72:73]\n"
"v_cndmask_b32 v164, v164, v157, s[74:75]\n"
"v_cndmask_b32 v164, v164, v158, s[76:77]\n"
"v_cndmask_b32 v164, v164, v159, s[78:79]\n"
"v_mul_f32 v160, s82, v160\n"
"v_fma_f32 v161, -v160, v160, s83\n"
"v_mfma_f32_16x16x4_f32 v[156:159], v250, v164, 0\n"
"ds_read_b64 v[180:181], v119 offset:800\n"
"s_add_u32 m0, s86, 56960\n"
"s_mul_i32 s68, s65, 20\n"
"s_add_u32 s68, s85, s68\n"
"s_cmp_lt_u32 s68, 384\n"
"s_cselect_b32 s68, s68, 0\n"
"s_cselect_b32 s38, 28311552, 0\n"
"s_mul_i32 s68, s68, 73728\n"
"buffer_load_dwordx4 v252, s[36:39], s68 offen lds\n"
"s_add_u32 m0, s86, 57984\n"
"s_nop 1\n"
"v_fma_f32 v164, v156, s82, v161\n"
"v_mul_f32 v156, s71, v156\n"
"v_cmp_nge_f32 vcc, v164, v156\n"
"v_rsq_f32 v164, v164\n"
"s_or_b64 s[80:81], s[80:81], vcc\n"
"v_mul_f32 v164, s84, v164\n"
"v_mul_f32 v165, -v160, v164\n"
"v_fma_mix_f32 v156, v120, v164, v165 op_sel_hi:[1,0,0]\n"
"v_fma_mix_f32 v157, v120, v164, v165 op_sel:[1,0,0] op_sel_hi:[1,0,0]\n"
"ds_read_b64 v[182:183], v119 offset:808\n"
"buffer_load_dwordx4 v253, s[36:39], s68 offen lds\n"
"s_add_u32 m0, s86, 59008\n"
"v_fma_mix_f32 v158, v121, v164, v165 op_sel_hi:[1,0,0]\n"
"v_fma_mix_f32 v159, v121, v164, v165 op_sel:[1,0,0] op_sel_hi:[1,0,0]\n"
"v_cvt_pk_bf16_f32 v120, v156, v157\n"
"v_fma_mix_f32 v160, v122, v164, v165 op_sel_hi:[1,0,0]\n"
"v_fma_mix_f32 v161, v122, v164, v165 op_sel:[1,0,0] op_sel_hi:[1,0,0]\n"
"v_cvt_pk_bf16_f32 v121, v158, v159\n"
"ds_read_b64 v[184:185], v119 offset:864\n"
"v_fma_mix_f32 v162, v123, v164, v165 op_sel_hi:[1,0,0]\n"
"v_fma_mix_f32 v163, v123, v164, v165 op_sel:[1,0,0] op_sel_hi:[1,0,0]\n"
"v_cvt_pk_bf16_f32 v122, v160, v161\n"
"buffer_load_dwordx4 v254, s[36:39], s68 offen lds\n"
"s_add_u32 m0, s86, 60032\n"
"v_fma_mix_f32 v156, v124, v164, v165 op_sel_hi:[1,0,0]\n"
"v_fma_mix_f32 v157, v124, v164, v165 op_sel:[1,0,0] op_sel_hi:[1,0,0]\n"
"v_cvt_pk_bf16_f32 v123, v162, v163\n"
"ds_read_b64 v[186:187], v119 offset:872\n"
"v_fma_mix_f32 v158, v125, v164, v165 op_sel_hi:[1,0,0]\n"
"v_fma_mix_f32 v159, v125, v164, v165 op_sel:[1,0,0] op_sel_hi:[1,0,0]\n"
"v_cvt_pk_bf16_f32 v124, v156, v157\n"
"buffer_load_dwordx4 v166, s[36:39], s68 offen lds\n"
"s_add_u32 m0, s86, 61056\n"
"v_fma_mix_f32 v160, v126, v164, v165 op_sel_hi:[1,0,0]\n"
"v_fma_mix_f32 v161, v126, v164, v165 op_sel:[1,0,0] op_sel_hi:[1,0,0]\n"
"v_cvt_pk_bf16_f32 v125, v158, v159\n"
"v_fma_mix_f32 v162, v127, v164, v165 op_sel_hi:[1,0,0]\n"
"v_fma_mix_f32 v163, v127, v164, v165 op_sel:[1,0,0] op_sel_hi:[1,0,0]\n"
"v_cvt_pk_bf16_f32 v126, v160, v161\n"
"ds_read_b64 v[188:189], v119 offset:928\n"
"v_fma_mix_f32 v156, v128, v164, v165 op_sel_hi:[1,0,0]\n"
"v_fma_mix_f32 v157, v128, v164, v165 op_sel:[1,0,0] op_sel_hi:[1,0,0]\n"
"v_cvt_pk_bf16_f32 v127, v162, v163\n"
"buffer_load_dwordx4 v167, s[36:39], s68 offen lds\n"
"s_add_u32 m0, s86, 62080\n"
"v_fma_mix_f32 v158, v129, v164, v165 op_sel_hi:[1,0,0]\n"
"v_fma_mix_f32 v159, v129, v164, v165 op_sel:[1,0,0] op_sel_hi:[1,0,0]\n"
"v_cvt_pk_bf16_f32 v128, v156, v157\n"
"ds_read_b64 v[190:191], v119 offset:936\n"
"v_fma_mix_f32 v160, v130, v164, v165 op_sel_hi:[1,0,0]\n"
"v_fma_mix_f32 v161, v130, v164, v165 op_sel:[1,0,0] op_sel_hi:[1,0,0]\n"
"v_cvt_pk_bf16_f32 v129, v158, v159\n"
"s_mov_b64 exec, 0x1fffffff\n"
"buffer_load_dwordx4 v178, s[36:39], s68 offen lds\n"
"s_mov_b64 exec, -1\n"
"v_fma_mix_f32 v162, v131, v164, v165 op_sel_hi:[1,0,0]\n"
"v_fma_mix_f32 v163, v131, v164, v165 op_sel:[1,0,0] op_sel_hi:[1,0,0]\n"
"v_cvt_pk_bf16_f32 v130, v160, v161\n"
"v_cvt_pk_bf16_f32 v131, v162, v163\n"
"s_setprio 0\n"
"s_waitcnt vmcnt(6)\n"
"s_waitcnt lgkmcnt(0)\n"
"s_barrier\n"
"ds_read_b64 v[30:31], v116 offset:22784\n"
"ds_read_b64 v[36:37], v116 offset:23184\n"
"ds_read_b64 v[42:43], v116 offset:23584\n"
"ds_read_b64 v[32:33], v115 offset:22784\n"
"ds_read_b64 v[34:35], v115 offset:28480\n"
"ds_read_b64 v[38:39], v115 offset:23184\n"
"ds_read_b64 v[40:41], v115 offset:28880\n"
"ds_read_b64 v[44:45], v115 offset:23584\n"
"ds_read_b64 v[46:47], v115 offset:29280\n"
"ds_read_b64 v[48:49], v116 offset:23984\n"
"ds_read_b64 v[50:51], v115 offset:23984\n"
"s_waitcnt lgkmcnt(8)\n"
"v_mfma_f32_16x16x16_bf16 v[90:93], v[0:1], v[30:31], 0\n"
"s_lshr_b32 s66, %[bid], 3\n"
"s_mul_i32 s66, s66, 2304\n"
"v_lshlrev_b32 v168, 6, v251\n"
"s_mov_b32 exec_hi, 0xf\n"
"buffer_load_dword v169, v168, s[40:43], s66 offen\n"
"buffer_load_dword v170, v168, s[44:47], s66 offen\n"
"s_mov_b32 exec_hi, -1\n"
"ds_read_b64 v[52:53], v115 offset:29680\n"
"ds_read_b64 v[54:55], v116 offset:24384\n"
"v_mfma_f32_16x16x16_bf16 v[94:97], v[0:1], v[36:37], 0\n"
"ds_read_b64 v[56:57], v115 offset:24384\n"
"ds_read_b64 v[58:59], v115 offset:30080\n"
"v_mfma_f32_16x16x16_bf16 v[98:101], v[0:1], v[42:43], 0\n"
"ds_read_b64 v[60:61], v116 offset:22848\n"
"s_waitcnt lgkmcnt(10)\n"
"v_smfmac_f32_16x16x64_bf16 v[90:93], v[2:5], v[32:39], v114\n"
"ds_read_b64 v[66:67], v116 offset:23248\n"
"ds_read_b64 v[72:73], v116 offset:23648\n"
"s_waitcnt lgkmcnt(10)\n"
"v_smfmac_f32_16x16x64_bf16 v[94:97], v[2:5], v[38:45], v114\n"
"ds_read_b64 v[62:63], v115 offset:22848\n"
"ds_read_b64 v[64:65], v115 offset:28544\n"
"s_waitcnt lgkmcnt(11)\n"
"v_smfmac_f32_16x16x64_bf16 v[90:93], v[6:9], v[40:47], v114\n"
"ds_read_b64 v[68:69], v115 offset:23248\n"
"s_waitcnt lgkmcnt(10)\n"
"v_smfmac_f32_16x16x64_bf16 v[98:101], v[2:5], v[44:51], v114\n"
"ds_read_b64 v[70:71], v115 offset:28944\n"
"ds_read_b64 v[74:75], v115 offset:23648\n"
"s_waitcnt lgkmcnt(11)\n"
"v_smfmac_f32_16x16x64_bf16 v[94:97], v[6:9], v[46:53], v114\n"
"ds_read_b64 v[76:77], v115 offset:29344\n"
"ds_read_b64 v[78:79], v116 offset:24048\n"
"s_waitcnt lgkmcnt(10)\n"
"v_smfmac_f32_16x16x64_bf16 v[98:101], v[6:9], v[52:59], v114\n"
"ds_read_b64 v[80:81], v115 offset:24048\n"
"v_cvt_pk_f16_f32 v90, v90, v91\n"
"v_cvt_pk_f16_f32 v91, v92, v93\n"
"s_waitcnt lgkmcnt(8)\n"
"v_mfma_f32_16x16x16_bf16 v[102:105], v[10:11], v[60:61], 0\n"
"ds_write_b64 v117, v[90:91] offset:0\n"
"ds_read_b64 v[82:83], v115 offset:29744\n"
"ds_read_b64 v[84:85], v116 offset:24448\n"
"v_mfma_f32_16x16x16_bf16 v[106:109], v[10:11], v[66:67], 0\n"
"ds_read_b64 v[86:87], v115 offset:24448\n"
"ds_read_b64 v[88:89], v115 offset:30144\n"
"v_mfma_f32_16x16x16_bf16 v[110:113], v[10:11], v[72:73], 0\n"
"ds_read_b64 v[30:31], v116 offset:22912\n"
"v_cvt_pk_f16_f32 v94, v94, v95\n"
"v_cvt_pk_f16_f32 v95, v96, v97\n"
"s_waitcnt lgkmcnt(11)\n"
"v_smfmac_f32_16x16x64_bf16 v[102:105], v[12:15], v[62:69], v114\n"
"ds_write_b64 v117, v[94:95] offset:3072\n"
"ds_read_b64 v[36:37], v116 offset:23312\n"
"ds_read_b64 v[42:43], v116 offset:23712\n"
"s_waitcnt lgkmcnt(12)\n"
"v_smfmac_f32_16x16x64_bf16 v[106:109], v[12:15], v[68:75], v114\n"
"ds_read_b64 v[32:33], v115 offset:22912\n"
"ds_read_b64 v[34:35], v115 offset:28608\n"
"v_cvt_pk_f16_f32 v98, v98, v99\n"
"v_cvt_pk_f16_f32 v99, v100, v101\n"
"s_waitcnt lgkmcnt(13)\n"
"v_smfmac_f32_16x16x64_bf16 v[102:105], v[16:19], v[70:77], v114\n"
"ds_write_b64 v117, v[98:99] offset:6144\n"
"ds_read_b64 v[38:39], v115 offset:23312\n"
"s_waitcnt lgkmcnt(13)\n"
"v_smfmac_f32_16x16x64_bf16 v[110:113], v[12:15], v[74:81], v114\n"
"ds_read_b64 v[40:41], v115 offset:29008\n"
"ds_read_b64 v[44:45], v115 offset:23712\n"
"s_waitcnt lgkmcnt(13)\n"
"v_smfmac_f32_16x16x64_bf16 v[106:109], v[16:19], v[76:83], v114\n"
"ds_read_b64 v[46:47], v115 offset:29408\n"
"ds_read_b64 v[48:49], v116 offset:24112\n"
"s_waitcnt lgkmcnt(12)\n"
"v_smfmac_f32_16x16x64_bf16 v[110:113], v[16:19], v[82:89], v114\n"
"ds_read_b64 v[50:51], v115 offset:24112\n"
"v_cvt_pk_f16_f32 v102, v102, v103\n"
"v_cvt_pk_f16_f32 v103, v104, v105\n"
"s_waitcnt lgkmcnt(9)\n"
"v_mfma_f32_16x16x16_bf16 v[90:93], v[20:21], v[30:31], 0\n"
"ds_write_b64 v117, v[102:103] offset:1024\n"
"ds_read_b64 v[52:53], v115 offset:29808\n"
"ds_read_b64 v[54:55], v116 offset:24512\n"
"v_mfma_f32_16x16x16_bf16 v[94:97], v[20:21], v[36:37], 0\n"
"ds_read_b64 v[56:57], v115 offset:24512\n"
"ds_read_b64 v[58:59], v115 offset:30208\n"
"v_mfma_f32_16x16x16_bf16 v[98:101], v[20:21], v[42:43], 0\n"
"v_cvt_pk_f16_f32 v106, v106, v107\n"
"v_cvt_pk_f16_f32 v107, v108, v109\n"
"s_waitcnt lgkmcnt(10)\n"
"v_smfmac_f32_16x16x64_bf16 v[90:93], v[22:25], v[32:39], v114\n"
"ds_write_b64 v117, v[106:107] offset:4096\n"
"s_waitcnt lgkmcnt(9)\n"
"v_smfmac_f32_16x16x64_bf16 v[94:97], v[22:25], v[38:45], v114\n"
"v_cvt_pk_f16_f32 v110, v110, v111\n"
"v_cvt_pk_f16_f32 v111, v112, v113\n"
"s_waitcnt lgkmcnt(8)\n"
"v_smfmac_f32_16x16x64_bf16 v[90:93], v[26:29], v[40:47], v114\n"
"ds_write_b64 v117, v[110:111] offset:7168\n"
"s_waitcnt lgkmcnt(7)\n"
"v_smfmac_f32_16x16x64_bf16 v[98:101], v[22:25], v[44:51], v114\n"
"s_waitcnt lgkmcnt(5)\n"
"v_smfmac_f32_16x16x64_bf16 v[94:97], v[26:29], v[46:53], v114\n"
"s_waitcnt lgkmcnt(2)\n"
"v_smfmac_f32_16x16x64_bf16 v[98:101], v[26:29], v[52:59], v114\n"
"s_nop 0\n"
"v_cvt_pk_f16_f32 v90, v90, v91\n"
"v_cvt_pk_f16_f32 v91, v92, v93\n"
"s_nop 2\n"
"v_cvt_pk_f16_f32 v94, v94, v95\n"
"v_cvt_pk_f16_f32 v95, v96, v97\n"
"v_cvt_pk_f16_f32 v98, v98, v99\n"
"v_cvt_pk_f16_f32 v99, v100, v101\n"
"ds_write_b64 v117, v[90:91] offset:2048\n"
"ds_write_b64 v117, v[94:95] offset:5120\n"
"ds_write_b64 v117, v[98:99] offset:8192\n"
"s_waitcnt lgkmcnt(0)\n"
"s_barrier\n"
"ds_read_b64 v[132:133], v118 offset:6144\n"
"ds_read_b64 v[134:135], v118 offset:6656\n"
"ds_read_b64 v[136:137], v118 offset:7168\n"
"ds_read_b64 v[138:139], v118 offset:7680\n"
"ds_read_b64 v[140:141], v118 offset:8192\n"
"ds_read_b64 v[142:143], v118 offset:8704\n"
"ds_read_b64 v[192:193], v119 offset:23584\n"
"ds_read_b64 v[194:195], v119 offset:23592\n"
"ds_read_b64 v[196:197], v119 offset:23648\n"
"ds_read_b64 v[198:199], v119 offset:23656\n"
"ds_read_b64 v[200:201], v119 offset:23712\n"
"ds_read_b64 v[202:203], v119 offset:23720\n"
"v_bfe_u32 v113, v251, 3, 1\n"
"v_mul_u32_u24 v113, 9984, v113\n"
"v_add_u32 v117, v117, v113\n"
"s_lshr_b32 s66, s59, 1\n"
"s_mul_i32 s66, s66, 9984\n"
"v_add_u32 v118, s66, v118\n"
"s_waitcnt lgkmcnt(10)\n"
"v_mfma_f32_16x16x32_f16 v[156:159], v[132:135], v[132:135], 0\n"
"s_waitcnt lgkmcnt(8)\n"
"v_mfma_f32_16x16x32_f16 v[156:159], v[136:139], v[136:139], v[156:159]\n"
"s_waitcnt lgkmcnt(6)\n"
"v_mfma_f32_16x16x32_f16 v[156:159], v[140:143], v[140:143], v[156:159]\n"
"v_mfma_f32_16x16x32_f16 v[160:163], v[240:243], v[132:135], 0\n"
"v_mfma_f32_16x16x32_f16 v[160:163], v[240:243], v[136:139], v[160:163]\n"
"v_mfma_f32_16x16x32_f16 v[160:163], v[240:243], v[140:143], v[160:163]\n"
"s_waitcnt vmcnt(2)\n"
"s_waitcnt lgkmcnt(0)\n"
"s_barrier\n"
"s_setprio 1\n"
"ds_read_b64 v[30:31], v116 offset:45568\n"
"ds_read_b64 v[36:37], v116 offset:45968\n"
"ds_read_b64 v[42:43], v116 offset:46368\n"
"ds_read_b64 v[32:33], v115 offset:45568\n"
"ds_read_b64 v[34:35], v115 offset:51264\n"
"ds_read_b64 v[38:39], v115 offset:45968\n"
"ds_read_b64 v[40:41], v115 offset:51664\n"
"ds_read_b64 v[44:45], v115 offset:46368\n"
"ds_read_b64 v[46:47], v115 offset:52064\n"
"ds_read_b64 v[48:49], v116 offset:46768\n"
"ds_read_b64 v[50:51], v115 offset:46768\n"
"ds_read_b64 v[52:53], v115 offset:52464\n"
"ds_read_b64 v[54:55], v116 offset:47168\n"
"ds_read_b64 v[56:57], v115 offset:47168\n"
"ds_read_b64 v[58:59], v115 offset:52864\n"
"s_waitcnt lgkmcnt(12)\n"
"v_mfma_f32_16x16x16_bf16 v[90:93], v[0:1], v[30:31], 0\n"
"ds_read_b64 v[60:61], v116 offset:45632\n"
"ds_read_b64 v[66:67], v116 offset:46032\n"
"v_mfma_f32_16x16x16_bf16 v[94:97], v[0:1], v[36:37], 0\n"
"ds_read_b64 v[72:73], v116 offset:46432\n"
"ds_read_b64 v[62:63], v115 offset:45632\n"
"v_mfma_f32_16x16x16_bf16 v[98:101], v[0:1], v[42:43], 0\n"
"ds_read_b64 v[64:65], v115 offset:51328\n"
"s_waitcnt lgkmcnt(14)\n"
"v_smfmac_f32_16x16x64_bf16 v[90:93], v[2:5], v[32:39], v114\n"
"ds_read_b64 v[68:69], v115 offset:46032\n"
"ds_read_b64 v[70:71], v115 offset:51728\n"
"s_waitcnt lgkmcnt(14)\n"
"v_smfmac_f32_16x16x64_bf16 v[94:97], v[2:5], v[38:45], v114\n"
"ds_read_b64 v[74:75], v115 offset:46432\n"
"ds_read_b64 v[76:77], v115 offset:52128\n"
"s_waitcnt lgkmcnt(14)\n"
"v_smfmac_f32_16x16x64_bf16 v[90:93], v[6:9], v[40:47], v114\n"
"ds_read_b64 v[78:79], v116 offset:46832\n"
"s_waitcnt lgkmcnt(14)\n"
"v_smfmac_f32_16x16x64_bf16 v[98:101], v[2:5], v[44:51], v114\n"
"ds_read_b64 v[80:81], v115 offset:46832\n"
"ds_read_b64 v[82:83], v115 offset:52528\n"
"s_waitcnt lgkmcnt(14)\n"
"v_smfmac_f32_16x16x64_bf16 v[94:97], v[6:9], v[46:53], v114\n"
"ds_read_b64 v[84:85], v116 offset:47232\n"
"ds_read_b64 v[86:87], v115 offset:47232\n"
"s_waitcnt lgkmcnt(14)\n"
"v_smfmac_f32_16x16x64_bf16 v[98:101], v[6:9], v[52:59], v114\n"
"ds_read_b64 v[88:89], v115 offset:52928\n"
"v_cvt_pk_f16_f32 v90, v90, v91\n"
"v_cvt_pk_f16_f32 v91, v92, v93\n"
"s_waitcnt lgkmcnt(12)\n"
"v_mfma_f32_16x16x16_bf16 v[102:105], v[10:11], v[60:61], 0\n"
"ds_write_b64 v117, v[90:91] offset:0\n"
"ds_read_b64 v[30:31], v116 offset:45696\n"
"ds_read_b64 v[36:37], v116 offset:46096\n"
"v_mfma_f32_16x16x16_bf16 v[106:109], v[10:11], v[66:67], 0\n"
"ds_read_b64 v[42:43], v116 offset:46496\n"
"ds_read_b64 v[32:33], v115 offset:45696\n"
"v_mfma_f32_16x16x16_bf16 v[110:113], v[10:11], v[72:73], 0\n"
"ds_read_b64 v[34:35], v115 offset:51392\n"
"v_cvt_pk_f16_f32 v94, v94, v95\n"
"v_cvt_pk_f16_f32 v95, v96, v97\n"
"s_waitcnt lgkmcnt(14)\n"
"v_smfmac_f32_16x16x64_bf16 v[102:105], v[12:15], v[62:69], v114\n"
"ds_write_b64 v117, v[94:95] offset:3072\n"
"ds_read_b64 v[38:39], v115 offset:46096\n"
"ds_read_b64 v[40:41], v115 offset:51792\n"
"s_waitcnt lgkmcnt(14)\n"
"v_smfmac_f32_16x16x64_bf16 v[106:109], v[12:15], v[68:75], v114\n"
"ds_read_b64 v[44:45], v115 offset:46496\n"
"ds_read_b64 v[46:47], v115 offset:52192\n"
"v_cvt_pk_f16_f32 v98, v98, v99\n"
"v_cvt_pk_f16_f32 v99, v100, v101\n"
"v_smfmac_f32_16x16x64_bf16 v[102:105], v[16:19], v[70:77], v114\n"
"ds_write_b64 v117, v[98:99] offset:6144\n"
"ds_read_b64 v[48:49], v116 offset:46896\n"
"s_waitcnt lgkmcnt(14)\n"
"v_smfmac_f32_16x16x64_bf16 v[110:113], v[12:15], v[74:81], v114\n"
"ds_read_b64 v[50:51], v115 offset:46896\n"
"ds_read_b64 v[52:53], v115 offset:52592\n"
"v_smfmac_f32_16x16x64_bf16 v[106:109], v[16:19], v[76:83], v114\n"
"ds_read_b64 v[54:55], v116 offset:47296\n"
"ds_read_b64 v[56:57], v115 offset:47296\n"
"s_waitcnt lgkmcnt(14)\n"
"v_smfmac_f32_16x16x64_bf16 v[110:113], v[16:19], v[82:89], v114\n"
"ds_read_b64 v[58:59], v115 offset:52992\n"
"v_cvt_pk_f16_f32 v102, v102, v103\n"
"v_cvt_pk_f16_f32 v103, v104, v105\n"
"s_waitcnt lgkmcnt(14)\n"
"v_mfma_f32_16x16x16_bf16 v[90:93], v[20:21], v[30:31], 0\n"
"ds_write_b64 v117, v[102:103] offset:1024\n"
"v_mfma_f32_16x16x16_bf16 v[94:97], v[20:21], v[36:37], 0\n"
"v_mfma_f32_16x16x16_bf16 v[98:101], v[20:21], v[42:43], 0\n"
"v_cvt_pk_f16_f32 v106, v106, v107\n"
"v_cvt_pk_f16_f32 v107, v108, v109\n"
"s_waitcnt lgkmcnt(11)\n"
"v_smfmac_f32_16x16x64_bf16 v[90:93], v[22:25], v[32:39], v114\n"
"ds_write_b64 v117, v[106:107] offset:4096\n"
"s_waitcnt lgkmcnt(10)\n"
"v_smfmac_f32_16x16x64_bf16 v[94:97], v[22:25], v[38:45], v114\n"
"v_cvt_pk_f16_f32 v110, v110, v111\n"
"v_cvt_pk_f16_f32 v111, v112, v113\n"
"s_waitcnt lgkmcnt(9)\n"
"v_smfmac_f32_16x16x64_bf16 v[90:93], v[26:29], v[40:47], v114\n"
"ds_write_b64 v117, v[110:111] offset:7168\n"
"s_waitcnt lgkmcnt(7)\n"
"v_smfmac_f32_16x16x64_bf16 v[98:101], v[22:25], v[44:51], v114\n"
"s_waitcnt lgkmcnt(6)\n"
"v_smfmac_f32_16x16x64_bf16 v[94:97], v[26:29], v[46:53], v114\n"
"s_waitcnt lgkmcnt(3)\n"
"v_smfmac_f32_16x16x64_bf16 v[98:101], v[26:29], v[52:59], v114\n"
"s_nop 0\n"
"v_cvt_pk_f16_f32 v90, v90, v91\n"
"v_cvt_pk_f16_f32 v91, v92, v93\n"
"s_nop 2\n"
"v_cvt_pk_f16_f32 v94, v94, v95\n"
"v_cvt_pk_f16_f32 v95, v96, v97\n"
"v_cvt_pk_f16_f32 v98, v98, v99\n"
"v_cvt_pk_f16_f32 v99, v100, v101\n"
"ds_write_b64 v117, v[90:91] offset:2048\n"
"ds_write_b64 v117, v[94:95] offset:5120\n"
"ds_write_b64 v117, v[98:99] offset:8192\n"
"s_setprio 0\n"
"v_cndmask_b32 v164, 0, v156, s[72:73]\n"
"v_cndmask_b32 v164, v164, v157, s[74:75]\n"
"v_cndmask_b32 v164, v164, v158, s[76:77]\n"
"v_cndmask_b32 v164, v164, v159, s[78:79]\n"
"v_mul_f32 v160, s82, v160\n"
"v_fma_f32 v161, -v160, v160, s83\n"
"v_mfma_f32_16x16x4_f32 v[156:159], v250, v164, 0\n"
"s_mul_i32 s88, s93, 6160\n"
"s_add_u32 s88, s88, s90\n"
"s_add_u32 m0, s88, 108336\n"
"s_add_u32 s66, s91, 0\n"
"buffer_load_dwordx4 v175, s[44:47], s66 offen lds\n"
"v_mfma_f32_16x16x32_bf16 v[0:3], v[246:249], v[180:183], 0\n"
"s_nop 5\n"
"v_fma_f32 v164, v156, s82, v161\n"
"v_mul_f32 v156, s71, v156\n"
"v_cmp_nge_f32 vcc, v164, v156\n"
"v_rsq_f32 v164, v164\n"
"s_or_b64 s[80:81], s[80:81], vcc\n"
"v_mul_f32 v164, s84, v164\n"
"v_mul_f32 v165, -v160, v164\n"
"v_mfma_f32_16x16x32_bf16 v[4:7], v[244:247], v[180:183], 0\n"
"v_fma_mix_f32 v156, v132, v164, v165 op_sel_hi:[1,0,0]\n"
"v_fma_mix_f32 v157, v132, v164, v165 op_sel:[1,0,0] op_sel_hi:[1,0,0]\n"
"v_mfma_f32_16x16x32_bf16 v[8:11], v[246:249], v[184:187], 0\n"
"v_fma_mix_f32 v158, v133, v164, v165 op_sel_hi:[1,0,0]\n"
"v_fma_mix_f32 v159, v133, v164, v165 op_sel:[1,0,0] op_sel_hi:[1,0,0]\n"
"v_cvt_pk_bf16_f32 v132, v156, v157\n"
"s_add_u32 m0, s88, 109360\n"
"s_add_u32 s66, s91, 1024\n"
"buffer_load_dwordx4 v175, s[44:47], s66 offen lds\n"
"v_mfma_f32_16x16x32_bf16 v[12:15], v[244:247], v[184:187], 0\n"
"v_fma_mix_f32 v160, v134, v164, v165 op_sel_hi:[1,0,0]\n"
"v_fma_mix_f32 v161, v134, v164, v165 op_sel:[1,0,0] op_sel_hi:[1,0,0]\n"
"v_cvt_pk_bf16_f32 v133, v158, v159\n"
"v_fma_mix_f32 v162, v135, v164, v165 op_sel_hi:[1,0,0]\n"
"v_fma_mix_f32 v163, v135, v164, v165 op_sel:[1,0,0] op_sel_hi:[1,0,0]\n"
"v_cvt_pk_bf16_f32 v134, v160, v161\n"
"v_mfma_f32_16x16x32_bf16 v[16:19], v[246:249], v[188:191], 0\n"
"v_fma_mix_f32 v156, v136, v164, v165 op_sel_hi:[1,0,0]\n"
"v_fma_mix_f32 v157, v136, v164, v165 op_sel:[1,0,0] op_sel_hi:[1,0,0]\n"
"v_cvt_pk_bf16_f32 v135, v162, v163\n"
"v_mfma_f32_16x16x32_bf16 v[20:23], v[244:247], v[188:191], 0\n"
"v_fma_mix_f32 v158, v137, v164, v165 op_sel_hi:[1,0,0]\n"
"v_fma_mix_f32 v159, v137, v164, v165 op_sel:[1,0,0] op_sel_hi:[1,0,0]\n"
"v_cvt_pk_bf16_f32 v136, v156, v157\n"
"s_add_u32 m0, s88, 110384\n"
"s_add_u32 s66, s91, 2048\n"
"buffer_load_dwordx4 v175, s[44:47], s66 offen lds\n"
"v_mfma_f32_16x16x32_bf16 v[24:27], v[246:249], v[192:195], 0\n"
"v_fma_mix_f32 v160, v138, v164, v165 op_sel_hi:[1,0,0]\n"
"v_fma_mix_f32 v161, v138, v164, v165 op_sel:[1,0,0] op_sel_hi:[1,0,0]\n"
"v_cvt_pk_bf16_f32 v137, v158, v159\n"
"v_mfma_f32_16x16x32_bf16 v[28:31], v[244:247], v[192:195], 0\n"
"v_fma_mix_f32 v162, v139, v164, v165 op_sel_hi:[1,0,0]\n"
"v_fma_mix_f32 v163, v139, v164, v165 op_sel:[1,0,0] op_sel_hi:[1,0,0]\n"
"v_cvt_pk_bf16_f32 v138, v160, v161\n"
"v_fma_mix_f32 v156, v140, v164, v165 op_sel_hi:[1,0,0]\n"
"v_fma_mix_f32 v157, v140, v164, v165 op_sel:[1,0,0] op_sel_hi:[1,0,0]\n"
"v_cvt_pk_bf16_f32 v139, v162, v163\n"
"v_mfma_f32_16x16x32_bf16 v[32:35], v[246:249], v[196:199], 0\n"
"v_fma_mix_f32 v158, v141, v164, v165 op_sel_hi:[1,0,0]\n"
"v_fma_mix_f32 v159, v141, v164, v165 op_sel:[1,0,0] op_sel_hi:[1,0,0]\n"
"v_cvt_pk_bf16_f32 v140, v156, v157\n"
"s_lshl_b32 s66, s59, 9\n"
"s_add_u32 s66, s66, 18432\n"
"v_lshrrev_b32 v176, 1, v175\n"
"v_add_u32 v176, s66, v176\n"
"v_add_u32 v177, 0x10000, v176\n"
"ds_write_b64 v176, v[228:229] offset:0\n"
"ds_write_b64 v177, v[230:231] offset:0\n"
"ds_write_b64 v176, v[232:233] offset:2048\n"
"ds_write_b64 v177, v[234:235] offset:2048\n"
"s_cmp_eq_u32 s59, 3\n"
"s_cselect_b32 exec_hi, 0, -1\n"
"ds_write_b64 v176, v[236:237] offset:4096\n"
"ds_write_b64 v177, v[238:239] offset:4096\n"
"s_mov_b64 exec, -1\n"
"v_mfma_f32_16x16x32_bf16 v[36:39], v[244:247], v[196:199], 0\n"
"v_fma_mix_f32 v160, v142, v164, v165 op_sel_hi:[1,0,0]\n"
"v_fma_mix_f32 v161, v142, v164, v165 op_sel:[1,0,0] op_sel_hi:[1,0,0]\n"
"v_cvt_pk_bf16_f32 v141, v158, v159\n"
"v_mfma_f32_16x16x32_bf16 v[40:43], v[246:249], v[200:203], 0\n"
"v_fma_mix_f32 v162, v143, v164, v165 op_sel_hi:[1,0,0]\n"
"v_fma_mix_f32 v163, v143, v164, v165 op_sel:[1,0,0] op_sel_hi:[1,0,0]\n"
"v_cvt_pk_bf16_f32 v142, v160, v161\n"
"v_mfma_f32_16x16x32_bf16 v[44:47], v[244:247], v[200:203], 0\n"
"v_cvt_pk_bf16_f32 v143, v162, v163\n"
"s_waitcnt lgkmcnt(0)\n"
"s_barrier\n"
"ds_read_b64 v[144:145], v118 offset:3072\n"
"ds_read_b64 v[146:147], v118 offset:3584\n"
"ds_read_b64 v[148:149], v118 offset:4096\n"
"ds_read_b64 v[150:151], v118 offset:4608\n"
"ds_read_b64 v[152:153], v118 offset:5120\n"
"ds_read_b64 v[154:155], v118 offset:5632\n"
"ds_read_b64 v[156:157], v118 offset:6144\n"
"ds_read_b64 v[158:159], v118 offset:6656\n"
"ds_read_b64 v[160:161], v118 offset:7168\n"
"ds_read_b64 v[162:163], v118 offset:7680\n"
"ds_read_b64 v[164:165], v118 offset:8192\n"
"ds_read_b64 v[166:167], v118 offset:8704\n"
"ds_read_b64 v[204:205], v119 offset:45968\n"
"ds_read_b64 v[206:207], v119 offset:45976\n"
"ds_read_b64 v[208:209], v119 offset:46032\n"
"ds_read_b64 v[210:211], v119 offset:46040\n"
"ds_read_b64 v[212:213], v119 offset:46096\n"
"ds_read_b64 v[214:215], v119 offset:46104\n"
"ds_read_b64 v[216:217], v119 offset:46368\n"
"ds_read_b64 v[218:219], v119 offset:46376\n"
"ds_read_b64 v[220:221], v119 offset:46432\n"
"ds_read_b64 v[222:223], v119 offset:46440\n"
"ds_read_b64 v[224:225], v119 offset:46496\n"
"ds_read_b64 v[226:227], v119 offset:46504\n"
"v_and_b32 v252, 60, v251\n"
"v_and_b32 v254, 1, v251\n"
"v_lshl_add_u32 v252, v254, 1, v252\n"
"v_bfe_u32 v254, v251, 1, 1\n"
"v_add_lshl_u32 v252, v252, v254, 3\n"
"v_add_u32 v252, 24320, v252\n"
"v_bfe_u32 v254, v251, 4, 1\n"
"v_mul_u32_u24 v253, 3080, v254\n"
"v_lshrrev_b32 v254, 5, v251\n"
"v_mul_u32_u24 v254, 768, v254\n"
"v_add_u32 v253, v253, v254\n"
"v_bfe_u32 v254, v251, 2, 2\n"
"v_mul_u32_u24 v254, 192, v254\n"
"v_add_u32 v253, v253, v254\n"
"v_and_b32 v254, 3, v251\n"
"v_lshl_add_u32 v253, v254, 4, v253\n"
"v_add_u32 v253, 108336, v253\n"
"s_waitcnt vmcnt(0)\n"
"s_waitcnt lgkmcnt(0)\n"
"s_barrier\n"
"ds_read_b64_tr_b16 v[180:181], v252 offset:0\n"
"ds_read_b64_tr_b16 v[182:183], v252 offset:3072\n"
"ds_read_b64_tr_b16 v[184:185], v252 offset:1024\n"
"ds_read_b64_tr_b16 v[186:187], v252 offset:4096\n"
"ds_read_b64_tr_b16 v[188:189], v252 offset:2048\n"
"ds_read_b64_tr_b16 v[190:191], v252 offset:5120\n"
"ds_read_b64_tr_b16 v[192:193], v252 offset:512\n"
"ds_read_b64_tr_b16 v[194:195], v252 offset:3584\n"
"ds_read_b64_tr_b16 v[196:197], v252 offset:1536\n"
"ds_read_b64_tr_b16 v[198:199], v252 offset:4608\n"
"ds_read_b64_tr_b16 v[200:201], v252 offset:2560\n"
"ds_read_b64_tr_b16 v[202:203], v252 offset:5632\n"
"v_mfma_f32_16x16x32_f16 v[96:99], v[144:147], v[144:147], 0\n"
"v_mfma_f32_16x16x32_f16 v[96:99], v[148:151], v[148:151], v[96:99]\n"
"v_mfma_f32_16x16x32_f16 v[96:99], v[152:155], v[152:155], v[96:99]\n"
"v_mfma_f32_16x16x32_f16 v[100:103], v[240:243], v[144:147], 0\n"
"v_mfma_f32_16x16x32_f16 v[100:103], v[240:243], v[148:151], v[100:103]\n"
"v_mfma_f32_16x16x32_f16 v[100:103], v[240:243], v[152:155], v[100:103]\n"
"v_mfma_f32_16x16x32_f16 v[106:109], v[156:159], v[156:159], 0\n"
"v_mfma_f32_16x16x32_f16 v[106:109], v[160:163], v[160:163], v[106:109]\n"
"v_mfma_f32_16x16x32_f16 v[106:109], v[164:167], v[164:167], v[106:109]\n"
"v_mfma_f32_16x16x32_f16 v[110:113], v[240:243], v[156:159], 0\n"
"v_mfma_f32_16x16x32_f16 v[110:113], v[240:243], v[160:163], v[110:113]\n"
"v_mfma_f32_16x16x32_f16 v[110:113], v[240:243], v[164:167], v[110:113]\n"
"s_mul_i32 s88, s93, 6160\n"
"s_add_u32 s88, s88, s90\n"
"s_add_u32 m0, s88, 120656\n"
"s_add_u32 s66, s91, 12288\n"
"buffer_load_dwordx4 v175, s[44:47], s66 offen lds\n"
"v_cndmask_b32 v104, 0, v96, s[72:73]\n"
"v_cndmask_b32 v114, 0, v106, s[72:73]\n"
"v_cndmask_b32 v104, v104, v97, s[74:75]\n"
"v_cndmask_b32 v114, v114, v107, s[74:75]\n"
"v_cndmask_b32 v104, v104, v98, s[76:77]\n"
"v_cndmask_b32 v114, v114, v108, s[76:77]\n"
"v_cndmask_b32 v104, v104, v99, s[78:79]\n"
"v_cndmask_b32 v114, v114, v109, s[78:79]\n"
"v_mul_f32 v100, s82, v100\n"
"v_mul_f32 v110, s82, v110\n"
"v_fma_f32 v101, -v100, v100, s83\n"
"v_fma_f32 v111, -v110, v110, s83\n"
"v_mfma_f32_16x16x4_f32 v[96:99], v250, v104, 0\n"
"v_mfma_f32_16x16x4_f32 v[106:109], v250, v114, 0\n"
"s_add_u32 m0, s88, 121680\n"
"s_add_u32 s66, s91, 13312\n"
"buffer_load_dwordx4 v175, s[44:47], s66 offen lds\n"
"s_nop 7\n"
"v_fma_f32 v104, v96, s82, v101\n"
"v_fma_f32 v114, v106, s82, v111\n"
"v_mul_f32 v96, s71, v96\n"
"v_mul_f32 v106, s71, v106\n"
"v_cmp_nge_f32 s[54:55], v104, v96\n"
"v_cmp_nge_f32 vcc, v114, v106\n"
"v_rsq_f32 v104, v104\n"
"v_rsq_f32 v114, v114\n"
"s_or_b64 s[80:81], s[80:81], s[54:55]\n"
"s_or_b64 s[80:81], s[80:81], vcc\n"
"v_mul_f32 v104, s84, v104\n"
"v_mul_f32 v114, s84, v114\n"
"v_mul_f32 v105, -v100, v104\n"
"v_mul_f32 v115, -v110, v114\n"
"s_add_u32 m0, s88, 122704\n"
"s_add_u32 s66, s91, 14336\n"
"buffer_load_dwordx4 v175, s[44:47], s66 offen lds\n"
"v_fma_mix_f32 v96, v144, v104, v105 op_sel_hi:[1,0,0]\n"
"v_fma_mix_f32 v97, v144, v104, v105 op_sel:[1,0,0] op_sel_hi:[1,0,0]\n"
"v_fma_mix_f32 v106, v156, v114, v115 op_sel_hi:[1,0,0]\n"
"v_fma_mix_f32 v107, v156, v114, v115 op_sel:[1,0,0] op_sel_hi:[1,0,0]\n"
"v_fma_mix_f32 v98, v145, v104, v105 op_sel_hi:[1,0,0]\n"
"v_fma_mix_f32 v99, v145, v104, v105 op_sel:[1,0,0] op_sel_hi:[1,0,0]\n"
"v_cvt_pk_bf16_f32 v144, v96, v97\n"
"v_fma_mix_f32 v108, v157, v114, v115 op_sel_hi:[1,0,0]\n"
"v_fma_mix_f32 v109, v157, v114, v115 op_sel:[1,0,0] op_sel_hi:[1,0,0]\n"
"v_cvt_pk_bf16_f32 v156, v106, v107\n"
"v_fma_mix_f32 v100, v146, v104, v105 op_sel_hi:[1,0,0]\n"
"v_fma_mix_f32 v101, v146, v104, v105 op_sel:[1,0,0] op_sel_hi:[1,0,0]\n"
"v_cvt_pk_bf16_f32 v145, v98, v99\n"
"s_mul_i32 s88, s93, 6160\n"
"s_add_u32 s88, s88, s90\n"
"s_add_u32 m0, s88, 132976\n"
"s_add_u32 s66, s91, 24576\n"
"buffer_load_dwordx4 v175, s[44:47], s66 offen lds\n"
"v_fma_mix_f32 v110, v158, v114, v115 op_sel_hi:[1,0,0]\n"
"v_fma_mix_f32 v111, v158, v114, v115 op_sel:[1,0,0] op_sel_hi:[1,0,0]\n"
"v_cvt_pk_bf16_f32 v157, v108, v109\n"
"v_fma_mix_f32 v102, v147, v104, v105 op_sel_hi:[1,0,0]\n"
"v_fma_mix_f32 v103, v147, v104, v105 op_sel:[1,0,0] op_sel_hi:[1,0,0]\n"
"v_cvt_pk_bf16_f32 v146, v100, v101\n"
"v_fma_mix_f32 v112, v159, v114, v115 op_sel_hi:[1,0,0]\n"
"v_fma_mix_f32 v113, v159, v114, v115 op_sel:[1,0,0] op_sel_hi:[1,0,0]\n"
"v_cvt_pk_bf16_f32 v158, v110, v111\n"
"v_fma_mix_f32 v96, v148, v104, v105 op_sel_hi:[1,0,0]\n"
"v_fma_mix_f32 v97, v148, v104, v105 op_sel:[1,0,0] op_sel_hi:[1,0,0]\n"
"v_cvt_pk_bf16_f32 v147, v102, v103\n"
"v_fma_mix_f32 v106, v160, v114, v115 op_sel_hi:[1,0,0]\n"
"v_fma_mix_f32 v107, v160, v114, v115 op_sel:[1,0,0] op_sel_hi:[1,0,0]\n"
"v_cvt_pk_bf16_f32 v159, v112, v113\n"
"v_fma_mix_f32 v98, v149, v104, v105 op_sel_hi:[1,0,0]\n"
"v_fma_mix_f32 v99, v149, v104, v105 op_sel:[1,0,0] op_sel_hi:[1,0,0]\n"
"v_cvt_pk_bf16_f32 v148, v96, v97\n"
"v_fma_mix_f32 v108, v161, v114, v115 op_sel_hi:[1,0,0]\n"
"v_fma_mix_f32 v109, v161, v114, v115 op_sel:[1,0,0] op_sel_hi:[1,0,0]\n"
"v_cvt_pk_bf16_f32 v160, v106, v107\n"
"s_add_u32 m0, s88, 134000\n"
"s_add_u32 s66, s91, 25600\n"
"buffer_load_dwordx4 v175, s[44:47], s66 offen lds\n"
"v_fma_mix_f32 v100, v150, v104, v105 op_sel_hi:[1,0,0]\n"
"v_fma_mix_f32 v101, v150, v104, v105 op_sel:[1,0,0] op_sel_hi:[1,0,0]\n"
"v_cvt_pk_bf16_f32 v149, v98, v99\n"
"v_fma_mix_f32 v110, v162, v114, v115 op_sel_hi:[1,0,0]\n"
"v_fma_mix_f32 v111, v162, v114, v115 op_sel:[1,0,0] op_sel_hi:[1,0,0]\n"
"v_cvt_pk_bf16_f32 v161, v108, v109\n"
"v_fma_mix_f32 v102, v151, v104, v105 op_sel_hi:[1,0,0]\n"
"v_fma_mix_f32 v103, v151, v104, v105 op_sel:[1,0,0] op_sel_hi:[1,0,0]\n"
"v_cvt_pk_bf16_f32 v150, v100, v101\n"
"v_fma_mix_f32 v112, v163, v114, v115 op_sel_hi:[1,0,0]\n"
"v_fma_mix_f32 v113, v163, v114, v115 op_sel:[1,0,0] op_sel_hi:[1,0,0]\n"
"v_cvt_pk_bf16_f32 v162, v110, v111\n"
"v_fma_mix_f32 v96, v152, v104, v105 op_sel_hi:[1,0,0]\n"
"v_fma_mix_f32 v97, v152, v104, v105 op_sel:[1,0,0] op_sel_hi:[1,0,0]\n"
"v_cvt_pk_bf16_f32 v151, v102, v103\n"
"v_fma_mix_f32 v106, v164, v114, v115 op_sel_hi:[1,0,0]\n"
"v_fma_mix_f32 v107, v164, v114, v115 op_sel:[1,0,0] op_sel_hi:[1,0,0]\n"
"v_cvt_pk_bf16_f32 v163, v112, v113\n"
"s_add_u32 m0, s88, 135024\n"
"s_add_u32 s66, s91, 26624\n"
"buffer_load_dwordx4 v175, s[44:47], s66 offen lds\n"
"v_fma_mix_f32 v98, v153, v104, v105 op_sel_hi:[1,0,0]\n"
"v_fma_mix_f32 v99, v153, v104, v105 op_sel:[1,0,0] op_sel_hi:[1,0,0]\n"
"v_cvt_pk_bf16_f32 v152, v96, v97\n"
"v_fma_mix_f32 v108, v165, v114, v115 op_sel_hi:[1,0,0]\n"
"v_fma_mix_f32 v109, v165, v114, v115 op_sel:[1,0,0] op_sel_hi:[1,0,0]\n"
"v_cvt_pk_bf16_f32 v164, v106, v107\n"
"v_fma_mix_f32 v100, v154, v104, v105 op_sel_hi:[1,0,0]\n"
"v_fma_mix_f32 v101, v154, v104, v105 op_sel:[1,0,0] op_sel_hi:[1,0,0]\n"
"v_cvt_pk_bf16_f32 v153, v98, v99\n"
"v_fma_mix_f32 v110, v166, v114, v115 op_sel_hi:[1,0,0]\n"
"v_fma_mix_f32 v111, v166, v114, v115 op_sel:[1,0,0] op_sel_hi:[1,0,0]\n"
"v_cvt_pk_bf16_f32 v165, v108, v109\n"
"v_fma_mix_f32 v102, v155, v104, v105 op_sel_hi:[1,0,0]\n"
"v_fma_mix_f32 v103, v155, v104, v105 op_sel:[1,0,0] op_sel_hi:[1,0,0]\n"
"v_cvt_pk_bf16_f32 v154, v100, v101\n"
"v_fma_mix_f32 v112, v167, v114, v115 op_sel_hi:[1,0,0]\n"
"v_fma_mix_f32 v113, v167, v114, v115 op_sel:[1,0,0] op_sel_hi:[1,0,0]\n"
"v_cvt_pk_bf16_f32 v166, v110, v111\n"
"v_cvt_pk_bf16_f32 v155, v102, v103\n"
"v_cvt_pk_bf16_f32 v167, v112, v113\n"
"s_waitcnt lgkmcnt(6)\n"
"v_mfma_f32_16x16x32_bf16 v[228:231], v[180:183], v[120:123], 0\n"
"s_setprio 1\n"
"v_mfma_f32_16x16x32_bf16 v[228:231], v[184:187], v[124:127], v[228:231]\n"
"s_mul_i32 s87, s93, 6144\n"
"s_add_u32 s87, s87, s89\n"
"s_add_u32 m0, s87, 61184\n"
"s_add_u32 s66, s92, 384\n"
"v_mfma_f32_16x16x32_bf16 v[228:231], v[188:191], v[128:131], v[228:231]\n"
"buffer_load_dwordx4 v174, s[40:43], s66 offen lds\n"
"s_waitcnt lgkmcnt(0)\n"
"v_mfma_f32_16x16x32_bf16 v[232:235], v[192:195], v[120:123], 0\n"
"v_mfma_f32_16x16x32_bf16 v[232:235], v[196:199], v[124:127], v[232:235]\n"
"s_add_u32 m0, s87, 62208\n"
"s_add_u32 s66, s92, 24960\n"
"v_mfma_f32_16x16x32_bf16 v[232:235], v[200:203], v[128:131], v[232:235]\n"
"buffer_load_dwordx4 v174, s[40:43], s66 offen lds\n"
"v_mfma_f32_16x16x32_bf16 v[48:51], v[246:249], v[204:207], 0\n"
"v_mfma_f32_16x16x32_bf16 v[52:55], v[244:247], v[204:207], 0\n"
"s_add_u32 m0, s87, 63232\n"
"s_add_u32 s66, s92, 49536\n"
"v_mfma_f32_16x16x32_bf16 v[56:59], v[246:249], v[208:211], 0\n"
"buffer_load_dwordx4 v174, s[40:43], s66 offen lds\n"
"v_lshlrev_b32_sdwa v228, 1, v228 dst_sel:DWORD dst_unused:UNUSED_PAD src0_sel:DWORD src1_sel:WORD_1\n"
"v_mul_f32_e32 v234, s64, v234\n"
"v_mfma_f32_16x16x32_bf16 v[60:63], v[244:247], v[208:211], 0\n"
"ds_read_u16_d16 v228, v228 offset:18432\n"
"v_mul_f32_e32 v235, s64, v235\n"
"v_lshlrev_b32_sdwa v229, 1, v229 dst_sel:DWORD dst_unused:UNUSED_PAD src0_sel:DWORD src1_sel:WORD_1\n"
"v_mfma_f32_16x16x32_bf16 v[64:67], v[246:249], v[212:215], 0\n"
"v_fma_f32 v254, -v234, v234, s62\n"
"v_fma_f32 v255, -v235, v235, s62\n"
"s_mul_i32 s88, s93, 6160\n"
"s_add_u32 s88, s88, s90\n"
"s_add_u32 m0, s88, 145296\n"
"s_add_u32 s66, s91, 36864\n"
"v_mfma_f32_16x16x32_bf16 v[68:71], v[244:247], v[212:215], 0\n"
"buffer_load_dwordx4 v175, s[44:47], s66 offen lds\n"
"ds_read_u16_d16_hi v229, v229 offset:18432\n"
"v_mul_f32_e32 v254, v254, v234\n"
"v_lshlrev_b32_sdwa v230, 1, v230 dst_sel:DWORD dst_unused:UNUSED_PAD src0_sel:DWORD src1_sel:WORD_1\n"
"s_waitcnt vmcnt(4)\n"
"v_mov_b32 v236, 163776\n"
"v_mov_b32 v237, 1\n"
"s_mov_b64 exec, 1\n"
"ds_add_u32 v236, v237\n"
"s_mov_b64 exec, -1\n"
"v_mfma_f32_16x16x32_bf16 v[236:239], v[180:183], v[132:135], 0\n"
"ds_read_u16_d16 v230, v230 offset:18432\n"
"v_mul_f32_e32 v255, v255, v235\n"
"s_setprio 1\n"
"v_mfma_f32_16x16x32_bf16 v[236:239], v[184:187], v[136:139], v[236:239]\n"
"v_exp_f32_e32 v254, v254\n"
"v_lshlrev_b32_sdwa v231, 1, v231 dst_sel:DWORD dst_unused:UNUSED_PAD src0_sel:DWORD src1_sel:WORD_1\n"
"s_add_u32 m0, s88, 146320\n"
"s_add_u32 s66, s91, 37888\n"
"v_mfma_f32_16x16x32_bf16 v[236:239], v[188:191], v[140:143], v[236:239]\n"
"buffer_load_dwordx4 v175, s[44:47], s66 offen lds\n"
"v_exp_f32_e32 v255, v255\n"
"v_mfma_f32_16x16x32_bf16 v[240:243], v[192:195], v[132:135], 0\n"
"ds_read_u16_d16_hi v231, v231 offset:18432\n"
"v_fma_f32 v254, v254, s63, s63\n"
"v_lshlrev_b32_sdwa v232, 1, v232 dst_sel:DWORD dst_unused:UNUSED_PAD src0_sel:DWORD src1_sel:WORD_1\n"
"v_mfma_f32_16x16x32_bf16 v[240:243], v[196:199], v[136:139], v[240:243]\n"
"ds_read_u16_d16 v232, v232 offset:18432\n"
"v_fma_f32 v255, v255, s63, s63\n"
"s_add_u32 m0, s88, 147344\n"
"s_add_u32 s66, s91, 38912\n"
"v_mfma_f32_16x16x32_bf16 v[240:243], v[200:203], v[140:143], v[240:243]\n"
"buffer_load_dwordx4 v175, s[44:47], s66 offen lds\n"
"v_rcp_f32_e32 v254, v254\n"
"v_lshlrev_b32_sdwa v233, 1, v233 dst_sel:DWORD dst_unused:UNUSED_PAD src0_sel:DWORD src1_sel:WORD_1\n"
"v_mfma_f32_16x16x32_bf16 v[72:75], v[246:249], v[216:219], 0\n"
"ds_read_b64_tr_b16 v[204:205], v253 offset:0\n"
"ds_read_b64_tr_b16 v[206:207], v253 offset:1536\n"
"v_rcp_f32_e32 v255, v255\n"
"v_mfma_f32_16x16x32_bf16 v[76:79], v[244:247], v[216:219], 0\n"
"ds_read_b64_tr_b16 v[208:209], v253 offset:8\n"
"ds_read_b64_tr_b16 v[210:211], v253 offset:1544\n"
"ds_read_b64_tr_b16 v[216:217], v253 offset:72\n"
"ds_read_b64_tr_b16 v[218:219], v253 offset:1608\n"
"ds_read_u16_d16_hi v233, v233 offset:18432\n"
"v_mul_f32_e32 v234, v254, v234\n"
"v_mul_f32_e32 v235, v255, v235\n"
"s_mul_i32 s87, s93, -73472\n"
"s_add_u32 s87, s87, s89\n"
"s_add_u32 m0, s87, 73472\n"
"s_add_u32 s66, s92, 512\n"
"v_mfma_f32_16x16x32_bf16 v[80:83], v[246:249], v[220:223], 0\n"
"buffer_load_dwordx4 v174, s[40:43], s66 offen lds\n"
"ds_read_b64_tr_b16 v[212:213], v253 offset:64\n"
"ds_read_b64_tr_b16 v[214:215], v253 offset:1600\n"
"v_lshlrev_b32_sdwa v236, 1, v236 dst_sel:DWORD dst_unused:UNUSED_PAD src0_sel:DWORD src1_sel:WORD_1\n"
"v_mul_f32_e32 v241, s64, v241\n"
"v_mul_f32_e32 v242, s64, v242\n"
"v_mfma_f32_16x16x32_bf16 v[84:87], v[244:247], v[220:223], 0\n"
"ds_read_b64_tr_b16 v[220:221], v253 offset:128\n"
"ds_read_b64_tr_b16 v[222:223], v253 offset:1664\n"
"ds_read_u16_d16 v236, v236 offset:18432\n"
"v_mul_f32_e32 v243, s64, v243\n"
"v_fma_f32 v168, -v241, v241, s62\n"
"v_mfma_f32_16x16x32_bf16 v[88:91], v[246:249], v[224:227], 0\n"
"v_lshlrev_b32_sdwa v237, 1, v237 dst_sel:DWORD dst_unused:UNUSED_PAD src0_sel:DWORD src1_sel:WORD_1\n"
"v_fma_f32 v169, -v242, v242, s62\n"
"v_fma_f32 v170, -v243, v243, s62\n"
"s_add_u32 m0, s87, 74496\n"
"s_add_u32 s66, s92, 25088\n"
"v_mfma_f32_16x16x32_bf16 v[92:95], v[244:247], v[224:227], 0\n"
"buffer_load_dwordx4 v174, s[40:43], s66 offen lds\n"
"ds_read_b64_tr_b16 v[224:225], v253 offset:136\n"
"ds_read_b64_tr_b16 v[226:227], v253 offset:1672\n"
"ds_read_u16_d16_hi v237, v237 offset:18432\n"
"v_mul_f32_e32 v168, v168, v241\n"
"v_mul_f32_e32 v169, v169, v242\n"
"s_setprio 0\n"
"v_mfma_f32_16x16x32_bf16 v[244:247], v[180:183], v[144:147], 0\n"
"ds_read_b64_tr_b16 v[180:181], v252 offset:6144\n"
"ds_read_b64_tr_b16 v[182:183], v252 offset:9216\n"
"v_lshlrev_b32_sdwa v238, 1, v238 dst_sel:DWORD dst_unused:UNUSED_PAD src0_sel:DWORD src1_sel:WORD_1\n"
"v_mul_f32_e32 v170, v170, v243\n"
"v_exp_f32_e32 v168, v168\n"
"s_setprio 1\n"
"v_mfma_f32_16x16x32_bf16 v[244:247], v[184:187], v[148:151], v[244:247]\n"
"ds_read_b64_tr_b16 v[184:185], v252 offset:7168\n"
"ds_read_b64_tr_b16 v[186:187], v252 offset:10240\n"
"s_waitcnt lgkmcnt(12)\n"
"ds_read_u16_d16 v238, v238 offset:18432\n"
"v_or_b32 v228, v228, v229\n"
"v_exp_f32_e32 v169, v169\n"
"s_add_u32 m0, s87, 75520\n"
"s_add_u32 s66, s92, 49664\n"
"v_mfma_f32_16x16x32_bf16 v[244:247], v[188:191], v[152:155], v[244:247]\n"
"buffer_load_dwordx4 v174, s[40:43], s66 offen lds\n"
"ds_read_b64_tr_b16 v[188:189], v252 offset:8192\n"
"ds_read_b64_tr_b16 v[190:191], v252 offset:11264\n"
"v_or_b32 v229, v230, v231\n"
"v_exp_f32_e32 v170, v170\n"
"v_lshlrev_b32_sdwa v239, 1, v239 dst_sel:DWORD dst_unused:UNUSED_PAD src0_sel:DWORD src1_sel:WORD_1\n"
"v_mfma_f32_16x16x32_bf16 v[248:251], v[192:195], v[144:147], 0\n"
"ds_read_b64_tr_b16 v[192:193], v252 offset:6656\n"
"ds_read_b64_tr_b16 v[194:195], v252 offset:9728\n"
"ds_read_u16_d16_hi v239, v239 offset:18432\n"
"v_or_b32 v230, v232, v233\n"
"v_fma_f32 v168, v168, s63, s63\n"
"v_fma_f32 v169, v169, s63, s63\n"
"v_mfma_f32_16x16x32_bf16 v[248:251], v[196:199], v[148:151], v[248:251]\n"
"ds_read_b64_tr_b16 v[196:197], v252 offset:7680\n"
"ds_read_b64_tr_b16 v[198:199], v252 offset:10752\n"
"v_cvt_pk_bf16_f32 v231, v234, v235\n"
"v_fma_f32 v170, v170, s63, s63\n"
"v_rcp_f32_e32 v168, v168\n"
"s_mul_i32 s88, s93, -67760\n"
"s_add_u32 s88, s88, s90\n"
"s_add_u32 m0, s88, 157616\n"
"s_add_u32 s66, s91, 49152\n"
"v_mfma_f32_16x16x32_bf16 v[248:251], v[200:203], v[152:155], v[248:251]\n"
"buffer_load_dwordx4 v175, s[44:47], s66 offen lds\n"
"ds_read_b64_tr_b16 v[200:201], v252 offset:8704\n"
"ds_read_b64_tr_b16 v[202:203], v252 offset:11776\n"
"v_lshlrev_b32_sdwa v240, 1, v240 dst_sel:DWORD dst_unused:UNUSED_PAD src0_sel:DWORD src1_sel:WORD_1\n"
"v_rcp_f32_e32 v169, v169\n"
"s_waitcnt lgkmcnt(14)\n"
"v_mfma_f32_16x16x32_bf16 v[0:3], v[204:207], v[228:231], v[0:3]\n"
"ds_read_u16_d16_hi v240, v240 offset:18432\n"
"v_rcp_f32_e32 v170, v170\n"
"v_mfma_f32_16x16x32_bf16 v[4:7], v[208:211], v[228:231], v[4:7]\n"
"v_mul_f32_e32 v241, v168, v241\n"
"v_mul_f32_e32 v242, v169, v242\n"
"v_mul_f32_e32 v243, v170, v243\n"
"s_add_u32 m0, s88, 158640\n"
"s_add_u32 s66, s91, 50176\n"
"v_mfma_f32_16x16x32_bf16 v[8:11], v[212:215], v[228:231], v[8:11]\n"
"buffer_load_dwordx4 v175, s[44:47], s66 offen lds\n"
"v_lshlrev_b32_sdwa v244, 1, v244 dst_sel:DWORD dst_unused:UNUSED_PAD src0_sel:DWORD src1_sel:WORD_1\n"
"v_mul_f32_e32 v250, s64, v250\n"
"v_mfma_f32_16x16x32_bf16 v[12:15], v[216:219], v[228:231], v[12:15]\n"
"ds_read_u16_d16 v244, v244 offset:18432\n"
"v_mul_f32_e32 v251, s64, v251\n"
"v_lshlrev_b32_sdwa v245, 1, v245 dst_sel:DWORD dst_unused:UNUSED_PAD src0_sel:DWORD src1_sel:WORD_1\n"
"v_mfma_f32_16x16x32_bf16 v[16:19], v[220:223], v[228:231], v[16:19]\n"
"v_fma_f32 v254, -v250, v250, s62\n"
"v_fma_f32 v255, -v251, v251, s62\n"
"s_add_u32 m0, s88, 159664\n"
"s_add_u32 s66, s91, 51200\n"
"v_mfma_f32_16x16x32_bf16 v[20:23], v[224:227], v[228:231], v[20:23]\n"
"buffer_load_dwordx4 v175, s[44:47], s66 offen lds\n"
"ds_read_u16_d16_hi v245, v245 offset:18432\n"
"v_mul_f32_e32 v254, v254, v250\n"
"v_lshlrev_b32_sdwa v246, 1, v246 dst_sel:DWORD dst_unused:UNUSED_PAD src0_sel:DWORD src1_sel:WORD_1\n"
"s_waitcnt lgkmcnt(10)\n"
"v_mfma_f32_16x16x32_bf16 v[228:231], v[180:183], v[120:123], 0\n"
"ds_read_u16_d16 v246, v246 offset:18432\n"
"v_mul_f32_e32 v255, v255, v251\n"
"s_setprio 1\n"
"v_mfma_f32_16x16x32_bf16 v[228:231], v[184:187], v[124:127], v[228:231]\n"
"s_waitcnt lgkmcnt(3)\n"
"v_or_b32 v236, v236, v237\n"
"v_exp_f32_e32 v254, v254\n"
"v_lshlrev_b32_sdwa v247, 1, v247 dst_sel:DWORD dst_unused:UNUSED_PAD src0_sel:DWORD src1_sel:WORD_1\n"
"s_mul_i32 s87, s93, 6144\n"
"s_add_u32 s87, s87, s89\n"
"s_add_u32 m0, s87, 6144\n"
"s_add_u32 s66, s92, 640\n"
"v_mfma_f32_16x16x32_bf16 v[228:231], v[188:191], v[128:131], v[228:231]\n"
"buffer_load_dwordx4 v174, s[40:43], s66 offen lds\n"
"v_or_b32 v237, v238, v239\n"
"v_exp_f32_e32 v255, v255\n"
"v_mfma_f32_16x16x32_bf16 v[232:235], v[192:195], v[120:123], 0\n"
"ds_read_u16_d16_hi v247, v247 offset:18432\n"
"v_cvt_pk_bf16_f32 v238, v240, v241\n"
"v_fma_f32 v254, v254, s63, s63\n"
"v_lshlrev_b32_sdwa v248, 1, v248 dst_sel:DWORD dst_unused:UNUSED_PAD src0_sel:DWORD src1_sel:WORD_1\n"
"v_mfma_f32_16x16x32_bf16 v[232:235], v[196:199], v[124:127], v[232:235]\n"
"ds_read_u16_d16 v248, v248 offset:18432\n"
"v_cvt_pk_bf16_f32 v239, v242, v243\n"
"v_fma_f32 v255, v255, s63, s63\n"
"s_add_u32 m0, s87, 7168\n"
"s_add_u32 s66, s92, 25216\n"
"v_mfma_f32_16x16x32_bf16 v[232:235], v[200:203], v[128:131], v[232:235]\n"
"buffer_load_dwordx4 v174, s[40:43], s66 offen lds\n"
"v_rcp_f32_e32 v254, v254\n"
"v_lshlrev_b32_sdwa v249, 1, v249 dst_sel:DWORD dst_unused:UNUSED_PAD src0_sel:DWORD src1_sel:WORD_1\n"
"v_mfma_f32_16x16x32_bf16 v[24:27], v[204:207], v[236:239], v[24:27]\n"
"v_rcp_f32_e32 v255, v255\n"
"v_mfma_f32_16x16x32_bf16 v[28:31], v[208:211], v[236:239], v[28:31]\n"
"ds_read_u16_d16_hi v249, v249 offset:18432\n"
"v_mul_f32_e32 v250, v254, v250\n"
"v_mul_f32_e32 v251, v255, v251\n"
"s_add_u32 m0, s87, 8192\n"
"s_add_u32 s66, s92, 49792\n"
"v_mfma_f32_16x16x32_bf16 v[32:35], v[212:215], v[236:239], v[32:35]\n"
"buffer_load_dwordx4 v174, s[40:43], s66 offen lds\n"
"v_lshlrev_b32_sdwa v228, 1, v228 dst_sel:DWORD dst_unused:UNUSED_PAD src0_sel:DWORD src1_sel:WORD_1\n"
"v_mul_f32_e32 v233, s64, v233\n"
"v_mul_f32_e32 v234, s64, v234\n"
"v_mfma_f32_16x16x32_bf16 v[36:39], v[216:219], v[236:239], v[36:39]\n"
"ds_read_u16_d16 v228, v228 offset:18432\n"
"v_mul_f32_e32 v235, s64, v235\n"
"v_fma_f32 v168, -v233, v233, s62\n"
"v_mfma_f32_16x16x32_bf16 v[40:43], v[220:223], v[236:239], v[40:43]\n"
"v_lshlrev_b32_sdwa v229, 1, v229 dst_sel:DWORD dst_unused:UNUSED_PAD src0_sel:DWORD src1_sel:WORD_1\n"
"v_fma_f32 v169, -v234, v234, s62\n"
"v_fma_f32 v170, -v235, v235, s62\n"
"s_mul_i32 s88, s93, 6160\n"
"s_add_u32 s88, s88, s90\n"
"s_add_u32 m0, s88, 96016\n"
"s_add_u32 s66, s91, 61440\n"
"v_mfma_f32_16x16x32_bf16 v[44:47], v[224:227], v[236:239], v[44:47]\n"
"buffer_load_dwordx4 v175, s[44:47], s66 offen lds\n"
"ds_read_u16_d16_hi v229, v229 offset:18432\n"
"v_mul_f32_e32 v168, v168, v233\n"
"v_mul_f32_e32 v169, v169, v234\n"
"s_setprio 0\n"
"v_mfma_f32_16x16x32_bf16 v[236:239], v[180:183], v[132:135], 0\n"
"v_lshlrev_b32_sdwa v230, 1, v230 dst_sel:DWORD dst_unused:UNUSED_PAD src0_sel:DWORD src1_sel:WORD_1\n"
"v_mul_f32_e32 v170, v170, v235\n"
"v_exp_f32_e32 v168, v168\n"
"s_setprio 1\n"
"v_mfma_f32_16x16x32_bf16 v[236:239], v[184:187], v[136:139], v[236:239]\n"
"s_waitcnt lgkmcnt(2)\n"
"ds_read_u16_d16 v230, v230 offset:18432\n"
"v_or_b32 v244, v244, v245\n"
"v_exp_f32_e32 v169, v169\n"
"s_add_u32 m0, s88, 97040\n"
"s_add_u32 s66, s91, 62464\n"
"v_mfma_f32_16x16x32_bf16 v[236:239], v[188:191], v[140:143], v[236:239]\n"
"buffer_load_dwordx4 v175, s[44:47], s66 offen lds\n"
"v_or_b32 v245, v246, v247\n"
"v_exp_f32_e32 v170, v170\n"
"v_lshlrev_b32_sdwa v231, 1, v231 dst_sel:DWORD dst_unused:UNUSED_PAD src0_sel:DWORD src1_sel:WORD_1\n"
"v_mfma_f32_16x16x32_bf16 v[240:243], v[192:195], v[132:135], 0\n"
"ds_read_u16_d16_hi v231, v231 offset:18432\n"
"v_or_b32 v246, v248, v249\n"
"v_fma_f32 v168, v168, s63, s63\n"
"v_fma_f32 v169, v169, s63, s63\n"
"v_mfma_f32_16x16x32_bf16 v[240:243], v[196:199], v[136:139], v[240:243]\n"
"v_cvt_pk_bf16_f32 v247, v250, v251\n"
"v_fma_f32 v170, v170, s63, s63\n"
"v_rcp_f32_e32 v168, v168\n"
"s_add_u32 m0, s88, 98064\n"
"s_add_u32 s66, s91, 63488\n"
"v_mfma_f32_16x16x32_bf16 v[240:243], v[200:203], v[140:143], v[240:243]\n"
"buffer_load_dwordx4 v175, s[44:47], s66 offen lds\n"
"v_lshlrev_b32_sdwa v232, 1, v232 dst_sel:DWORD dst_unused:UNUSED_PAD src0_sel:DWORD src1_sel:WORD_1\n"
"v_rcp_f32_e32 v169, v169\n"
"v_mfma_f32_16x16x32_bf16 v[48:51], v[204:207], v[244:247], v[48:51]\n"
"ds_read_b64_tr_b16 v[204:205], v253 offset:6160\n"
"ds_read_b64_tr_b16 v[206:207], v253 offset:7696\n"
"ds_read_u16_d16_hi v232, v232 offset:18432\n"
"v_rcp_f32_e32 v170, v170\n"
"v_mfma_f32_16x16x32_bf16 v[52:55], v[208:211], v[244:247], v[52:55]\n"
"ds_read_b64_tr_b16 v[208:209], v253 offset:6168\n"
"ds_read_b64_tr_b16 v[210:211], v253 offset:7704\n"
"v_mul_f32_e32 v233, v168, v233\n"
"v_mul_f32_e32 v234, v169, v234\n"
"v_mul_f32_e32 v235, v170, v235\n"
"v_mfma_f32_16x16x32_bf16 v[56:59], v[212:215], v[244:247], v[56:59]\n"
"ds_read_b64_tr_b16 v[212:213], v253 offset:6224\n"
"ds_read_b64_tr_b16 v[214:215], v253 offset:7760\n"
"v_lshlrev_b32_sdwa v236, 1, v236 dst_sel:DWORD dst_unused:UNUSED_PAD src0_sel:DWORD src1_sel:WORD_1\n"
"v_mul_f32_e32 v242, s64, v242\n"
"v_mfma_f32_16x16x32_bf16 v[60:63], v[216:219], v[244:247], v[60:63]\n"
"ds_read_b64_tr_b16 v[216:217], v253 offset:6232\n"
"ds_read_b64_tr_b16 v[218:219], v253 offset:7768\n"
"ds_read_u16_d16 v236, v236 offset:18432\n"
"v_mul_f32_e32 v243, s64, v243\n"
"v_lshlrev_b32_sdwa v237, 1, v237 dst_sel:DWORD dst_unused:UNUSED_PAD src0_sel:DWORD src1_sel:WORD_1\n"
"v_mfma_f32_16x16x32_bf16 v[64:67], v[220:223], v[244:247], v[64:67]\n"
"ds_read_b64_tr_b16 v[220:221], v253 offset:6288\n"
"ds_read_b64_tr_b16 v[222:223], v253 offset:7824\n"
"v_fma_f32 v254, -v242, v242, s62\n"
"v_fma_f32 v255, -v243, v243, s62\n"
"v_mfma_f32_16x16x32_bf16 v[68:71], v[224:227], v[244:247], v[68:71]\n"
"ds_read_b64_tr_b16 v[224:225], v253 offset:6296\n"
"ds_read_b64_tr_b16 v[226:227], v253 offset:7832\n"
"ds_read_u16_d16_hi v237, v237 offset:18432\n"
"v_mul_f32_e32 v254, v254, v242\n"
"v_lshlrev_b32_sdwa v238, 1, v238 dst_sel:DWORD dst_unused:UNUSED_PAD src0_sel:DWORD src1_sel:WORD_1\n"
"v_mfma_f32_16x16x32_bf16 v[244:247], v[180:183], v[144:147], 0\n"
"ds_read_u16_d16 v238, v238 offset:18432\n"
"v_mul_f32_e32 v255, v255, v243\n"
"s_setprio 1\n"
"v_mfma_f32_16x16x32_bf16 v[244:247], v[184:187], v[148:151], v[244:247]\n"
"s_waitcnt lgkmcnt(13)\n"
"v_or_b32 v228, v228, v229\n"
"v_exp_f32_e32 v254, v254\n"
"v_lshlrev_b32_sdwa v239, 1, v239 dst_sel:DWORD dst_unused:UNUSED_PAD src0_sel:DWORD src1_sel:WORD_1\n"
"v_mfma_f32_16x16x32_bf16 v[244:247], v[188:191], v[152:155], v[244:247]\n"
"v_or_b32 v229, v230, v231\n"
"v_exp_f32_e32 v255, v255\n"
"v_mfma_f32_16x16x32_bf16 v[248:251], v[192:195], v[144:147], 0\n"
"ds_read_u16_d16_hi v239, v239 offset:18432\n"
"v_cvt_pk_bf16_f32 v230, v232, v233\n"
"v_fma_f32 v254, v254, s63, s63\n"
"v_lshlrev_b32_sdwa v240, 1, v240 dst_sel:DWORD dst_unused:UNUSED_PAD src0_sel:DWORD src1_sel:WORD_1\n"
"v_mfma_f32_16x16x32_bf16 v[248:251], v[196:199], v[148:151], v[248:251]\n"
"ds_read_u16_d16 v240, v240 offset:18432\n"
"v_cvt_pk_bf16_f32 v231, v234, v235\n"
"v_fma_f32 v255, v255, s63, s63\n"
"v_mfma_f32_16x16x32_bf16 v[248:251], v[200:203], v[152:155], v[248:251]\n"
"v_rcp_f32_e32 v254, v254\n"
"v_lshlrev_b32_sdwa v241, 1, v241 dst_sel:DWORD dst_unused:UNUSED_PAD src0_sel:DWORD src1_sel:WORD_1\n"
"s_waitcnt lgkmcnt(11)\n"
"v_mfma_f32_16x16x32_bf16 v[0:3], v[204:207], v[228:231], v[0:3]\n"
"v_rcp_f32_e32 v255, v255\n"
"v_mfma_f32_16x16x32_bf16 v[4:7], v[208:211], v[228:231], v[4:7]\n"
"ds_read_u16_d16_hi v241, v241 offset:18432\n"
"v_mul_f32_e32 v242, v254, v242\n"
"v_mul_f32_e32 v243, v255, v243\n"
"v_mfma_f32_16x16x32_bf16 v[8:11], v[212:215], v[228:231], v[8:11]\n"
"v_lshlrev_b32_sdwa v244, 1, v244 dst_sel:DWORD dst_unused:UNUSED_PAD src0_sel:DWORD src1_sel:WORD_1\n"
"v_mul_f32_e32 v249, s64, v249\n"
"v_mul_f32_e32 v250, s64, v250\n"
"s_waitcnt lgkmcnt(5)\n"
"v_mfma_f32_16x16x32_bf16 v[12:15], v[216:219], v[228:231], v[12:15]\n"
"ds_read_u16_d16 v244, v244 offset:18432\n"
"v_mul_f32_e32 v251, s64, v251\n"
"v_fma_f32 v168, -v249, v249, s62\n"
"v_mfma_f32_16x16x32_bf16 v[16:19], v[220:223], v[228:231], v[16:19]\n"
"v_lshlrev_b32_sdwa v245, 1, v245 dst_sel:DWORD dst_unused:UNUSED_PAD src0_sel:DWORD src1_sel:WORD_1\n"
"v_fma_f32 v169, -v250, v250, s62\n"
"v_fma_f32 v170, -v251, v251, s62\n"
"v_mfma_f32_16x16x32_bf16 v[20:23], v[224:227], v[228:231], v[20:23]\n"
"ds_read_u16_d16_hi v245, v245 offset:18432\n"
"v_mul_f32_e32 v168, v168, v249\n"
"v_mul_f32_e32 v169, v169, v250\n"
"v_mov_b32 v228, 163776\n"
"s_setprio 0\n"
".Lpb_%=:\n"
"ds_read_b32 v229, v228\n"
"s_waitcnt lgkmcnt(0)\n"
"v_readfirstlane_b32 s66, v229\n"
"s_nop 1\n"
"s_cmp_lt_u32 s66, 8\n"
"s_cbranch_scc1 .Lpb_%=\n"
"v_mfma_f32_16x16x32_bf16 v[228:231], v[180:183], v[156:159], 0\n"
"ds_read_b64_tr_b16 v[180:181], v252 offset:12288\n"
"ds_read_b64_tr_b16 v[182:183], v252 offset:15360\n"
"v_lshlrev_b32_sdwa v246, 1, v246 dst_sel:DWORD dst_unused:UNUSED_PAD src0_sel:DWORD src1_sel:WORD_1\n"
"v_mul_f32_e32 v170, v170, v251\n"
"v_exp_f32_e32 v168, v168\n"
"s_setprio 1\n"
"v_mfma_f32_16x16x32_bf16 v[228:231], v[184:187], v[160:163], v[228:231]\n"
"ds_read_b64_tr_b16 v[184:185], v252 offset:13312\n"
"ds_read_b64_tr_b16 v[186:187], v252 offset:16384\n"
"ds_read_u16_d16 v246, v246 offset:18432\n"
"v_or_b32 v236, v236, v237\n"
"v_exp_f32_e32 v169, v169\n"
"v_mfma_f32_16x16x32_bf16 v[228:231], v[188:191], v[164:167], v[228:231]\n"
"ds_read_b64_tr_b16 v[188:189], v252 offset:14336\n"
"ds_read_b64_tr_b16 v[190:191], v252 offset:17408\n"
"v_or_b32 v237, v238, v239\n"
"v_exp_f32_e32 v170, v170\n"
"v_lshlrev_b32_sdwa v247, 1, v247 dst_sel:DWORD dst_unused:UNUSED_PAD src0_sel:DWORD src1_sel:WORD_1\n"
"v_mfma_f32_16x16x32_bf16 v[232:235], v[192:195], v[156:159], 0\n"
"ds_read_b64_tr_b16 v[192:193], v252 offset:12800\n"
"ds_read_b64_tr_b16 v[194:195], v252 offset:15872\n"
"ds_read_u16_d16_hi v247, v247 offset:18432\n"
"v_or_b32 v238, v240, v241\n"
"v_fma_f32 v168, v168, s63, s63\n"
"v_fma_f32 v169, v169, s63, s63\n"
"v_mfma_f32_16x16x32_bf16 v[232:235], v[196:199], v[160:163], v[232:235]\n"
"ds_read_b64_tr_b16 v[196:197], v252 offset:13824\n"
"ds_read_b64_tr_b16 v[198:199], v252 offset:16896\n"
"v_cvt_pk_bf16_f32 v239, v242, v243\n"
"v_fma_f32 v170, v170, s63, s63\n"
"v_rcp_f32_e32 v168, v168\n"
"v_mfma_f32_16x16x32_bf16 v[232:235], v[200:203], v[164:167], v[232:235]\n"
"ds_read_b64_tr_b16 v[200:201], v252 offset:14848\n"
"ds_read_b64_tr_b16 v[202:203], v252 offset:17920\n"
"v_lshlrev_b32_sdwa v248, 1, v248 dst_sel:DWORD dst_unused:UNUSED_PAD src0_sel:DWORD src1_sel:WORD_1\n"
"v_rcp_f32_e32 v169, v169\n"
"v_mfma_f32_16x16x32_bf16 v[24:27], v[204:207], v[236:239], v[24:27]\n"
"ds_read_u16_d16_hi v248, v248 offset:18432\n"
"v_rcp_f32_e32 v170, v170\n"
"v_mfma_f32_16x16x32_bf16 v[28:31], v[208:211], v[236:239], v[28:31]\n"
"v_mul_f32_e32 v249, v168, v249\n"
"v_mul_f32_e32 v250, v169, v250\n"
"v_mul_f32_e32 v251, v170, v251\n"
"v_mfma_f32_16x16x32_bf16 v[32:35], v[212:215], v[236:239], v[32:35]\n"
"v_lshlrev_b32_sdwa v228, 1, v228 dst_sel:DWORD dst_unused:UNUSED_PAD src0_sel:DWORD src1_sel:WORD_1\n"
"v_mul_f32_e32 v234, s64, v234\n"
"v_mfma_f32_16x16x32_bf16 v[36:39], v[216:219], v[236:239], v[36:39]\n"
"ds_read_u16_d16 v228, v228 offset:18432\n"
"v_mul_f32_e32 v235, s64, v235\n"
"v_lshlrev_b32_sdwa v229, 1, v229 dst_sel:DWORD dst_unused:UNUSED_PAD src0_sel:DWORD src1_sel:WORD_1\n"
"v_mfma_f32_16x16x32_bf16 v[40:43], v[220:223], v[236:239], v[40:43]\n"
"v_fma_f32 v254, -v234, v234, s62\n"
"v_fma_f32 v255, -v235, v235, s62\n"
"v_mfma_f32_16x16x32_bf16 v[44:47], v[224:227], v[236:239], v[44:47]\n"
"ds_read_u16_d16_hi v229, v229 offset:18432\n"
"v_mul_f32_e32 v254, v254, v234\n"
"v_lshlrev_b32_sdwa v230, 1, v230 dst_sel:DWORD dst_unused:UNUSED_PAD src0_sel:DWORD src1_sel:WORD_1\n"
"s_setprio 0\n"
"s_waitcnt lgkmcnt(10)\n"
"v_mfma_f32_16x16x32_bf16 v[236:239], v[180:183], v[120:123], 0\n"
"ds_read_u16_d16 v230, v230 offset:18432\n"
"v_mul_f32_e32 v255, v255, v235\n"
"s_setprio 1\n"
"v_mfma_f32_16x16x32_bf16 v[236:239], v[184:187], v[124:127], v[236:239]\n"
"s_waitcnt lgkmcnt(3)\n"
"v_or_b32 v244, v244, v245\n"
"v_exp_f32_e32 v254, v254\n"
"v_lshlrev_b32_sdwa v231, 1, v231 dst_sel:DWORD dst_unused:UNUSED_PAD src0_sel:DWORD src1_sel:WORD_1\n"
"v_mfma_f32_16x16x32_bf16 v[236:239], v[188:191], v[128:131], v[236:239]\n"
"v_or_b32 v245, v246, v247\n"
"v_exp_f32_e32 v255, v255\n"
"v_mfma_f32_16x16x32_bf16 v[240:243], v[192:195], v[120:123], 0\n"
"ds_read_u16_d16_hi v231, v231 offset:18432\n"
"v_cvt_pk_bf16_f32 v246, v248, v249\n"
"v_fma_f32 v254, v254, s63, s63\n"
"v_lshlrev_b32_sdwa v232, 1, v232 dst_sel:DWORD dst_unused:UNUSED_PAD src0_sel:DWORD src1_sel:WORD_1\n"
"v_mfma_f32_16x16x32_bf16 v[240:243], v[196:199], v[124:127], v[240:243]\n"
"ds_read_u16_d16 v232, v232 offset:18432\n"
"v_cvt_pk_bf16_f32 v247, v250, v251\n"
"v_fma_f32 v255, v255, s63, s63\n"
"v_mfma_f32_16x16x32_bf16 v[240:243], v[200:203], v[128:131], v[240:243]\n"
"v_rcp_f32_e32 v254, v254\n"
"v_lshlrev_b32_sdwa v233, 1, v233 dst_sel:DWORD dst_unused:UNUSED_PAD src0_sel:DWORD src1_sel:WORD_1\n"
"v_mfma_f32_16x16x32_bf16 v[48:51], v[204:207], v[244:247], v[48:51]\n"
"v_rcp_f32_e32 v255, v255\n"
"v_mfma_f32_16x16x32_bf16 v[52:55], v[208:211], v[244:247], v[52:55]\n"
"ds_read_u16_d16_hi v233, v233 offset:18432\n"
"v_mul_f32_e32 v234, v254, v234\n"
"v_mul_f32_e32 v235, v255, v235\n"
"v_mfma_f32_16x16x32_bf16 v[56:59], v[212:215], v[244:247], v[56:59]\n"
"v_lshlrev_b32_sdwa v236, 1, v236 dst_sel:DWORD dst_unused:UNUSED_PAD src0_sel:DWORD src1_sel:WORD_1\n"
"v_mul_f32_e32 v241, s64, v241\n"
"v_mul_f32_e32 v242, s64, v242\n"
"v_mfma_f32_16x16x32_bf16 v[60:63], v[216:219], v[244:247], v[60:63]\n"
"ds_read_u16_d16 v236, v236 offset:18432\n"
"v_mul_f32_e32 v243, s64, v243\n"
"v_fma_f32 v168, -v241, v241, s62\n"
"v_mfma_f32_16x16x32_bf16 v[64:67], v[220:223], v[244:247], v[64:67]\n"
"v_lshlrev_b32_sdwa v237, 1, v237 dst_sel:DWORD dst_unused:UNUSED_PAD src0_sel:DWORD src1_sel:WORD_1\n"
"v_fma_f32 v169, -v242, v242, s62\n"
"v_fma_f32 v170, -v243, v243, s62\n"
"v_mfma_f32_16x16x32_bf16 v[68:71], v[224:227], v[244:247], v[68:71]\n"
"ds_read_u16_d16_hi v237, v237 offset:18432\n"
"v_mul_f32_e32 v168, v168, v241\n"
"v_mul_f32_e32 v169, v169, v242\n"
"v_mfma_f32_16x16x32_bf16 v[244:247], v[180:183], v[132:135], 0\n"
"v_lshlrev_b32_sdwa v238, 1, v238 dst_sel:DWORD dst_unused:UNUSED_PAD src0_sel:DWORD src1_sel:WORD_1\n"
"v_mul_f32_e32 v170, v170, v243\n"
"v_exp_f32_e32 v168, v168\n"
"s_setprio 1\n"
"v_mfma_f32_16x16x32_bf16 v[244:247], v[184:187], v[136:139], v[244:247]\n"
"s_waitcnt lgkmcnt(2)\n"
"ds_read_u16_d16 v238, v238 offset:18432\n"
"v_or_b32 v228, v228, v229\n"
"v_exp_f32_e32 v169, v169\n"
"v_mfma_f32_16x16x32_bf16 v[244:247], v[188:191], v[140:143], v[244:247]\n"
"v_or_b32 v229, v230, v231\n"
"v_exp_f32_e32 v170, v170\n"
"v_lshlrev_b32_sdwa v239, 1, v239 dst_sel:DWORD dst_unused:UNUSED_PAD src0_sel:DWORD src1_sel:WORD_1\n"
"v_mfma_f32_16x16x32_bf16 v[248:251], v[192:195], v[132:135], 0\n"
"ds_read_u16_d16_hi v239, v239 offset:18432\n"
"v_or_b32 v230, v232, v233\n"
"v_fma_f32 v168, v168, s63, s63\n"
"v_fma_f32 v169, v169, s63, s63\n"
"v_mfma_f32_16x16x32_bf16 v[248:251], v[196:199], v[136:139], v[248:251]\n"
"v_cvt_pk_bf16_f32 v231, v234, v235\n"
"v_fma_f32 v170, v170, s63, s63\n"
"v_rcp_f32_e32 v168, v168\n"
"v_mfma_f32_16x16x32_bf16 v[248:251], v[200:203], v[140:143], v[248:251]\n"
"v_lshlrev_b32_sdwa v240, 1, v240 dst_sel:DWORD dst_unused:UNUSED_PAD src0_sel:DWORD src1_sel:WORD_1\n"
"v_rcp_f32_e32 v169, v169\n"
"v_mfma_f32_16x16x32_bf16 v[72:75], v[204:207], v[228:231], v[72:75]\n"
"ds_read_b64_tr_b16 v[204:205], v253 offset:12320\n"
"ds_read_b64_tr_b16 v[206:207], v253 offset:13856\n"
"ds_read_u16_d16_hi v240, v240 offset:18432\n"
"v_rcp_f32_e32 v170, v170\n"
"v_mfma_f32_16x16x32_bf16 v[76:79], v[208:211], v[228:231], v[76:79]\n"
"ds_read_b64_tr_b16 v[208:209], v253 offset:12328\n"
"ds_read_b64_tr_b16 v[210:211], v253 offset:13864\n"
"v_mul_f32_e32 v241, v168, v241\n"
"v_mul_f32_e32 v242, v169, v242\n"
"v_mul_f32_e32 v243, v170, v243\n"
"v_mfma_f32_16x16x32_bf16 v[80:83], v[212:215], v[228:231], v[80:83]\n"
"ds_read_b64_tr_b16 v[212:213], v253 offset:12384\n"
"ds_read_b64_tr_b16 v[214:215], v253 offset:13920\n"
"v_lshlrev_b32_sdwa v244, 1, v244 dst_sel:DWORD dst_unused:UNUSED_PAD src0_sel:DWORD src1_sel:WORD_1\n"
"v_mul_f32_e32 v250, s64, v250\n"
"v_mfma_f32_16x16x32_bf16 v[84:87], v[216:219], v[228:231], v[84:87]\n"
"ds_read_b64_tr_b16 v[216:217], v253 offset:12392\n"
"ds_read_b64_tr_b16 v[218:219], v253 offset:13928\n"
"ds_read_u16_d16 v244, v244 offset:18432\n"
"v_mul_f32_e32 v251, s64, v251\n"
"v_lshlrev_b32_sdwa v245, 1, v245 dst_sel:DWORD dst_unused:UNUSED_PAD src0_sel:DWORD src1_sel:WORD_1\n"
"v_mfma_f32_16x16x32_bf16 v[88:91], v[220:223], v[228:231], v[88:91]\n"
"ds_read_b64_tr_b16 v[220:221], v253 offset:12448\n"
"ds_read_b64_tr_b16 v[222:223], v253 offset:13984\n"
"v_fma_f32 v254, -v250, v250, s62\n"
"v_fma_f32 v255, -v251, v251, s62\n"
"v_mfma_f32_16x16x32_bf16 v[92:95], v[224:227], v[228:231], v[92:95]\n"
"ds_read_b64_tr_b16 v[224:225], v253 offset:12456\n"
"ds_read_b64_tr_b16 v[226:227], v253 offset:13992\n"
"ds_read_u16_d16_hi v245, v245 offset:18432\n"
"v_mul_f32_e32 v254, v254, v250\n"
"v_lshlrev_b32_sdwa v246, 1, v246 dst_sel:DWORD dst_unused:UNUSED_PAD src0_sel:DWORD src1_sel:WORD_1\n"
"s_setprio 0\n"
"s_waitcnt vmcnt(0)\n"
"v_mov_b32 v228, 163784\n"
"v_mov_b32 v229, 1\n"
"s_mov_b64 exec, 1\n"
"ds_add_u32 v228, v229\n"
"s_mov_b64 exec, -1\n"
"v_mfma_f32_16x16x32_bf16 v[228:231], v[180:183], v[144:147], 0\n"
"ds_read_u16_d16 v246, v246 offset:18432\n"
"v_mul_f32_e32 v255, v255, v251\n"
"s_setprio 1\n"
"v_mfma_f32_16x16x32_bf16 v[228:231], v[184:187], v[148:151], v[228:231]\n"
"s_waitcnt lgkmcnt(14)\n"
"v_or_b32 v236, v236, v237\n"
"v_exp_f32_e32 v254, v254\n"
"v_lshlrev_b32_sdwa v247, 1, v247 dst_sel:DWORD dst_unused:UNUSED_PAD src0_sel:DWORD src1_sel:WORD_1\n"
"v_mfma_f32_16x16x32_bf16 v[228:231], v[188:191], v[152:155], v[228:231]\n"
"v_or_b32 v237, v238, v239\n"
"v_exp_f32_e32 v255, v255\n"
"v_mfma_f32_16x16x32_bf16 v[232:235], v[192:195], v[144:147], 0\n"
"ds_read_u16_d16_hi v247, v247 offset:18432\n"
"v_cvt_pk_bf16_f32 v238, v240, v241\n"
"v_fma_f32 v254, v254, s63, s63\n"
"v_lshlrev_b32_sdwa v248, 1, v248 dst_sel:DWORD dst_unused:UNUSED_PAD src0_sel:DWORD src1_sel:WORD_1\n"
"v_mfma_f32_16x16x32_bf16 v[232:235], v[196:199], v[148:151], v[232:235]\n"
"ds_read_u16_d16 v248, v248 offset:18432\n"
"v_cvt_pk_bf16_f32 v239, v242, v243\n"
"v_fma_f32 v255, v255, s63, s63\n"
"v_mfma_f32_16x16x32_bf16 v[232:235], v[200:203], v[152:155], v[232:235]\n"
"v_rcp_f32_e32 v254, v254\n"
"v_lshlrev_b32_sdwa v249, 1, v249 dst_sel:DWORD dst_unused:UNUSED_PAD src0_sel:DWORD src1_sel:WORD_1\n"
"s_waitcnt lgkmcnt(12)\n"
"v_mfma_f32_16x16x32_bf16 v[0:3], v[204:207], v[236:239], v[0:3]\n"
"v_rcp_f32_e32 v255, v255\n"
"v_mfma_f32_16x16x32_bf16 v[4:7], v[208:211], v[236:239], v[4:7]\n"
"ds_read_u16_d16_hi v249, v249 offset:18432\n"
"v_mul_f32_e32 v250, v254, v250\n"
"v_mul_f32_e32 v251, v255, v251\n"
"v_mfma_f32_16x16x32_bf16 v[8:11], v[212:215], v[236:239], v[8:11]\n"
"v_lshlrev_b32_sdwa v228, 1, v228 dst_sel:DWORD dst_unused:UNUSED_PAD src0_sel:DWORD src1_sel:WORD_1\n"
"v_mul_f32_e32 v233, s64, v233\n"
"v_mul_f32_e32 v234, s64, v234\n"
"s_waitcnt lgkmcnt(6)\n"
"v_mfma_f32_16x16x32_bf16 v[12:15], v[216:219], v[236:239], v[12:15]\n"
"ds_read_u16_d16 v228, v228 offset:18432\n"
"v_mul_f32_e32 v235, s64, v235\n"
"v_fma_f32 v168, -v233, v233, s62\n"
"v_mfma_f32_16x16x32_bf16 v[16:19], v[220:223], v[236:239], v[16:19]\n"
"v_lshlrev_b32_sdwa v229, 1, v229 dst_sel:DWORD dst_unused:UNUSED_PAD src0_sel:DWORD src1_sel:WORD_1\n"
"v_fma_f32 v169, -v234, v234, s62\n"
"v_fma_f32 v170, -v235, v235, s62\n"
"v_mfma_f32_16x16x32_bf16 v[20:23], v[224:227], v[236:239], v[20:23]\n"
"ds_read_u16_d16_hi v229, v229 offset:18432\n"
"v_mul_f32_e32 v168, v168, v233\n"
"v_mul_f32_e32 v169, v169, v234\n"
"v_mfma_f32_16x16x32_bf16 v[236:239], v[180:183], v[156:159], 0\n"
"ds_read_b64_tr_b16 v[180:181], v252 offset:18432\n"
"ds_read_b64_tr_b16 v[182:183], v252 offset:21504\n"
"v_lshlrev_b32_sdwa v230, 1, v230 dst_sel:DWORD dst_unused:UNUSED_PAD src0_sel:DWORD src1_sel:WORD_1\n"
"v_mul_f32_e32 v170, v170, v235\n"
"v_exp_f32_e32 v168, v168\n"
"s_setprio 1\n"
"v_mfma_f32_16x16x32_bf16 v[236:239], v[184:187], v[160:163], v[236:239]\n"
"ds_read_b64_tr_b16 v[184:185], v252 offset:19456\n"
"ds_read_b64_tr_b16 v[186:187], v252 offset:22528\n"
"s_waitcnt lgkmcnt(6)\n"
"ds_read_u16_d16 v230, v230 offset:18432\n"
"v_or_b32 v244, v244, v245\n"
"v_exp_f32_e32 v169, v169\n"
"v_mfma_f32_16x16x32_bf16 v[236:239], v[188:191], v[164:167], v[236:239]\n"
"ds_read_b64_tr_b16 v[188:189], v252 offset:20480\n"
"ds_read_b64_tr_b16 v[190:191], v252 offset:23552\n"
"v_or_b32 v245, v246, v247\n"
"v_exp_f32_e32 v170, v170\n"
"v_lshlrev_b32_sdwa v231, 1, v231 dst_sel:DWORD dst_unused:UNUSED_PAD src0_sel:DWORD src1_sel:WORD_1\n"
"v_mfma_f32_16x16x32_bf16 v[240:243], v[192:195], v[156:159], 0\n"
"ds_read_b64_tr_b16 v[192:193], v252 offset:18944\n"
"ds_read_b64_tr_b16 v[194:195], v252 offset:22016\n"
"ds_read_u16_d16_hi v231, v231 offset:18432\n"
"v_or_b32 v246, v248, v249\n"
"v_fma_f32 v168, v168, s63, s63\n"
"v_fma_f32 v169, v169, s63, s63\n"
"v_mfma_f32_16x16x32_bf16 v[240:243], v[196:199], v[160:163], v[240:243]\n"
"ds_read_b64_tr_b16 v[196:197], v252 offset:19968\n"
"ds_read_b64_tr_b16 v[198:199], v252 offset:23040\n"
"v_cvt_pk_bf16_f32 v247, v250, v251\n"
"v_fma_f32 v170, v170, s63, s63\n"
"v_rcp_f32_e32 v168, v168\n"
"v_mfma_f32_16x16x32_bf16 v[240:243], v[200:203], v[164:167], v[240:243]\n"
"ds_read_b64_tr_b16 v[200:201], v252 offset:20992\n"
"ds_read_b64_tr_b16 v[202:203], v252 offset:24064\n"
"v_lshlrev_b32_sdwa v232, 1, v232 dst_sel:DWORD dst_unused:UNUSED_PAD src0_sel:DWORD src1_sel:WORD_1\n"
"v_rcp_f32_e32 v169, v169\n"
"v_mfma_f32_16x16x32_bf16 v[24:27], v[204:207], v[244:247], v[24:27]\n"
"ds_read_u16_d16_hi v232, v232 offset:18432\n"
"v_rcp_f32_e32 v170, v170\n"
"v_mfma_f32_16x16x32_bf16 v[28:31], v[208:211], v[244:247], v[28:31]\n"
"v_mul_f32_e32 v233, v168, v233\n"
"v_mul_f32_e32 v234, v169, v234\n"
"v_mul_f32_e32 v235, v170, v235\n"
"v_mfma_f32_16x16x32_bf16 v[32:35], v[212:215], v[244:247], v[32:35]\n"
"v_lshlrev_b32_sdwa v236, 1, v236 dst_sel:DWORD dst_unused:UNUSED_PAD src0_sel:DWORD src1_sel:WORD_1\n"
"v_mul_f32_e32 v242, s64, v242\n"
"v_mfma_f32_16x16x32_bf16 v[36:39], v[216:219], v[244:247], v[36:39]\n"
"ds_read_u16_d16 v236, v236 offset:18432\n"
"v_mul_f32_e32 v243, s64, v243\n"
"v_lshlrev_b32_sdwa v237, 1, v237 dst_sel:DWORD dst_unused:UNUSED_PAD src0_sel:DWORD src1_sel:WORD_1\n"
"v_mfma_f32_16x16x32_bf16 v[40:43], v[220:223], v[244:247], v[40:43]\n"
"v_fma_f32 v254, -v242, v242, s62\n"
"v_fma_f32 v255, -v243, v243, s62\n"
"v_mfma_f32_16x16x32_bf16 v[44:47], v[224:227], v[244:247], v[44:47]\n"
"ds_read_u16_d16_hi v237, v237 offset:18432\n"
"v_mul_f32_e32 v254, v254, v242\n"
"v_lshlrev_b32_sdwa v238, 1, v238 dst_sel:DWORD dst_unused:UNUSED_PAD src0_sel:DWORD src1_sel:WORD_1\n"
"s_waitcnt lgkmcnt(10)\n"
"v_mfma_f32_16x16x32_bf16 v[244:247], v[180:183], v[120:123], 0\n"
"ds_read_u16_d16 v238, v238 offset:18432\n"
"v_mul_f32_e32 v255, v255, v243\n"
"s_setprio 1\n"
"v_mfma_f32_16x16x32_bf16 v[244:247], v[184:187], v[124:127], v[244:247]\n"
"s_waitcnt lgkmcnt(3)\n"
"v_or_b32 v228, v228, v229\n"
"v_exp_f32_e32 v254, v254\n"
"v_lshlrev_b32_sdwa v239, 1, v239 dst_sel:DWORD dst_unused:UNUSED_PAD src0_sel:DWORD src1_sel:WORD_1\n"
"v_mfma_f32_16x16x32_bf16 v[244:247], v[188:191], v[128:131], v[244:247]\n"
"v_or_b32 v229, v230, v231\n"
"v_exp_f32_e32 v255, v255\n"
"v_mfma_f32_16x16x32_bf16 v[248:251], v[192:195], v[120:123], 0\n"
"ds_read_u16_d16_hi v239, v239 offset:18432\n"
"v_cvt_pk_bf16_f32 v230, v232, v233\n"
"v_fma_f32 v254, v254, s63, s63\n"
"v_lshlrev_b32_sdwa v240, 1, v240 dst_sel:DWORD dst_unused:UNUSED_PAD src0_sel:DWORD src1_sel:WORD_1\n"
"v_mfma_f32_16x16x32_bf16 v[248:251], v[196:199], v[124:127], v[248:251]\n"
"ds_read_u16_d16 v240, v240 offset:18432\n"
"v_cvt_pk_bf16_f32 v231, v234, v235\n"
"v_fma_f32 v255, v255, s63, s63\n"
"v_mfma_f32_16x16x32_bf16 v[248:251], v[200:203], v[128:131], v[248:251]\n"
"v_rcp_f32_e32 v254, v254\n"
"v_lshlrev_b32_sdwa v241, 1, v241 dst_sel:DWORD dst_unused:UNUSED_PAD src0_sel:DWORD src1_sel:WORD_1\n"
"v_mfma_f32_16x16x32_bf16 v[48:51], v[204:207], v[228:231], v[48:51]\n"
"v_rcp_f32_e32 v255, v255\n"
"v_mfma_f32_16x16x32_bf16 v[52:55], v[208:211], v[228:231], v[52:55]\n"
"ds_read_u16_d16_hi v241, v241 offset:18432\n"
"v_mul_f32_e32 v242, v254, v242\n"
"v_mul_f32_e32 v243, v255, v243\n"
"v_mfma_f32_16x16x32_bf16 v[56:59], v[212:215], v[228:231], v[56:59]\n"
"v_lshlrev_b32_sdwa v244, 1, v244 dst_sel:DWORD dst_unused:UNUSED_PAD src0_sel:DWORD src1_sel:WORD_1\n"
"v_mul_f32_e32 v249, s64, v249\n"
"v_mul_f32_e32 v250, s64, v250\n"
"v_mfma_f32_16x16x32_bf16 v[60:63], v[216:219], v[228:231], v[60:63]\n"
"ds_read_u16_d16 v244, v244 offset:18432\n"
"v_mul_f32_e32 v251, s64, v251\n"
"v_fma_f32 v168, -v249, v249, s62\n"
"v_mfma_f32_16x16x32_bf16 v[64:67], v[220:223], v[228:231], v[64:67]\n"
"v_lshlrev_b32_sdwa v245, 1, v245 dst_sel:DWORD dst_unused:UNUSED_PAD src0_sel:DWORD src1_sel:WORD_1\n"
"v_fma_f32 v169, -v250, v250, s62\n"
"v_fma_f32 v170, -v251, v251, s62\n"
"v_mfma_f32_16x16x32_bf16 v[68:71], v[224:227], v[228:231], v[68:71]\n"
"ds_read_u16_d16_hi v245, v245 offset:18432\n"
"v_mul_f32_e32 v168, v168, v249\n"
"v_mul_f32_e32 v169, v169, v250\n"
"s_setprio 0\n"
"v_mfma_f32_16x16x32_bf16 v[228:231], v[180:183], v[132:135], 0\n"
"v_lshlrev_b32_sdwa v246, 1, v246 dst_sel:DWORD dst_unused:UNUSED_PAD src0_sel:DWORD src1_sel:WORD_1\n"
"v_mul_f32_e32 v170, v170, v251\n"
"v_exp_f32_e32 v168, v168\n"
"s_setprio 1\n"
"v_mfma_f32_16x16x32_bf16 v[228:231], v[184:187], v[136:139], v[228:231]\n"
"s_waitcnt lgkmcnt(2)\n"
"ds_read_u16_d16 v246, v246 offset:18432\n"
"v_or_b32 v236, v236, v237\n"
"v_exp_f32_e32 v169, v169\n"
"v_mfma_f32_16x16x32_bf16 v[228:231], v[188:191], v[140:143], v[228:231]\n"
"v_or_b32 v237, v238, v239\n"
"v_exp_f32_e32 v170, v170\n"
"v_lshlrev_b32_sdwa v247, 1, v247 dst_sel:DWORD dst_unused:UNUSED_PAD src0_sel:DWORD src1_sel:WORD_1\n"
"v_mfma_f32_16x16x32_bf16 v[232:235], v[192:195], v[132:135], 0\n"
"ds_read_u16_d16_hi v247, v247 offset:18432\n"
"v_or_b32 v238, v240, v241\n"
"v_fma_f32 v168, v168, s63, s63\n"
"v_fma_f32 v169, v169, s63, s63\n"
"v_mfma_f32_16x16x32_bf16 v[232:235], v[196:199], v[136:139], v[232:235]\n"
"v_cvt_pk_bf16_f32 v239, v242, v243\n"
"v_fma_f32 v170, v170, s63, s63\n"
"v_rcp_f32_e32 v168, v168\n"
"v_mfma_f32_16x16x32_bf16 v[232:235], v[200:203], v[140:143], v[232:235]\n"
"v_lshlrev_b32_sdwa v248, 1, v248 dst_sel:DWORD dst_unused:UNUSED_PAD src0_sel:DWORD src1_sel:WORD_1\n"
"v_rcp_f32_e32 v169, v169\n"
"v_mfma_f32_16x16x32_bf16 v[72:75], v[204:207], v[236:239], v[72:75]\n"
"ds_read_b64_tr_b16 v[204:205], v253 offset:18480\n"
"ds_read_b64_tr_b16 v[206:207], v253 offset:20016\n"
"ds_read_u16_d16_hi v248, v248 offset:18432\n"
"v_rcp_f32_e32 v170, v170\n"
"v_mfma_f32_16x16x32_bf16 v[76:79], v[208:211], v[236:239], v[76:79]\n"
"ds_read_b64_tr_b16 v[208:209], v253 offset:18488\n"
"ds_read_b64_tr_b16 v[210:211], v253 offset:20024\n"
"v_mul_f32_e32 v249, v168, v249\n"
"v_mul_f32_e32 v250, v169, v250\n"
"v_mul_f32_e32 v251, v170, v251\n"
"v_mfma_f32_16x16x32_bf16 v[80:83], v[212:215], v[236:239], v[80:83]\n"
"ds_read_b64_tr_b16 v[212:213], v253 offset:18544\n"
"ds_read_b64_tr_b16 v[214:215], v253 offset:20080\n"
"v_lshlrev_b32_sdwa v228, 1, v228 dst_sel:DWORD dst_unused:UNUSED_PAD src0_sel:DWORD src1_sel:WORD_1\n"
"v_mul_f32_e32 v234, s64, v234\n"
"v_mfma_f32_16x16x32_bf16 v[84:87], v[216:219], v[236:239], v[84:87]\n"
"ds_read_b64_tr_b16 v[216:217], v253 offset:18552\n"
"ds_read_b64_tr_b16 v[218:219], v253 offset:20088\n"
"ds_read_u16_d16 v228, v228 offset:18432\n"
"v_mul_f32_e32 v235, s64, v235\n"
"v_lshlrev_b32_sdwa v229, 1, v229 dst_sel:DWORD dst_unused:UNUSED_PAD src0_sel:DWORD src1_sel:WORD_1\n"
"v_mfma_f32_16x16x32_bf16 v[88:91], v[220:223], v[236:239], v[88:91]\n"
"ds_read_b64_tr_b16 v[220:221], v253 offset:18608\n"
"ds_read_b64_tr_b16 v[222:223], v253 offset:20144\n"
"v_fma_f32 v254, -v234, v234, s62\n"
"v_fma_f32 v255, -v235, v235, s62\n"
"v_mfma_f32_16x16x32_bf16 v[92:95], v[224:227], v[236:239], v[92:95]\n"
"ds_read_b64_tr_b16 v[224:225], v253 offset:18616\n"
"ds_read_b64_tr_b16 v[226:227], v253 offset:20152\n"
"ds_read_u16_d16_hi v229, v229 offset:18432\n"
"v_mul_f32_e32 v254, v254, v234\n"
"v_lshlrev_b32_sdwa v230, 1, v230 dst_sel:DWORD dst_unused:UNUSED_PAD src0_sel:DWORD src1_sel:WORD_1\n"
"v_mfma_f32_16x16x32_bf16 v[236:239], v[180:183], v[144:147], 0\n"
"ds_read_u16_d16 v230, v230 offset:18432\n"
"v_mul_f32_e32 v255, v255, v235\n"
"s_setprio 1\n"
"v_mfma_f32_16x16x32_bf16 v[236:239], v[184:187], v[148:151], v[236:239]\n"
"s_waitcnt lgkmcnt(13)\n"
"v_or_b32 v244, v244, v245\n"
"v_exp_f32_e32 v254, v254\n"
"v_lshlrev_b32_sdwa v231, 1, v231 dst_sel:DWORD dst_unused:UNUSED_PAD src0_sel:DWORD src1_sel:WORD_1\n"
"v_mfma_f32_16x16x32_bf16 v[236:239], v[188:191], v[152:155], v[236:239]\n"
"v_or_b32 v245, v246, v247\n"
"v_exp_f32_e32 v255, v255\n"
"v_mfma_f32_16x16x32_bf16 v[240:243], v[192:195], v[144:147], 0\n"
"ds_read_u16_d16_hi v231, v231 offset:18432\n"
"v_cvt_pk_bf16_f32 v246, v248, v249\n"
"v_fma_f32 v254, v254, s63, s63\n"
"v_lshlrev_b32_sdwa v232, 1, v232 dst_sel:DWORD dst_unused:UNUSED_PAD src0_sel:DWORD src1_sel:WORD_1\n"
"v_mfma_f32_16x16x32_bf16 v[240:243], v[196:199], v[148:151], v[240:243]\n"
"ds_read_u16_d16 v232, v232 offset:18432\n"
"v_cvt_pk_bf16_f32 v247, v250, v251\n"
"v_fma_f32 v255, v255, s63, s63\n"
"v_mfma_f32_16x16x32_bf16 v[240:243], v[200:203], v[152:155], v[240:243]\n"
"v_rcp_f32_e32 v254, v254\n"
"v_lshlrev_b32_sdwa v233, 1, v233 dst_sel:DWORD dst_unused:UNUSED_PAD src0_sel:DWORD src1_sel:WORD_1\n"
"s_waitcnt lgkmcnt(11)\n"
"v_mfma_f32_16x16x32_bf16 v[0:3], v[204:207], v[244:247], v[0:3]\n"
"v_rcp_f32_e32 v255, v255\n"
"v_mfma_f32_16x16x32_bf16 v[4:7], v[208:211], v[244:247], v[4:7]\n"
"ds_read_u16_d16_hi v233, v233 offset:18432\n"
"v_mul_f32_e32 v234, v254, v234\n"
"v_mul_f32_e32 v235, v255, v235\n"
"v_mfma_f32_16x16x32_bf16 v[8:11], v[212:215], v[244:247], v[8:11]\n"
"v_lshlrev_b32_sdwa v236, 1, v236 dst_sel:DWORD dst_unused:UNUSED_PAD src0_sel:DWORD src1_sel:WORD_1\n"
"v_mul_f32_e32 v241, s64, v241\n"
"v_mul_f32_e32 v242, s64, v242\n"
"s_waitcnt lgkmcnt(5)\n"
"v_mfma_f32_16x16x32_bf16 v[12:15], v[216:219], v[244:247], v[12:15]\n"
"ds_read_u16_d16 v236, v236 offset:18432\n"
"v_mul_f32_e32 v243, s64, v243\n"
"v_fma_f32 v168, -v241, v241, s62\n"
"v_mfma_f32_16x16x32_bf16 v[16:19], v[220:223], v[244:247], v[16:19]\n"
"v_lshlrev_b32_sdwa v237, 1, v237 dst_sel:DWORD dst_unused:UNUSED_PAD src0_sel:DWORD src1_sel:WORD_1\n"
"v_fma_f32 v169, -v242, v242, s62\n"
"v_fma_f32 v170, -v243, v243, s62\n"
"v_mfma_f32_16x16x32_bf16 v[20:23], v[224:227], v[244:247], v[20:23]\n"
"ds_read_u16_d16_hi v237, v237 offset:18432\n"
"v_mul_f32_e32 v168, v168, v241\n"
"v_mul_f32_e32 v169, v169, v242\n"
"s_setprio 0\n"
"v_mfma_f32_16x16x32_bf16 v[244:247], v[180:183], v[156:159], 0\n"
"ds_read_b64_tr_b16 v[180:181], v252 offset:24576\n"
"ds_read_b64_tr_b16 v[182:183], v252 offset:27648\n"
"v_lshlrev_b32_sdwa v238, 1, v238 dst_sel:DWORD dst_unused:UNUSED_PAD src0_sel:DWORD src1_sel:WORD_1\n"
"v_mul_f32_e32 v170, v170, v243\n"
"v_exp_f32_e32 v168, v168\n"
"s_setprio 1\n"
"v_mfma_f32_16x16x32_bf16 v[244:247], v[184:187], v[160:163], v[244:247]\n"
"ds_read_b64_tr_b16 v[184:185], v252 offset:25600\n"
"ds_read_b64_tr_b16 v[186:187], v252 offset:28672\n"
"s_waitcnt lgkmcnt(6)\n"
"ds_read_u16_d16 v238, v238 offset:18432\n"
"v_or_b32 v228, v228, v229\n"
"v_exp_f32_e32 v169, v169\n"
"v_mfma_f32_16x16x32_bf16 v[244:247], v[188:191], v[164:167], v[244:247]\n"
"ds_read_b64_tr_b16 v[188:189], v252 offset:26624\n"
"ds_read_b64_tr_b16 v[190:191], v252 offset:29696\n"
"v_or_b32 v229, v230, v231\n"
"v_exp_f32_e32 v170, v170\n"
"v_lshlrev_b32_sdwa v239, 1, v239 dst_sel:DWORD dst_unused:UNUSED_PAD src0_sel:DWORD src1_sel:WORD_1\n"
"v_mfma_f32_16x16x32_bf16 v[248:251], v[192:195], v[156:159], 0\n"
"ds_read_b64_tr_b16 v[192:193], v252 offset:25088\n"
"ds_read_b64_tr_b16 v[194:195], v252 offset:28160\n"
"ds_read_u16_d16_hi v239, v239 offset:18432\n"
"v_or_b32 v230, v232, v233\n"
"v_fma_f32 v168, v168, s63, s63\n"
"v_fma_f32 v169, v169, s63, s63\n"
"v_mfma_f32_16x16x32_bf16 v[248:251], v[196:199], v[160:163], v[248:251]\n"
"ds_read_b64_tr_b16 v[196:197], v252 offset:26112\n"
"ds_read_b64_tr_b16 v[198:199], v252 offset:29184\n"
"v_cvt_pk_bf16_f32 v231, v234, v235\n"
"v_fma_f32 v170, v170, s63, s63\n"
"v_rcp_f32_e32 v168, v168\n"
"v_mfma_f32_16x16x32_bf16 v[248:251], v[200:203], v[164:167], v[248:251]\n"
"ds_read_b64_tr_b16 v[200:201], v252 offset:27136\n"
"ds_read_b64_tr_b16 v[202:203], v252 offset:30208\n"
"v_lshlrev_b32_sdwa v240, 1, v240 dst_sel:DWORD dst_unused:UNUSED_PAD src0_sel:DWORD src1_sel:WORD_1\n"
"v_rcp_f32_e32 v169, v169\n"
"v_mfma_f32_16x16x32_bf16 v[24:27], v[204:207], v[228:231], v[24:27]\n"
"ds_read_u16_d16_hi v240, v240 offset:18432\n"
"v_rcp_f32_e32 v170, v170\n"
"v_mfma_f32_16x16x32_bf16 v[28:31], v[208:211], v[228:231], v[28:31]\n"
"v_mul_f32_e32 v241, v168, v241\n"
"v_mul_f32_e32 v242, v169, v242\n"
"v_mul_f32_e32 v243, v170, v243\n"
"v_mfma_f32_16x16x32_bf16 v[32:35], v[212:215], v[228:231], v[32:35]\n"
"v_lshlrev_b32_sdwa v244, 1, v244 dst_sel:DWORD dst_unused:UNUSED_PAD src0_sel:DWORD src1_sel:WORD_1\n"
"v_mul_f32_e32 v250, s64, v250\n"
"v_mfma_f32_16x16x32_bf16 v[36:39], v[216:219], v[228:231], v[36:39]\n"
"ds_read_u16_d16 v244, v244 offset:18432\n"
"v_mul_f32_e32 v251, s64, v251\n"
"v_lshlrev_b32_sdwa v245, 1, v245 dst_sel:DWORD dst_unused:UNUSED_PAD src0_sel:DWORD src1_sel:WORD_1\n"
"v_mfma_f32_16x16x32_bf16 v[40:43], v[220:223], v[228:231], v[40:43]\n"
"v_fma_f32 v254, -v250, v250, s62\n"
"v_fma_f32 v255, -v251, v251, s62\n"
"v_mfma_f32_16x16x32_bf16 v[44:47], v[224:227], v[228:231], v[44:47]\n"
"ds_read_u16_d16_hi v245, v245 offset:18432\n"
"v_mul_f32_e32 v254, v254, v250\n"
"v_lshlrev_b32_sdwa v246, 1, v246 dst_sel:DWORD dst_unused:UNUSED_PAD src0_sel:DWORD src1_sel:WORD_1\n"
"s_waitcnt lgkmcnt(10)\n"
"v_mfma_f32_16x16x32_bf16 v[228:231], v[180:183], v[120:123], 0\n"
"ds_read_u16_d16 v246, v246 offset:18432\n"
"v_mul_f32_e32 v255, v255, v251\n"
"s_setprio 1\n"
"v_mfma_f32_16x16x32_bf16 v[228:231], v[184:187], v[124:127], v[228:231]\n"
"s_waitcnt lgkmcnt(3)\n"
"v_or_b32 v236, v236, v237\n"
"v_exp_f32_e32 v254, v254\n"
"v_lshlrev_b32_sdwa v247, 1, v247 dst_sel:DWORD dst_unused:UNUSED_PAD src0_sel:DWORD src1_sel:WORD_1\n"
"v_mfma_f32_16x16x32_bf16 v[228:231], v[188:191], v[128:131], v[228:231]\n"
"v_or_b32 v237, v238, v239\n"
"v_exp_f32_e32 v255, v255\n"
"v_mfma_f32_16x16x32_bf16 v[232:235], v[192:195], v[120:123], 0\n"
"ds_read_u16_d16_hi v247, v247 offset:18432\n"
"v_cvt_pk_bf16_f32 v238, v240, v241\n"
"v_fma_f32 v254, v254, s63, s63\n"
"v_lshlrev_b32_sdwa v248, 1, v248 dst_sel:DWORD dst_unused:UNUSED_PAD src0_sel:DWORD src1_sel:WORD_1\n"
"v_mfma_f32_16x16x32_bf16 v[232:235], v[196:199], v[124:127], v[232:235]\n"
"ds_read_u16_d16 v248, v248 offset:18432\n"
"v_cvt_pk_bf16_f32 v239, v242, v243\n"
"v_fma_f32 v255, v255, s63, s63\n"
"v_mfma_f32_16x16x32_bf16 v[232:235], v[200:203], v[128:131], v[232:235]\n"
"v_rcp_f32_e32 v254, v254\n"
"v_lshlrev_b32_sdwa v249, 1, v249 dst_sel:DWORD dst_unused:UNUSED_PAD src0_sel:DWORD src1_sel:WORD_1\n"
"v_mfma_f32_16x16x32_bf16 v[48:51], v[204:207], v[236:239], v[48:51]\n"
"v_rcp_f32_e32 v255, v255\n"
"v_mfma_f32_16x16x32_bf16 v[52:55], v[208:211], v[236:239], v[52:55]\n"
"ds_read_u16_d16_hi v249, v249 offset:18432\n"
"v_mul_f32_e32 v250, v254, v250\n"
"v_mul_f32_e32 v251, v255, v251\n"
"v_mfma_f32_16x16x32_bf16 v[56:59], v[212:215], v[236:239], v[56:59]\n"
"v_lshlrev_b32_sdwa v228, 1, v228 dst_sel:DWORD dst_unused:UNUSED_PAD src0_sel:DWORD src1_sel:WORD_1\n"
"v_mul_f32_e32 v233, s64, v233\n"
"v_mul_f32_e32 v234, s64, v234\n"
"v_mfma_f32_16x16x32_bf16 v[60:63], v[216:219], v[236:239], v[60:63]\n"
"ds_read_u16_d16 v228, v228 offset:18432\n"
"v_mul_f32_e32 v235, s64, v235\n"
"v_fma_f32 v168, -v233, v233, s62\n"
"v_mfma_f32_16x16x32_bf16 v[64:67], v[220:223], v[236:239], v[64:67]\n"
"v_lshlrev_b32_sdwa v229, 1, v229 dst_sel:DWORD dst_unused:UNUSED_PAD src0_sel:DWORD src1_sel:WORD_1\n"
"v_fma_f32 v169, -v234, v234, s62\n"
"v_fma_f32 v170, -v235, v235, s62\n"
"v_mfma_f32_16x16x32_bf16 v[68:71], v[224:227], v[236:239], v[68:71]\n"
"ds_read_u16_d16_hi v229, v229 offset:18432\n"
"v_mul_f32_e32 v168, v168, v233\n"
"v_mul_f32_e32 v169, v169, v234\n"
"v_mfma_f32_16x16x32_bf16 v[236:239], v[180:183], v[132:135], 0\n"
"v_lshlrev_b32_sdwa v230, 1, v230 dst_sel:DWORD dst_unused:UNUSED_PAD src0_sel:DWORD src1_sel:WORD_1\n"
"v_mul_f32_e32 v170, v170, v235\n"
"v_exp_f32_e32 v168, v168\n"
"s_setprio 1\n"
"v_mfma_f32_16x16x32_bf16 v[236:239], v[184:187], v[136:139], v[236:239]\n"
"s_waitcnt lgkmcnt(2)\n"
"ds_read_u16_d16 v230, v230 offset:18432\n"
"v_or_b32 v244, v244, v245\n"
"v_exp_f32_e32 v169, v169\n"
"v_mfma_f32_16x16x32_bf16 v[236:239], v[188:191], v[140:143], v[236:239]\n"
"v_or_b32 v245, v246, v247\n"
"v_exp_f32_e32 v170, v170\n"
"v_lshlrev_b32_sdwa v231, 1, v231 dst_sel:DWORD dst_unused:UNUSED_PAD src0_sel:DWORD src1_sel:WORD_1\n"
"v_mfma_f32_16x16x32_bf16 v[240:243], v[192:195], v[132:135], 0\n"
"ds_read_u16_d16_hi v231, v231 offset:18432\n"
"v_or_b32 v246, v248, v249\n"
"v_fma_f32 v168, v168, s63, s63\n"
"v_fma_f32 v169, v169, s63, s63\n"
"v_mfma_f32_16x16x32_bf16 v[240:243], v[196:199], v[136:139], v[240:243]\n"
"v_cvt_pk_bf16_f32 v247, v250, v251\n"
"v_fma_f32 v170, v170, s63, s63\n"
"v_rcp_f32_e32 v168, v168\n"
"v_mfma_f32_16x16x32_bf16 v[240:243], v[200:203], v[140:143], v[240:243]\n"
"v_lshlrev_b32_sdwa v232, 1, v232 dst_sel:DWORD dst_unused:UNUSED_PAD src0_sel:DWORD src1_sel:WORD_1\n"
"v_rcp_f32_e32 v169, v169\n"
"v_mfma_f32_16x16x32_bf16 v[72:75], v[204:207], v[244:247], v[72:75]\n"
"ds_read_b64_tr_b16 v[204:205], v253 offset:24640\n"
"ds_read_b64_tr_b16 v[206:207], v253 offset:26176\n"
"ds_read_u16_d16_hi v232, v232 offset:18432\n"
"v_rcp_f32_e32 v170, v170\n"
"v_mfma_f32_16x16x32_bf16 v[76:79], v[208:211], v[244:247], v[76:79]\n"
"ds_read_b64_tr_b16 v[208:209], v253 offset:24648\n"
"ds_read_b64_tr_b16 v[210:211], v253 offset:26184\n"
"v_mul_f32_e32 v233, v168, v233\n"
"v_mul_f32_e32 v234, v169, v234\n"
"v_mul_f32_e32 v235, v170, v235\n"
"v_mfma_f32_16x16x32_bf16 v[80:83], v[212:215], v[244:247], v[80:83]\n"
"ds_read_b64_tr_b16 v[212:213], v253 offset:24704\n"
"ds_read_b64_tr_b16 v[214:215], v253 offset:26240\n"
"v_lshlrev_b32_sdwa v236, 1, v236 dst_sel:DWORD dst_unused:UNUSED_PAD src0_sel:DWORD src1_sel:WORD_1\n"
"v_mul_f32_e32 v242, s64, v242\n"
"v_mfma_f32_16x16x32_bf16 v[84:87], v[216:219], v[244:247], v[84:87]\n"
"ds_read_b64_tr_b16 v[216:217], v253 offset:24712\n"
"ds_read_b64_tr_b16 v[218:219], v253 offset:26248\n"
"ds_read_u16_d16 v236, v236 offset:18432\n"
"v_mul_f32_e32 v243, s64, v243\n"
"v_lshlrev_b32_sdwa v237, 1, v237 dst_sel:DWORD dst_unused:UNUSED_PAD src0_sel:DWORD src1_sel:WORD_1\n"
"v_mfma_f32_16x16x32_bf16 v[88:91], v[220:223], v[244:247], v[88:91]\n"
"ds_read_b64_tr_b16 v[220:221], v253 offset:24768\n"
"ds_read_b64_tr_b16 v[222:223], v253 offset:26304\n"
"v_fma_f32 v254, -v242, v242, s62\n"
"v_fma_f32 v255, -v243, v243, s62\n"
"v_mfma_f32_16x16x32_bf16 v[92:95], v[224:227], v[244:247], v[92:95]\n"
"ds_read_b64_tr_b16 v[224:225], v253 offset:24776\n"
"ds_read_b64_tr_b16 v[226:227], v253 offset:26312\n"
"ds_read_u16_d16_hi v237, v237 offset:18432\n"
"v_mul_f32_e32 v254, v254, v242\n"
"v_lshlrev_b32_sdwa v238, 1, v238 dst_sel:DWORD dst_unused:UNUSED_PAD src0_sel:DWORD src1_sel:WORD_1\n"
"s_setprio 0\n"
"v_mfma_f32_16x16x32_bf16 v[244:247], v[180:183], v[144:147], 0\n"
"ds_read_u16_d16 v238, v238 offset:18432\n"
"v_mul_f32_e32 v255, v255, v243\n"
"s_setprio 1\n"
"v_mfma_f32_16x16x32_bf16 v[244:247], v[184:187], v[148:151], v[244:247]\n"
"s_waitcnt lgkmcnt(13)\n"
"v_or_b32 v228, v228, v229\n"
"v_exp_f32_e32 v254, v254\n"
"v_lshlrev_b32_sdwa v239, 1, v239 dst_sel:DWORD dst_unused:UNUSED_PAD src0_sel:DWORD src1_sel:WORD_1\n"
"v_mfma_f32_16x16x32_bf16 v[244:247], v[188:191], v[152:155], v[244:247]\n"
"v_or_b32 v229, v230, v231\n"
"v_exp_f32_e32 v255, v255\n"
"v_mfma_f32_16x16x32_bf16 v[248:251], v[192:195], v[144:147], 0\n"
"ds_read_u16_d16_hi v239, v239 offset:18432\n"
"v_cvt_pk_bf16_f32 v230, v232, v233\n"
"v_fma_f32 v254, v254, s63, s63\n"
"v_lshlrev_b32_sdwa v240, 1, v240 dst_sel:DWORD dst_unused:UNUSED_PAD src0_sel:DWORD src1_sel:WORD_1\n"
"v_mfma_f32_16x16x32_bf16 v[248:251], v[196:199], v[148:151], v[248:251]\n"
"ds_read_u16_d16 v240, v240 offset:18432\n"
"v_cvt_pk_bf16_f32 v231, v234, v235\n"
"v_fma_f32 v255, v255, s63, s63\n"
"v_mfma_f32_16x16x32_bf16 v[248:251], v[200:203], v[152:155], v[248:251]\n"
"v_rcp_f32_e32 v254, v254\n"
"v_lshlrev_b32_sdwa v241, 1, v241 dst_sel:DWORD dst_unused:UNUSED_PAD src0_sel:DWORD src1_sel:WORD_1\n"
"s_waitcnt lgkmcnt(11)\n"
"v_mfma_f32_16x16x32_bf16 v[0:3], v[204:207], v[228:231], v[0:3]\n"
"v_rcp_f32_e32 v255, v255\n"
"v_mfma_f32_16x16x32_bf16 v[4:7], v[208:211], v[228:231], v[4:7]\n"
"ds_read_u16_d16_hi v241, v241 offset:18432\n"
"v_mul_f32_e32 v242, v254, v242\n"
"v_mul_f32_e32 v243, v255, v243\n"
"v_mfma_f32_16x16x32_bf16 v[8:11], v[212:215], v[228:231], v[8:11]\n"
"v_lshlrev_b32_sdwa v244, 1, v244 dst_sel:DWORD dst_unused:UNUSED_PAD src0_sel:DWORD src1_sel:WORD_1\n"
"v_mul_f32_e32 v249, s64, v249\n"
"v_mul_f32_e32 v250, s64, v250\n"
"s_waitcnt lgkmcnt(5)\n"
"v_mfma_f32_16x16x32_bf16 v[12:15], v[216:219], v[228:231], v[12:15]\n"
"ds_read_u16_d16 v244, v244 offset:18432\n"
"v_mul_f32_e32 v251, s64, v251\n"
"v_fma_f32 v168, -v249, v249, s62\n"
"v_mfma_f32_16x16x32_bf16 v[16:19], v[220:223], v[228:231], v[16:19]\n"
"v_lshlrev_b32_sdwa v245, 1, v245 dst_sel:DWORD dst_unused:UNUSED_PAD src0_sel:DWORD src1_sel:WORD_1\n"
"v_fma_f32 v169, -v250, v250, s62\n"
"v_fma_f32 v170, -v251, v251, s62\n"
"v_mfma_f32_16x16x32_bf16 v[20:23], v[224:227], v[228:231], v[20:23]\n"
"ds_read_u16_d16_hi v245, v245 offset:18432\n"
"v_mul_f32_e32 v168, v168, v249\n"
"v_mul_f32_e32 v169, v169, v250\n"
"v_mfma_f32_16x16x32_bf16 v[228:231], v[180:183], v[156:159], 0\n"
"ds_read_b64_tr_b16 v[180:181], v252 offset:30720\n"
"ds_read_b64_tr_b16 v[182:183], v252 offset:33792\n"
"v_lshlrev_b32_sdwa v246, 1, v246 dst_sel:DWORD dst_unused:UNUSED_PAD src0_sel:DWORD src1_sel:WORD_1\n"
"v_mul_f32_e32 v170, v170, v251\n"
"v_exp_f32_e32 v168, v168\n"
"s_setprio 1\n"
"v_mfma_f32_16x16x32_bf16 v[228:231], v[184:187], v[160:163], v[228:231]\n"
"ds_read_b64_tr_b16 v[184:185], v252 offset:31744\n"
"ds_read_b64_tr_b16 v[186:187], v252 offset:34816\n"
"s_waitcnt lgkmcnt(6)\n"
"ds_read_u16_d16 v246, v246 offset:18432\n"
"v_or_b32 v236, v236, v237\n"
"v_exp_f32_e32 v169, v169\n"
"v_mfma_f32_16x16x32_bf16 v[228:231], v[188:191], v[164:167], v[228:231]\n"
"ds_read_b64_tr_b16 v[188:189], v252 offset:32768\n"
"ds_read_b64_tr_b16 v[190:191], v252 offset:35840\n"
"v_or_b32 v237, v238, v239\n"
"v_exp_f32_e32 v170, v170\n"
"v_lshlrev_b32_sdwa v247, 1, v247 dst_sel:DWORD dst_unused:UNUSED_PAD src0_sel:DWORD src1_sel:WORD_1\n"
"v_mfma_f32_16x16x32_bf16 v[232:235], v[192:195], v[156:159], 0\n"
"ds_read_b64_tr_b16 v[192:193], v252 offset:31232\n"
"ds_read_b64_tr_b16 v[194:195], v252 offset:34304\n"
"ds_read_u16_d16_hi v247, v247 offset:18432\n"
"v_or_b32 v238, v240, v241\n"
"v_fma_f32 v168, v168, s63, s63\n"
"v_fma_f32 v169, v169, s63, s63\n"
"v_mfma_f32_16x16x32_bf16 v[232:235], v[196:199], v[160:163], v[232:235]\n"
"ds_read_b64_tr_b16 v[196:197], v252 offset:32256\n"
"ds_read_b64_tr_b16 v[198:199], v252 offset:35328\n"
"v_cvt_pk_bf16_f32 v239, v242, v243\n"
"v_fma_f32 v170, v170, s63, s63\n"
"v_rcp_f32_e32 v168, v168\n"
"v_mfma_f32_16x16x32_bf16 v[232:235], v[200:203], v[164:167], v[232:235]\n"
"ds_read_b64_tr_b16 v[200:201], v252 offset:33280\n"
"ds_read_b64_tr_b16 v[202:203], v252 offset:36352\n"
"v_lshlrev_b32_sdwa v248, 1, v248 dst_sel:DWORD dst_unused:UNUSED_PAD src0_sel:DWORD src1_sel:WORD_1\n"
"v_rcp_f32_e32 v169, v169\n"
"v_mfma_f32_16x16x32_bf16 v[24:27], v[204:207], v[236:239], v[24:27]\n"
"ds_read_u16_d16_hi v248, v248 offset:18432\n"
"v_rcp_f32_e32 v170, v170\n"
"v_mfma_f32_16x16x32_bf16 v[28:31], v[208:211], v[236:239], v[28:31]\n"
"v_mul_f32_e32 v249, v168, v249\n"
"v_mul_f32_e32 v250, v169, v250\n"
"v_mul_f32_e32 v251, v170, v251\n"
"v_mfma_f32_16x16x32_bf16 v[32:35], v[212:215], v[236:239], v[32:35]\n"
"v_lshlrev_b32_sdwa v228, 1, v228 dst_sel:DWORD dst_unused:UNUSED_PAD src0_sel:DWORD src1_sel:WORD_1\n"
"v_mul_f32_e32 v234, s64, v234\n"
"v_mfma_f32_16x16x32_bf16 v[36:39], v[216:219], v[236:239], v[36:39]\n"
"ds_read_u16_d16 v228, v228 offset:18432\n"
"v_mul_f32_e32 v235, s64, v235\n"
"v_lshlrev_b32_sdwa v229, 1, v229 dst_sel:DWORD dst_unused:UNUSED_PAD src0_sel:DWORD src1_sel:WORD_1\n"
"v_mfma_f32_16x16x32_bf16 v[40:43], v[220:223], v[236:239], v[40:43]\n"
"v_fma_f32 v254, -v234, v234, s62\n"
"v_fma_f32 v255, -v235, v235, s62\n"
"v_mfma_f32_16x16x32_bf16 v[44:47], v[224:227], v[236:239], v[44:47]\n"
"ds_read_u16_d16_hi v229, v229 offset:18432\n"
"v_mul_f32_e32 v254, v254, v234\n"
"v_lshlrev_b32_sdwa v230, 1, v230 dst_sel:DWORD dst_unused:UNUSED_PAD src0_sel:DWORD src1_sel:WORD_1\n"
"s_setprio 0\n"
"s_waitcnt lgkmcnt(10)\n"
"v_mfma_f32_16x16x32_bf16 v[236:239], v[180:183], v[120:123], 0\n"
"ds_read_u16_d16 v230, v230 offset:18432\n"
"v_mul_f32_e32 v255, v255, v235\n"
"s_setprio 1\n"
"v_mfma_f32_16x16x32_bf16 v[236:239], v[184:187], v[124:127], v[236:239]\n"
"s_waitcnt lgkmcnt(3)\n"
"v_or_b32 v244, v244, v245\n"
"v_exp_f32_e32 v254, v254\n"
"v_lshlrev_b32_sdwa v231, 1, v231 dst_sel:DWORD dst_unused:UNUSED_PAD src0_sel:DWORD src1_sel:WORD_1\n"
"v_mfma_f32_16x16x32_bf16 v[236:239], v[188:191], v[128:131], v[236:239]\n"
"v_or_b32 v245, v246, v247\n"
"v_exp_f32_e32 v255, v255\n"
"v_mfma_f32_16x16x32_bf16 v[240:243], v[192:195], v[120:123], 0\n"
"ds_read_u16_d16_hi v231, v231 offset:18432\n"
"v_cvt_pk_bf16_f32 v246, v248, v249\n"
"v_fma_f32 v254, v254, s63, s63\n"
"v_lshlrev_b32_sdwa v232, 1, v232 dst_sel:DWORD dst_unused:UNUSED_PAD src0_sel:DWORD src1_sel:WORD_1\n"
"v_mfma_f32_16x16x32_bf16 v[240:243], v[196:199], v[124:127], v[240:243]\n"
"ds_read_u16_d16 v232, v232 offset:18432\n"
"v_cvt_pk_bf16_f32 v247, v250, v251\n"
"v_fma_f32 v255, v255, s63, s63\n"
"v_mfma_f32_16x16x32_bf16 v[240:243], v[200:203], v[128:131], v[240:243]\n"
"v_rcp_f32_e32 v254, v254\n"
"v_lshlrev_b32_sdwa v233, 1, v233 dst_sel:DWORD dst_unused:UNUSED_PAD src0_sel:DWORD src1_sel:WORD_1\n"
"v_mfma_f32_16x16x32_bf16 v[48:51], v[204:207], v[244:247], v[48:51]\n"
"v_rcp_f32_e32 v255, v255\n"
"v_mfma_f32_16x16x32_bf16 v[52:55], v[208:211], v[244:247], v[52:55]\n"
"ds_read_u16_d16_hi v233, v233 offset:18432\n"
"v_mul_f32_e32 v234, v254, v234\n"
"v_mul_f32_e32 v235, v255, v235\n"
"v_mfma_f32_16x16x32_bf16 v[56:59], v[212:215], v[244:247], v[56:59]\n"
"v_lshlrev_b32_sdwa v236, 1, v236 dst_sel:DWORD dst_unused:UNUSED_PAD src0_sel:DWORD src1_sel:WORD_1\n"
"v_mul_f32_e32 v241, s64, v241\n"
"v_mul_f32_e32 v242, s64, v242\n"
"v_mfma_f32_16x16x32_bf16 v[60:63], v[216:219], v[244:247], v[60:63]\n"
"ds_read_u16_d16 v236, v236 offset:18432\n"
"v_mul_f32_e32 v243, s64, v243\n"
"v_fma_f32 v168, -v241, v241, s62\n"
"v_mfma_f32_16x16x32_bf16 v[64:67], v[220:223], v[244:247], v[64:67]\n"
"v_lshlrev_b32_sdwa v237, 1, v237 dst_sel:DWORD dst_unused:UNUSED_PAD src0_sel:DWORD src1_sel:WORD_1\n"
"v_fma_f32 v169, -v242, v242, s62\n"
"v_fma_f32 v170, -v243, v243, s62\n"
"v_mfma_f32_16x16x32_bf16 v[68:71], v[224:227], v[244:247], v[68:71]\n"
"ds_read_u16_d16_hi v237, v237 offset:18432\n"
"v_mul_f32_e32 v168, v168, v241\n"
"v_mul_f32_e32 v169, v169, v242\n"
"v_mfma_f32_16x16x32_bf16 v[244:247], v[180:183], v[132:135], 0\n"
"v_lshlrev_b32_sdwa v238, 1, v238 dst_sel:DWORD dst_unused:UNUSED_PAD src0_sel:DWORD src1_sel:WORD_1\n"
"v_mul_f32_e32 v170, v170, v243\n"
"v_exp_f32_e32 v168, v168\n"
"s_setprio 1\n"
"v_mfma_f32_16x16x32_bf16 v[244:247], v[184:187], v[136:139], v[244:247]\n"
"s_waitcnt lgkmcnt(2)\n"
"ds_read_u16_d16 v238, v238 offset:18432\n"
"v_or_b32 v228, v228, v229\n"
"v_exp_f32_e32 v169, v169\n"
"v_mfma_f32_16x16x32_bf16 v[244:247], v[188:191], v[140:143], v[244:247]\n"
"v_or_b32 v229, v230, v231\n"
"v_exp_f32_e32 v170, v170\n"
"v_lshlrev_b32_sdwa v239, 1, v239 dst_sel:DWORD dst_unused:UNUSED_PAD src0_sel:DWORD src1_sel:WORD_1\n"
"v_mfma_f32_16x16x32_bf16 v[248:251], v[192:195], v[132:135], 0\n"
"ds_read_u16_d16_hi v239, v239 offset:18432\n"
"v_or_b32 v230, v232, v233\n"
"v_fma_f32 v168, v168, s63, s63\n"
"v_fma_f32 v169, v169, s63, s63\n"
"v_mfma_f32_16x16x32_bf16 v[248:251], v[196:199], v[136:139], v[248:251]\n"
"v_cvt_pk_bf16_f32 v231, v234, v235\n"
"v_fma_f32 v170, v170, s63, s63\n"
"v_rcp_f32_e32 v168, v168\n"
"v_mfma_f32_16x16x32_bf16 v[248:251], v[200:203], v[140:143], v[248:251]\n"
"v_lshlrev_b32_sdwa v240, 1, v240 dst_sel:DWORD dst_unused:UNUSED_PAD src0_sel:DWORD src1_sel:WORD_1\n"
"v_rcp_f32_e32 v169, v169\n"
"v_mfma_f32_16x16x32_bf16 v[72:75], v[204:207], v[228:231], v[72:75]\n"
"ds_read_b64_tr_b16 v[204:205], v253 offset:30800\n"
"ds_read_b64_tr_b16 v[206:207], v253 offset:32336\n"
"ds_read_u16_d16_hi v240, v240 offset:18432\n"
"v_rcp_f32_e32 v170, v170\n"
"v_mfma_f32_16x16x32_bf16 v[76:79], v[208:211], v[228:231], v[76:79]\n"
"ds_read_b64_tr_b16 v[208:209], v253 offset:30808\n"
"ds_read_b64_tr_b16 v[210:211], v253 offset:32344\n"
"v_mul_f32_e32 v241, v168, v241\n"
"v_mul_f32_e32 v242, v169, v242\n"
"v_mul_f32_e32 v243, v170, v243\n"
"v_mfma_f32_16x16x32_bf16 v[80:83], v[212:215], v[228:231], v[80:83]\n"
"ds_read_b64_tr_b16 v[212:213], v253 offset:30864\n"
"ds_read_b64_tr_b16 v[214:215], v253 offset:32400\n"
"v_lshlrev_b32_sdwa v244, 1, v244 dst_sel:DWORD dst_unused:UNUSED_PAD src0_sel:DWORD src1_sel:WORD_1\n"
"v_mul_f32_e32 v250, s64, v250\n"
"v_mfma_f32_16x16x32_bf16 v[84:87], v[216:219], v[228:231], v[84:87]\n"
"ds_read_b64_tr_b16 v[216:217], v253 offset:30872\n"
"ds_read_b64_tr_b16 v[218:219], v253 offset:32408\n"
"ds_read_u16_d16 v244, v244 offset:18432\n"
"v_mul_f32_e32 v251, s64, v251\n"
"v_lshlrev_b32_sdwa v245, 1, v245 dst_sel:DWORD dst_unused:UNUSED_PAD src0_sel:DWORD src1_sel:WORD_1\n"
"v_mfma_f32_16x16x32_bf16 v[88:91], v[220:223], v[228:231], v[88:91]\n"
"ds_read_b64_tr_b16 v[220:221], v253 offset:30928\n"
"ds_read_b64_tr_b16 v[222:223], v253 offset:32464\n"
"v_fma_f32 v254, -v250, v250, s62\n"
"v_fma_f32 v255, -v251, v251, s62\n"
"v_mfma_f32_16x16x32_bf16 v[92:95], v[224:227], v[228:231], v[92:95]\n"
"ds_read_b64_tr_b16 v[224:225], v253 offset:30936\n"
"ds_read_b64_tr_b16 v[226:227], v253 offset:32472\n"
"ds_read_u16_d16_hi v245, v245 offset:18432\n"
"v_mul_f32_e32 v254, v254, v250\n"
"v_lshlrev_b32_sdwa v246, 1, v246 dst_sel:DWORD dst_unused:UNUSED_PAD src0_sel:DWORD src1_sel:WORD_1\n"
"v_mfma_f32_16x16x32_bf16 v[228:231], v[180:183], v[144:147], 0\n"
"ds_read_u16_d16 v246, v246 offset:18432\n"
"v_mul_f32_e32 v255, v255, v251\n"
"s_setprio 1\n"
"v_mfma_f32_16x16x32_bf16 v[228:231], v[184:187], v[148:151], v[228:231]\n"
"s_waitcnt lgkmcnt(13)\n"
"v_or_b32 v236, v236, v237\n"
"v_exp_f32_e32 v254, v254\n"
"v_lshlrev_b32_sdwa v247, 1, v247 dst_sel:DWORD dst_unused:UNUSED_PAD src0_sel:DWORD src1_sel:WORD_1\n"
"v_mfma_f32_16x16x32_bf16 v[228:231], v[188:191], v[152:155], v[228:231]\n"
"v_or_b32 v237, v238, v239\n"
"v_exp_f32_e32 v255, v255\n"
"v_mfma_f32_16x16x32_bf16 v[232:235], v[192:195], v[144:147], 0\n"
"ds_read_u16_d16_hi v247, v247 offset:18432\n"
"v_cvt_pk_bf16_f32 v238, v240, v241\n"
"v_fma_f32 v254, v254, s63, s63\n"
"v_lshlrev_b32_sdwa v248, 1, v248 dst_sel:DWORD dst_unused:UNUSED_PAD src0_sel:DWORD src1_sel:WORD_1\n"
"v_mfma_f32_16x16x32_bf16 v[232:235], v[196:199], v[148:151], v[232:235]\n"
"ds_read_u16_d16 v248, v248 offset:18432\n"
"v_cvt_pk_bf16_f32 v239, v242, v243\n"
"v_fma_f32 v255, v255, s63, s63\n"
"v_mfma_f32_16x16x32_bf16 v[232:235], v[200:203], v[152:155], v[232:235]\n"
"v_rcp_f32_e32 v254, v254\n"
"v_lshlrev_b32_sdwa v249, 1, v249 dst_sel:DWORD dst_unused:UNUSED_PAD src0_sel:DWORD src1_sel:WORD_1\n"
"s_waitcnt lgkmcnt(11)\n"
"v_mfma_f32_16x16x32_bf16 v[0:3], v[204:207], v[236:239], v[0:3]\n"
"v_rcp_f32_e32 v255, v255\n"
"v_mfma_f32_16x16x32_bf16 v[4:7], v[208:211], v[236:239], v[4:7]\n"
"ds_read_u16_d16_hi v249, v249 offset:18432\n"
"v_mul_f32_e32 v250, v254, v250\n"
"v_mul_f32_e32 v251, v255, v251\n"
"v_mfma_f32_16x16x32_bf16 v[8:11], v[212:215], v[236:239], v[8:11]\n"
"v_lshlrev_b32_sdwa v228, 1, v228 dst_sel:DWORD dst_unused:UNUSED_PAD src0_sel:DWORD src1_sel:WORD_1\n"
"v_mul_f32_e32 v233, s64, v233\n"
"v_mul_f32_e32 v234, s64, v234\n"
"s_waitcnt lgkmcnt(5)\n"
"v_mfma_f32_16x16x32_bf16 v[12:15], v[216:219], v[236:239], v[12:15]\n"
"ds_read_u16_d16 v228, v228 offset:18432\n"
"v_mul_f32_e32 v235, s64, v235\n"
"v_fma_f32 v168, -v233, v233, s62\n"
"v_mfma_f32_16x16x32_bf16 v[16:19], v[220:223], v[236:239], v[16:19]\n"
"v_lshlrev_b32_sdwa v229, 1, v229 dst_sel:DWORD dst_unused:UNUSED_PAD src0_sel:DWORD src1_sel:WORD_1\n"
"v_fma_f32 v169, -v234, v234, s62\n"
"v_fma_f32 v170, -v235, v235, s62\n"
"v_mfma_f32_16x16x32_bf16 v[20:23], v[224:227], v[236:239], v[20:23]\n"
"ds_read_u16_d16_hi v229, v229 offset:18432\n"
"v_mul_f32_e32 v168, v168, v233\n"
"v_mul_f32_e32 v169, v169, v234\n"
"s_setprio 0\n"
"v_mov_b32 v236, 163784\n"
"s_setprio 0\n"
".Lqb_%=:\n"
"ds_read_b32 v237, v236\n"
"s_waitcnt lgkmcnt(0)\n"
"v_readfirstlane_b32 s66, v237\n"
"s_nop 1\n"
"s_cmp_lt_u32 s66, 4\n"
"s_cbranch_scc1 .Lqb_%=\n"
"v_mfma_f32_16x16x32_bf16 v[236:239], v[180:183], v[156:159], 0\n"
"ds_read_b64_tr_b16 v[180:181], v252 offset:36864\n"
"ds_read_b64_tr_b16 v[182:183], v252 offset:39936\n"
"v_lshlrev_b32_sdwa v230, 1, v230 dst_sel:DWORD dst_unused:UNUSED_PAD src0_sel:DWORD src1_sel:WORD_1\n"
"v_mul_f32_e32 v170, v170, v235\n"
"v_exp_f32_e32 v168, v168\n"
"s_setprio 1\n"
"v_mfma_f32_16x16x32_bf16 v[236:239], v[184:187], v[160:163], v[236:239]\n"
"ds_read_b64_tr_b16 v[184:185], v252 offset:37888\n"
"ds_read_b64_tr_b16 v[186:187], v252 offset:40960\n"
"ds_read_u16_d16 v230, v230 offset:18432\n"
"v_or_b32 v244, v244, v245\n"
"v_exp_f32_e32 v169, v169\n"
"v_mfma_f32_16x16x32_bf16 v[236:239], v[188:191], v[164:167], v[236:239]\n"
"ds_read_b64_tr_b16 v[188:189], v252 offset:38912\n"
"ds_read_b64_tr_b16 v[190:191], v252 offset:41984\n"
"v_or_b32 v245, v246, v247\n"
"v_exp_f32_e32 v170, v170\n"
"v_lshlrev_b32_sdwa v231, 1, v231 dst_sel:DWORD dst_unused:UNUSED_PAD src0_sel:DWORD src1_sel:WORD_1\n"
"v_mfma_f32_16x16x32_bf16 v[240:243], v[192:195], v[156:159], 0\n"
"ds_read_b64_tr_b16 v[192:193], v252 offset:37376\n"
"ds_read_b64_tr_b16 v[194:195], v252 offset:40448\n"
"ds_read_u16_d16_hi v231, v231 offset:18432\n"
"v_or_b32 v246, v248, v249\n"
"v_fma_f32 v168, v168, s63, s63\n"
"v_fma_f32 v169, v169, s63, s63\n"
"v_mfma_f32_16x16x32_bf16 v[240:243], v[196:199], v[160:163], v[240:243]\n"
"ds_read_b64_tr_b16 v[196:197], v252 offset:38400\n"
"ds_read_b64_tr_b16 v[198:199], v252 offset:41472\n"
"v_cvt_pk_bf16_f32 v247, v250, v251\n"
"v_fma_f32 v170, v170, s63, s63\n"
"v_rcp_f32_e32 v168, v168\n"
"v_mfma_f32_16x16x32_bf16 v[240:243], v[200:203], v[164:167], v[240:243]\n"
"ds_read_b64_tr_b16 v[200:201], v252 offset:39424\n"
"ds_read_b64_tr_b16 v[202:203], v252 offset:42496\n"
"v_lshlrev_b32_sdwa v232, 1, v232 dst_sel:DWORD dst_unused:UNUSED_PAD src0_sel:DWORD src1_sel:WORD_1\n"
"v_rcp_f32_e32 v169, v169\n"
"v_mfma_f32_16x16x32_bf16 v[24:27], v[204:207], v[244:247], v[24:27]\n"
"ds_read_u16_d16_hi v232, v232 offset:18432\n"
"v_rcp_f32_e32 v170, v170\n"
"v_mfma_f32_16x16x32_bf16 v[28:31], v[208:211], v[244:247], v[28:31]\n"
"v_mul_f32_e32 v233, v168, v233\n"
"v_mul_f32_e32 v234, v169, v234\n"
"v_mul_f32_e32 v235, v170, v235\n"
"v_mfma_f32_16x16x32_bf16 v[32:35], v[212:215], v[244:247], v[32:35]\n"
"v_lshlrev_b32_sdwa v236, 1, v236 dst_sel:DWORD dst_unused:UNUSED_PAD src0_sel:DWORD src1_sel:WORD_1\n"
"v_mul_f32_e32 v242, s64, v242\n"
"v_mfma_f32_16x16x32_bf16 v[36:39], v[216:219], v[244:247], v[36:39]\n"
"ds_read_u16_d16 v236, v236 offset:18432\n"
"v_mul_f32_e32 v243, s64, v243\n"
"v_lshlrev_b32_sdwa v237, 1, v237 dst_sel:DWORD dst_unused:UNUSED_PAD src0_sel:DWORD src1_sel:WORD_1\n"
"v_mfma_f32_16x16x32_bf16 v[40:43], v[220:223], v[244:247], v[40:43]\n"
"v_fma_f32 v254, -v242, v242, s62\n"
"v_fma_f32 v255, -v243, v243, s62\n"
"v_mfma_f32_16x16x32_bf16 v[44:47], v[224:227], v[244:247], v[44:47]\n"
"ds_read_u16_d16_hi v237, v237 offset:18432\n"
"v_mul_f32_e32 v254, v254, v242\n"
"v_lshlrev_b32_sdwa v238, 1, v238 dst_sel:DWORD dst_unused:UNUSED_PAD src0_sel:DWORD src1_sel:WORD_1\n"
"s_waitcnt lgkmcnt(10)\n"
"v_mfma_f32_16x16x32_bf16 v[244:247], v[180:183], v[120:123], 0\n"
"ds_read_u16_d16 v238, v238 offset:18432\n"
"v_mul_f32_e32 v255, v255, v243\n"
"s_setprio 1\n"
"v_mfma_f32_16x16x32_bf16 v[244:247], v[184:187], v[124:127], v[244:247]\n"
"s_waitcnt lgkmcnt(3)\n"
"v_or_b32 v228, v228, v229\n"
"v_exp_f32_e32 v254, v254\n"
"v_lshlrev_b32_sdwa v239, 1, v239 dst_sel:DWORD dst_unused:UNUSED_PAD src0_sel:DWORD src1_sel:WORD_1\n"
"v_mfma_f32_16x16x32_bf16 v[244:247], v[188:191], v[128:131], v[244:247]\n"
"v_or_b32 v229, v230, v231\n"
"v_exp_f32_e32 v255, v255\n"
"v_mfma_f32_16x16x32_bf16 v[248:251], v[192:195], v[120:123], 0\n"
"ds_read_u16_d16_hi v239, v239 offset:18432\n"
"v_cvt_pk_bf16_f32 v230, v232, v233\n"
"v_fma_f32 v254, v254, s63, s63\n"
"v_lshlrev_b32_sdwa v240, 1, v240 dst_sel:DWORD dst_unused:UNUSED_PAD src0_sel:DWORD src1_sel:WORD_1\n"
"v_mfma_f32_16x16x32_bf16 v[248:251], v[196:199], v[124:127], v[248:251]\n"
"ds_read_u16_d16 v240, v240 offset:18432\n"
"v_cvt_pk_bf16_f32 v231, v234, v235\n"
"v_fma_f32 v255, v255, s63, s63\n"
"v_mfma_f32_16x16x32_bf16 v[248:251], v[200:203], v[128:131], v[248:251]\n"
"v_rcp_f32_e32 v254, v254\n"
"v_lshlrev_b32_sdwa v241, 1, v241 dst_sel:DWORD dst_unused:UNUSED_PAD src0_sel:DWORD src1_sel:WORD_1\n"
"v_mfma_f32_16x16x32_bf16 v[48:51], v[204:207], v[228:231], v[48:51]\n"
"v_rcp_f32_e32 v255, v255\n"
"v_mfma_f32_16x16x32_bf16 v[52:55], v[208:211], v[228:231], v[52:55]\n"
"ds_read_u16_d16_hi v241, v241 offset:18432\n"
"v_mul_f32_e32 v242, v254, v242\n"
"v_mul_f32_e32 v243, v255, v243\n"
"v_mfma_f32_16x16x32_bf16 v[56:59], v[212:215], v[228:231], v[56:59]\n"
"v_lshlrev_b32_sdwa v244, 1, v244 dst_sel:DWORD dst_unused:UNUSED_PAD src0_sel:DWORD src1_sel:WORD_1\n"
"v_mul_f32_e32 v249, s64, v249\n"
"v_mul_f32_e32 v250, s64, v250\n"
"v_mfma_f32_16x16x32_bf16 v[60:63], v[216:219], v[228:231], v[60:63]\n"
"ds_read_u16_d16 v244, v244 offset:18432\n"
"v_mul_f32_e32 v251, s64, v251\n"
"v_fma_f32 v168, -v249, v249, s62\n"
"v_mfma_f32_16x16x32_bf16 v[64:67], v[220:223], v[228:231], v[64:67]\n"
"v_lshlrev_b32_sdwa v245, 1, v245 dst_sel:DWORD dst_unused:UNUSED_PAD src0_sel:DWORD src1_sel:WORD_1\n"
"v_fma_f32 v169, -v250, v250, s62\n"
"v_fma_f32 v170, -v251, v251, s62\n"
"v_mfma_f32_16x16x32_bf16 v[68:71], v[224:227], v[228:231], v[68:71]\n"
"ds_read_u16_d16_hi v245, v245 offset:18432\n"
"v_mul_f32_e32 v168, v168, v249\n"
"v_mul_f32_e32 v169, v169, v250\n"
"s_setprio 0\n"
"v_mfma_f32_16x16x32_bf16 v[228:231], v[180:183], v[132:135], 0\n"
"v_lshlrev_b32_sdwa v246, 1, v246 dst_sel:DWORD dst_unused:UNUSED_PAD src0_sel:DWORD src1_sel:WORD_1\n"
"v_mul_f32_e32 v170, v170, v251\n"
"v_exp_f32_e32 v168, v168\n"
"s_setprio 1\n"
"v_mfma_f32_16x16x32_bf16 v[228:231], v[184:187], v[136:139], v[228:231]\n"
"s_waitcnt lgkmcnt(2)\n"
"ds_read_u16_d16 v246, v246 offset:18432\n"
"v_or_b32 v236, v236, v237\n"
"v_exp_f32_e32 v169, v169\n"
"v_mfma_f32_16x16x32_bf16 v[228:231], v[188:191], v[140:143], v[228:231]\n"
"v_or_b32 v237, v238, v239\n"
"v_exp_f32_e32 v170, v170\n"
"v_lshlrev_b32_sdwa v247, 1, v247 dst_sel:DWORD dst_unused:UNUSED_PAD src0_sel:DWORD src1_sel:WORD_1\n"
"v_mfma_f32_16x16x32_bf16 v[232:235], v[192:195], v[132:135], 0\n"
"ds_read_u16_d16_hi v247, v247 offset:18432\n"
"v_or_b32 v238, v240, v241\n"
"v_fma_f32 v168, v168, s63, s63\n"
"v_fma_f32 v169, v169, s63, s63\n"
"v_mfma_f32_16x16x32_bf16 v[232:235], v[196:199], v[136:139], v[232:235]\n"
"v_cvt_pk_bf16_f32 v239, v242, v243\n"
"v_fma_f32 v170, v170, s63, s63\n"
"v_rcp_f32_e32 v168, v168\n"
"v_mfma_f32_16x16x32_bf16 v[232:235], v[200:203], v[140:143], v[232:235]\n"
"v_lshlrev_b32_sdwa v248, 1, v248 dst_sel:DWORD dst_unused:UNUSED_PAD src0_sel:DWORD src1_sel:WORD_1\n"
"v_rcp_f32_e32 v169, v169\n"
"v_mfma_f32_16x16x32_bf16 v[72:75], v[204:207], v[236:239], v[72:75]\n"
"ds_read_b64_tr_b16 v[204:205], v253 offset:36960\n"
"ds_read_b64_tr_b16 v[206:207], v253 offset:38496\n"
"ds_read_u16_d16_hi v248, v248 offset:18432\n"
"v_rcp_f32_e32 v170, v170\n"
"v_mfma_f32_16x16x32_bf16 v[76:79], v[208:211], v[236:239], v[76:79]\n"
"ds_read_b64_tr_b16 v[208:209], v253 offset:36968\n"
"ds_read_b64_tr_b16 v[210:211], v253 offset:38504\n"
"v_mul_f32_e32 v249, v168, v249\n"
"v_mul_f32_e32 v250, v169, v250\n"
"v_mul_f32_e32 v251, v170, v251\n"
"v_mfma_f32_16x16x32_bf16 v[80:83], v[212:215], v[236:239], v[80:83]\n"
"ds_read_b64_tr_b16 v[212:213], v253 offset:37024\n"
"ds_read_b64_tr_b16 v[214:215], v253 offset:38560\n"
"v_lshlrev_b32_sdwa v228, 1, v228 dst_sel:DWORD dst_unused:UNUSED_PAD src0_sel:DWORD src1_sel:WORD_1\n"
"v_mul_f32_e32 v234, s64, v234\n"
"v_mfma_f32_16x16x32_bf16 v[84:87], v[216:219], v[236:239], v[84:87]\n"
"ds_read_b64_tr_b16 v[216:217], v253 offset:37032\n"
"ds_read_b64_tr_b16 v[218:219], v253 offset:38568\n"
"ds_read_u16_d16 v228, v228 offset:18432\n"
"v_mul_f32_e32 v235, s64, v235\n"
"v_lshlrev_b32_sdwa v229, 1, v229 dst_sel:DWORD dst_unused:UNUSED_PAD src0_sel:DWORD src1_sel:WORD_1\n"
"v_mfma_f32_16x16x32_bf16 v[88:91], v[220:223], v[236:239], v[88:91]\n"
"ds_read_b64_tr_b16 v[220:221], v253 offset:37088\n"
"ds_read_b64_tr_b16 v[222:223], v253 offset:38624\n"
"v_fma_f32 v254, -v234, v234, s62\n"
"v_fma_f32 v255, -v235, v235, s62\n"
"v_mfma_f32_16x16x32_bf16 v[92:95], v[224:227], v[236:239], v[92:95]\n"
"ds_read_b64_tr_b16 v[224:225], v253 offset:37096\n"
"ds_read_b64_tr_b16 v[226:227], v253 offset:38632\n"
"ds_read_u16_d16_hi v229, v229 offset:18432\n"
"v_mul_f32_e32 v254, v254, v234\n"
"v_lshlrev_b32_sdwa v230, 1, v230 dst_sel:DWORD dst_unused:UNUSED_PAD src0_sel:DWORD src1_sel:WORD_1\n"
"v_mfma_f32_16x16x32_bf16 v[236:239], v[180:183], v[144:147], 0\n"
"ds_read_u16_d16 v230, v230 offset:18432\n"
"v_mul_f32_e32 v255, v255, v235\n"
"s_setprio 1\n"
"v_mfma_f32_16x16x32_bf16 v[236:239], v[184:187], v[148:151], v[236:239]\n"
"s_waitcnt lgkmcnt(13)\n"
"v_or_b32 v244, v244, v245\n"
"v_exp_f32_e32 v254, v254\n"
"v_lshlrev_b32_sdwa v231, 1, v231 dst_sel:DWORD dst_unused:UNUSED_PAD src0_sel:DWORD src1_sel:WORD_1\n"
"v_mfma_f32_16x16x32_bf16 v[236:239], v[188:191], v[152:155], v[236:239]\n"
"v_or_b32 v245, v246, v247\n"
"v_exp_f32_e32 v255, v255\n"
"v_mfma_f32_16x16x32_bf16 v[240:243], v[192:195], v[144:147], 0\n"
"ds_read_u16_d16_hi v231, v231 offset:18432\n"
"v_cvt_pk_bf16_f32 v246, v248, v249\n"
"v_fma_f32 v254, v254, s63, s63\n"
"v_lshlrev_b32_sdwa v232, 1, v232 dst_sel:DWORD dst_unused:UNUSED_PAD src0_sel:DWORD src1_sel:WORD_1\n"
"v_mfma_f32_16x16x32_bf16 v[240:243], v[196:199], v[148:151], v[240:243]\n"
"ds_read_u16_d16 v232, v232 offset:18432\n"
"v_cvt_pk_bf16_f32 v247, v250, v251\n"
"v_fma_f32 v255, v255, s63, s63\n"
"v_mfma_f32_16x16x32_bf16 v[240:243], v[200:203], v[152:155], v[240:243]\n"
"v_rcp_f32_e32 v254, v254\n"
"v_lshlrev_b32_sdwa v233, 1, v233 dst_sel:DWORD dst_unused:UNUSED_PAD src0_sel:DWORD src1_sel:WORD_1\n"
"s_waitcnt lgkmcnt(11)\n"
"v_mfma_f32_16x16x32_bf16 v[0:3], v[204:207], v[244:247], v[0:3]\n"
"v_rcp_f32_e32 v255, v255\n"
"v_mfma_f32_16x16x32_bf16 v[4:7], v[208:211], v[244:247], v[4:7]\n"
"ds_read_u16_d16_hi v233, v233 offset:18432\n"
"v_mul_f32_e32 v234, v254, v234\n"
"v_mul_f32_e32 v235, v255, v235\n"
"v_mfma_f32_16x16x32_bf16 v[8:11], v[212:215], v[244:247], v[8:11]\n"
"v_lshlrev_b32_sdwa v236, 1, v236 dst_sel:DWORD dst_unused:UNUSED_PAD src0_sel:DWORD src1_sel:WORD_1\n"
"v_mul_f32_e32 v241, s64, v241\n"
"v_mul_f32_e32 v242, s64, v242\n"
"s_waitcnt lgkmcnt(5)\n"
"v_mfma_f32_16x16x32_bf16 v[12:15], v[216:219], v[244:247], v[12:15]\n"
"ds_read_u16_d16 v236, v236 offset:18432\n"
"v_mul_f32_e32 v243, s64, v243\n"
"v_fma_f32 v168, -v241, v241, s62\n"
"v_mfma_f32_16x16x32_bf16 v[16:19], v[220:223], v[244:247], v[16:19]\n"
"v_lshlrev_b32_sdwa v237, 1, v237 dst_sel:DWORD dst_unused:UNUSED_PAD src0_sel:DWORD src1_sel:WORD_1\n"
"v_fma_f32 v169, -v242, v242, s62\n"
"v_fma_f32 v170, -v243, v243, s62\n"
"v_mfma_f32_16x16x32_bf16 v[20:23], v[224:227], v[244:247], v[20:23]\n"
"ds_read_u16_d16_hi v237, v237 offset:18432\n"
"v_mul_f32_e32 v168, v168, v241\n"
"v_mul_f32_e32 v169, v169, v242\n"
"v_mfma_f32_16x16x32_bf16 v[244:247], v[180:183], v[156:159], 0\n"
"ds_read_b64_tr_b16 v[180:181], v252 offset:43008\n"
"ds_read_b64_tr_b16 v[182:183], v252 offset:46080\n"
"v_lshlrev_b32_sdwa v238, 1, v238 dst_sel:DWORD dst_unused:UNUSED_PAD src0_sel:DWORD src1_sel:WORD_1\n"
"v_mul_f32_e32 v170, v170, v243\n"
"v_exp_f32_e32 v168, v168\n"
"s_setprio 1\n"
"v_mfma_f32_16x16x32_bf16 v[244:247], v[184:187], v[160:163], v[244:247]\n"
"ds_read_b64_tr_b16 v[184:185], v252 offset:44032\n"
"ds_read_b64_tr_b16 v[186:187], v252 offset:47104\n"
"s_waitcnt lgkmcnt(6)\n"
"ds_read_u16_d16 v238, v238 offset:18432\n"
"v_or_b32 v228, v228, v229\n"
"v_exp_f32_e32 v169, v169\n"
"v_mfma_f32_16x16x32_bf16 v[244:247], v[188:191], v[164:167], v[244:247]\n"
"ds_read_b64_tr_b16 v[188:189], v252 offset:45056\n"
"ds_read_b64_tr_b16 v[190:191], v252 offset:48128\n"
"v_or_b32 v229, v230, v231\n"
"v_exp_f32_e32 v170, v170\n"
"v_lshlrev_b32_sdwa v239, 1, v239 dst_sel:DWORD dst_unused:UNUSED_PAD src0_sel:DWORD src1_sel:WORD_1\n"
"v_mfma_f32_16x16x32_bf16 v[248:251], v[192:195], v[156:159], 0\n"
"ds_read_b64_tr_b16 v[192:193], v252 offset:43520\n"
"ds_read_b64_tr_b16 v[194:195], v252 offset:46592\n"
"ds_read_u16_d16_hi v239, v239 offset:18432\n"
"v_or_b32 v230, v232, v233\n"
"v_fma_f32 v168, v168, s63, s63\n"
"v_fma_f32 v169, v169, s63, s63\n"
"v_mfma_f32_16x16x32_bf16 v[248:251], v[196:199], v[160:163], v[248:251]\n"
"ds_read_b64_tr_b16 v[196:197], v252 offset:44544\n"
"ds_read_b64_tr_b16 v[198:199], v252 offset:47616\n"
"v_cvt_pk_bf16_f32 v231, v234, v235\n"
"v_fma_f32 v170, v170, s63, s63\n"
"v_rcp_f32_e32 v168, v168\n"
"v_mfma_f32_16x16x32_bf16 v[248:251], v[200:203], v[164:167], v[248:251]\n"
"ds_read_b64_tr_b16 v[200:201], v252 offset:45568\n"
"ds_read_b64_tr_b16 v[202:203], v252 offset:48640\n"
"v_lshlrev_b32_sdwa v240, 1, v240 dst_sel:DWORD dst_unused:UNUSED_PAD src0_sel:DWORD src1_sel:WORD_1\n"
"v_rcp_f32_e32 v169, v169\n"
"v_mfma_f32_16x16x32_bf16 v[24:27], v[204:207], v[228:231], v[24:27]\n"
"ds_read_u16_d16_hi v240, v240 offset:18432\n"
"v_rcp_f32_e32 v170, v170\n"
"v_mfma_f32_16x16x32_bf16 v[28:31], v[208:211], v[228:231], v[28:31]\n"
"v_mul_f32_e32 v241, v168, v241\n"
"v_mul_f32_e32 v242, v169, v242\n"
"v_mul_f32_e32 v243, v170, v243\n"
"v_mfma_f32_16x16x32_bf16 v[32:35], v[212:215], v[228:231], v[32:35]\n"
"v_lshlrev_b32_sdwa v244, 1, v244 dst_sel:DWORD dst_unused:UNUSED_PAD src0_sel:DWORD src1_sel:WORD_1\n"
"v_mul_f32_e32 v250, s64, v250\n"
"v_mfma_f32_16x16x32_bf16 v[36:39], v[216:219], v[228:231], v[36:39]\n"
"ds_read_u16_d16 v244, v244 offset:18432\n"
"v_mul_f32_e32 v251, s64, v251\n"
"v_lshlrev_b32_sdwa v245, 1, v245 dst_sel:DWORD dst_unused:UNUSED_PAD src0_sel:DWORD src1_sel:WORD_1\n"
"v_mfma_f32_16x16x32_bf16 v[40:43], v[220:223], v[228:231], v[40:43]\n"
"v_fma_f32 v254, -v250, v250, s62\n"
"v_fma_f32 v255, -v251, v251, s62\n"
"v_mfma_f32_16x16x32_bf16 v[44:47], v[224:227], v[228:231], v[44:47]\n"
"ds_read_u16_d16_hi v245, v245 offset:18432\n"
"v_mul_f32_e32 v254, v254, v250\n"
"v_lshlrev_b32_sdwa v246, 1, v246 dst_sel:DWORD dst_unused:UNUSED_PAD src0_sel:DWORD src1_sel:WORD_1\n"
"s_setprio 0\n"
"s_waitcnt lgkmcnt(10)\n"
"v_mfma_f32_16x16x32_bf16 v[228:231], v[180:183], v[120:123], 0\n"
"ds_read_u16_d16 v246, v246 offset:18432\n"
"v_mul_f32_e32 v255, v255, v251\n"
"s_setprio 1\n"
"v_mfma_f32_16x16x32_bf16 v[228:231], v[184:187], v[124:127], v[228:231]\n"
"s_waitcnt lgkmcnt(3)\n"
"v_or_b32 v236, v236, v237\n"
"v_exp_f32_e32 v254, v254\n"
"v_lshlrev_b32_sdwa v247, 1, v247 dst_sel:DWORD dst_unused:UNUSED_PAD src0_sel:DWORD src1_sel:WORD_1\n"
"v_mfma_f32_16x16x32_bf16 v[228:231], v[188:191], v[128:131], v[228:231]\n"
"v_or_b32 v237, v238, v239\n"
"v_exp_f32_e32 v255, v255\n"
"v_mfma_f32_16x16x32_bf16 v[232:235], v[192:195], v[120:123], 0\n"
"ds_read_u16_d16_hi v247, v247 offset:18432\n"
"v_cvt_pk_bf16_f32 v238, v240, v241\n"
"v_fma_f32 v254, v254, s63, s63\n"
"v_lshlrev_b32_sdwa v248, 1, v248 dst_sel:DWORD dst_unused:UNUSED_PAD src0_sel:DWORD src1_sel:WORD_1\n"
"v_mfma_f32_16x16x32_bf16 v[232:235], v[196:199], v[124:127], v[232:235]\n"
"ds_read_u16_d16 v248, v248 offset:18432\n"
"v_cvt_pk_bf16_f32 v239, v242, v243\n"
"v_fma_f32 v255, v255, s63, s63\n"
"v_mfma_f32_16x16x32_bf16 v[232:235], v[200:203], v[128:131], v[232:235]\n"
"v_rcp_f32_e32 v254, v254\n"
"v_lshlrev_b32_sdwa v249, 1, v249 dst_sel:DWORD dst_unused:UNUSED_PAD src0_sel:DWORD src1_sel:WORD_1\n"
"v_mfma_f32_16x16x32_bf16 v[48:51], v[204:207], v[236:239], v[48:51]\n"
"v_rcp_f32_e32 v255, v255\n"
"v_mfma_f32_16x16x32_bf16 v[52:55], v[208:211], v[236:239], v[52:55]\n"
"ds_read_u16_d16_hi v249, v249 offset:18432\n"
"v_mul_f32_e32 v250, v254, v250\n"
"v_mul_f32_e32 v251, v255, v251\n"
"v_mfma_f32_16x16x32_bf16 v[56:59], v[212:215], v[236:239], v[56:59]\n"
"v_lshlrev_b32_sdwa v228, 1, v228 dst_sel:DWORD dst_unused:UNUSED_PAD src0_sel:DWORD src1_sel:WORD_1\n"
"v_mul_f32_e32 v233, s64, v233\n"
"v_mul_f32_e32 v234, s64, v234\n"
"v_mfma_f32_16x16x32_bf16 v[60:63], v[216:219], v[236:239], v[60:63]\n"
"ds_read_u16_d16 v228, v228 offset:18432\n"
"v_mul_f32_e32 v235, s64, v235\n"
"v_fma_f32 v168, -v233, v233, s62\n"
"v_mfma_f32_16x16x32_bf16 v[64:67], v[220:223], v[236:239], v[64:67]\n"
"v_lshlrev_b32_sdwa v229, 1, v229 dst_sel:DWORD dst_unused:UNUSED_PAD src0_sel:DWORD src1_sel:WORD_1\n"
"v_fma_f32 v169, -v234, v234, s62\n"
"v_fma_f32 v170, -v235, v235, s62\n"
"v_mfma_f32_16x16x32_bf16 v[68:71], v[224:227], v[236:239], v[68:71]\n"
"ds_read_u16_d16_hi v229, v229 offset:18432\n"
"v_mul_f32_e32 v168, v168, v233\n"
"v_mul_f32_e32 v169, v169, v234\n"
"v_mfma_f32_16x16x32_bf16 v[236:239], v[180:183], v[132:135], 0\n"
"v_lshlrev_b32_sdwa v230, 1, v230 dst_sel:DWORD dst_unused:UNUSED_PAD src0_sel:DWORD src1_sel:WORD_1\n"
"v_mul_f32_e32 v170, v170, v235\n"
"v_exp_f32_e32 v168, v168\n"
"s_setprio 1\n"
"v_mfma_f32_16x16x32_bf16 v[236:239], v[184:187], v[136:139], v[236:239]\n"
"s_waitcnt lgkmcnt(2)\n"
"ds_read_u16_d16 v230, v230 offset:18432\n"
"v_or_b32 v244, v244, v245\n"
"v_exp_f32_e32 v169, v169\n"
"v_mfma_f32_16x16x32_bf16 v[236:239], v[188:191], v[140:143], v[236:239]\n"
"v_or_b32 v245, v246, v247\n"
"v_exp_f32_e32 v170, v170\n"
"v_lshlrev_b32_sdwa v231, 1, v231 dst_sel:DWORD dst_unused:UNUSED_PAD src0_sel:DWORD src1_sel:WORD_1\n"
"v_mfma_f32_16x16x32_bf16 v[240:243], v[192:195], v[132:135], 0\n"
"ds_read_u16_d16_hi v231, v231 offset:18432\n"
"v_or_b32 v246, v248, v249\n"
"v_fma_f32 v168, v168, s63, s63\n"
"v_fma_f32 v169, v169, s63, s63\n"
"v_mfma_f32_16x16x32_bf16 v[240:243], v[196:199], v[136:139], v[240:243]\n"
"v_cvt_pk_bf16_f32 v247, v250, v251\n"
"v_fma_f32 v170, v170, s63, s63\n"
"v_rcp_f32_e32 v168, v168\n"
"v_mfma_f32_16x16x32_bf16 v[240:243], v[200:203], v[140:143], v[240:243]\n"
"v_lshlrev_b32_sdwa v232, 1, v232 dst_sel:DWORD dst_unused:UNUSED_PAD src0_sel:DWORD src1_sel:WORD_1\n"
"v_rcp_f32_e32 v169, v169\n"
"v_mfma_f32_16x16x32_bf16 v[72:75], v[204:207], v[244:247], v[72:75]\n"
"ds_read_b64_tr_b16 v[204:205], v253 offset:43120\n"
"ds_read_b64_tr_b16 v[206:207], v253 offset:44656\n"
"ds_read_u16_d16_hi v232, v232 offset:18432\n"
"v_rcp_f32_e32 v170, v170\n"
"v_mfma_f32_16x16x32_bf16 v[76:79], v[208:211], v[244:247], v[76:79]\n"
"ds_read_b64_tr_b16 v[208:209], v253 offset:43128\n"
"ds_read_b64_tr_b16 v[210:211], v253 offset:44664\n"
"v_mul_f32_e32 v233, v168, v233\n"
"v_mul_f32_e32 v234, v169, v234\n"
"v_mul_f32_e32 v235, v170, v235\n"
"v_mfma_f32_16x16x32_bf16 v[80:83], v[212:215], v[244:247], v[80:83]\n"
"ds_read_b64_tr_b16 v[212:213], v253 offset:43184\n"
"ds_read_b64_tr_b16 v[214:215], v253 offset:44720\n"
"v_lshlrev_b32_sdwa v236, 1, v236 dst_sel:DWORD dst_unused:UNUSED_PAD src0_sel:DWORD src1_sel:WORD_1\n"
"v_mul_f32_e32 v242, s64, v242\n"
"v_mfma_f32_16x16x32_bf16 v[84:87], v[216:219], v[244:247], v[84:87]\n"
"ds_read_b64_tr_b16 v[216:217], v253 offset:43192\n"
"ds_read_b64_tr_b16 v[218:219], v253 offset:44728\n"
"ds_read_u16_d16 v236, v236 offset:18432\n"
"v_mul_f32_e32 v243, s64, v243\n"
"v_lshlrev_b32_sdwa v237, 1, v237 dst_sel:DWORD dst_unused:UNUSED_PAD src0_sel:DWORD src1_sel:WORD_1\n"
"v_mfma_f32_16x16x32_bf16 v[88:91], v[220:223], v[244:247], v[88:91]\n"
"ds_read_b64_tr_b16 v[220:221], v253 offset:43248\n"
"ds_read_b64_tr_b16 v[222:223], v253 offset:44784\n"
"v_fma_f32 v254, -v242, v242, s62\n"
"v_fma_f32 v255, -v243, v243, s62\n"
"v_mfma_f32_16x16x32_bf16 v[92:95], v[224:227], v[244:247], v[92:95]\n"
"ds_read_b64_tr_b16 v[224:225], v253 offset:43256\n"
"ds_read_b64_tr_b16 v[226:227], v253 offset:44792\n"
"ds_read_u16_d16_hi v237, v237 offset:18432\n"
"v_mul_f32_e32 v254, v254, v242\n"
"v_lshlrev_b32_sdwa v238, 1, v238 dst_sel:DWORD dst_unused:UNUSED_PAD src0_sel:DWORD src1_sel:WORD_1\n"
"s_setprio 0\n"
"v_mfma_f32_16x16x32_bf16 v[244:247], v[180:183], v[144:147], 0\n"
"ds_read_u16_d16 v238, v238 offset:18432\n"
"v_mul_f32_e32 v255, v255, v243\n"
"s_setprio 1\n"
"v_mfma_f32_16x16x32_bf16 v[244:247], v[184:187], v[148:151], v[244:247]\n"
"s_waitcnt lgkmcnt(13)\n"
"v_or_b32 v228, v228, v229\n"
"v_exp_f32_e32 v254, v254\n"
"v_lshlrev_b32_sdwa v239, 1, v239 dst_sel:DWORD dst_unused:UNUSED_PAD src0_sel:DWORD src1_sel:WORD_1\n"
"v_mfma_f32_16x16x32_bf16 v[244:247], v[188:191], v[152:155], v[244:247]\n"
"v_or_b32 v229, v230, v231\n"
"v_exp_f32_e32 v255, v255\n"
"v_mfma_f32_16x16x32_bf16 v[248:251], v[192:195], v[144:147], 0\n"
"ds_read_u16_d16_hi v239, v239 offset:18432\n"
"v_cvt_pk_bf16_f32 v230, v232, v233\n"
"v_fma_f32 v254, v254, s63, s63\n"
"v_lshlrev_b32_sdwa v240, 1, v240 dst_sel:DWORD dst_unused:UNUSED_PAD src0_sel:DWORD src1_sel:WORD_1\n"
"v_mfma_f32_16x16x32_bf16 v[248:251], v[196:199], v[148:151], v[248:251]\n"
"ds_read_u16_d16 v240, v240 offset:18432\n"
"v_cvt_pk_bf16_f32 v231, v234, v235\n"
"v_fma_f32 v255, v255, s63, s63\n"
"v_mfma_f32_16x16x32_bf16 v[248:251], v[200:203], v[152:155], v[248:251]\n"
"v_rcp_f32_e32 v254, v254\n"
"v_lshlrev_b32_sdwa v241, 1, v241 dst_sel:DWORD dst_unused:UNUSED_PAD src0_sel:DWORD src1_sel:WORD_1\n"
"s_waitcnt lgkmcnt(11)\n"
"v_mfma_f32_16x16x32_bf16 v[0:3], v[204:207], v[228:231], v[0:3]\n"
"v_rcp_f32_e32 v255, v255\n"
"v_mfma_f32_16x16x32_bf16 v[4:7], v[208:211], v[228:231], v[4:7]\n"
"ds_read_u16_d16_hi v241, v241 offset:18432\n"
"v_mul_f32_e32 v242, v254, v242\n"
"v_mul_f32_e32 v243, v255, v243\n"
"v_mfma_f32_16x16x32_bf16 v[8:11], v[212:215], v[228:231], v[8:11]\n"
"v_lshlrev_b32_sdwa v244, 1, v244 dst_sel:DWORD dst_unused:UNUSED_PAD src0_sel:DWORD src1_sel:WORD_1\n"
"v_mul_f32_e32 v249, s64, v249\n"
"v_mul_f32_e32 v250, s64, v250\n"
"s_waitcnt lgkmcnt(5)\n"
"v_mfma_f32_16x16x32_bf16 v[12:15], v[216:219], v[228:231], v[12:15]\n"
"ds_read_u16_d16 v244, v244 offset:18432\n"
"v_mul_f32_e32 v251, s64, v251\n"
"v_fma_f32 v168, -v249, v249, s62\n"
"v_mfma_f32_16x16x32_bf16 v[16:19], v[220:223], v[228:231], v[16:19]\n"
"v_lshlrev_b32_sdwa v245, 1, v245 dst_sel:DWORD dst_unused:UNUSED_PAD src0_sel:DWORD src1_sel:WORD_1\n"
"v_fma_f32 v169, -v250, v250, s62\n"
"v_fma_f32 v170, -v251, v251, s62\n"
"v_mfma_f32_16x16x32_bf16 v[20:23], v[224:227], v[228:231], v[20:23]\n"
"ds_read_u16_d16_hi v245, v245 offset:18432\n"
"v_mul_f32_e32 v168, v168, v249\n"
"v_mul_f32_e32 v169, v169, v250\n"
"v_mfma_f32_16x16x32_bf16 v[228:231], v[180:183], v[156:159], 0\n"
"ds_read_b64_tr_b16 v[180:181], v252 offset:49152\n"
"ds_read_b64_tr_b16 v[182:183], v252 offset:52224\n"
"v_lshlrev_b32_sdwa v246, 1, v246 dst_sel:DWORD dst_unused:UNUSED_PAD src0_sel:DWORD src1_sel:WORD_1\n"
"v_mul_f32_e32 v170, v170, v251\n"
"v_exp_f32_e32 v168, v168\n"
"s_setprio 1\n"
"v_mfma_f32_16x16x32_bf16 v[228:231], v[184:187], v[160:163], v[228:231]\n"
"ds_read_b64_tr_b16 v[184:185], v252 offset:50176\n"
"ds_read_b64_tr_b16 v[186:187], v252 offset:53248\n"
"s_waitcnt lgkmcnt(6)\n"
"ds_read_u16_d16 v246, v246 offset:18432\n"
"v_or_b32 v236, v236, v237\n"
"v_exp_f32_e32 v169, v169\n"
"v_mfma_f32_16x16x32_bf16 v[228:231], v[188:191], v[164:167], v[228:231]\n"
"ds_read_b64_tr_b16 v[188:189], v252 offset:51200\n"
"ds_read_b64_tr_b16 v[190:191], v252 offset:54272\n"
"v_or_b32 v237, v238, v239\n"
"v_exp_f32_e32 v170, v170\n"
"v_lshlrev_b32_sdwa v247, 1, v247 dst_sel:DWORD dst_unused:UNUSED_PAD src0_sel:DWORD src1_sel:WORD_1\n"
"v_mfma_f32_16x16x32_bf16 v[232:235], v[192:195], v[156:159], 0\n"
"ds_read_b64_tr_b16 v[192:193], v252 offset:49664\n"
"ds_read_b64_tr_b16 v[194:195], v252 offset:52736\n"
"ds_read_u16_d16_hi v247, v247 offset:18432\n"
"v_or_b32 v238, v240, v241\n"
"v_fma_f32 v168, v168, s63, s63\n"
"v_fma_f32 v169, v169, s63, s63\n"
"v_mfma_f32_16x16x32_bf16 v[232:235], v[196:199], v[160:163], v[232:235]\n"
"ds_read_b64_tr_b16 v[196:197], v252 offset:50688\n"
"ds_read_b64_tr_b16 v[198:199], v252 offset:53760\n"
"v_cvt_pk_bf16_f32 v239, v242, v243\n"
"v_fma_f32 v170, v170, s63, s63\n"
"v_rcp_f32_e32 v168, v168\n"
"v_mfma_f32_16x16x32_bf16 v[232:235], v[200:203], v[164:167], v[232:235]\n"
"ds_read_b64_tr_b16 v[200:201], v252 offset:51712\n"
"ds_read_b64_tr_b16 v[202:203], v252 offset:54784\n"
"v_lshlrev_b32_sdwa v248, 1, v248 dst_sel:DWORD dst_unused:UNUSED_PAD src0_sel:DWORD src1_sel:WORD_1\n"
"v_rcp_f32_e32 v169, v169\n"
"v_mfma_f32_16x16x32_bf16 v[24:27], v[204:207], v[236:239], v[24:27]\n"
"ds_read_u16_d16_hi v248, v248 offset:18432\n"
"v_rcp_f32_e32 v170, v170\n"
"v_mfma_f32_16x16x32_bf16 v[28:31], v[208:211], v[236:239], v[28:31]\n"
"v_mul_f32_e32 v249, v168, v249\n"
"v_mul_f32_e32 v250, v169, v250\n"
"v_mul_f32_e32 v251, v170, v251\n"
"v_mfma_f32_16x16x32_bf16 v[32:35], v[212:215], v[236:239], v[32:35]\n"
"v_lshlrev_b32_sdwa v228, 1, v228 dst_sel:DWORD dst_unused:UNUSED_PAD src0_sel:DWORD src1_sel:WORD_1\n"
"v_mul_f32_e32 v234, s64, v234\n"
"v_mfma_f32_16x16x32_bf16 v[36:39], v[216:219], v[236:239], v[36:39]\n"
"ds_read_u16_d16 v228, v228 offset:18432\n"
"v_mul_f32_e32 v235, s64, v235\n"
"v_lshlrev_b32_sdwa v229, 1, v229 dst_sel:DWORD dst_unused:UNUSED_PAD src0_sel:DWORD src1_sel:WORD_1\n"
"v_mfma_f32_16x16x32_bf16 v[40:43], v[220:223], v[236:239], v[40:43]\n"
"v_fma_f32 v254, -v234, v234, s62\n"
"v_fma_f32 v255, -v235, v235, s62\n"
"v_mfma_f32_16x16x32_bf16 v[44:47], v[224:227], v[236:239], v[44:47]\n"
"ds_read_u16_d16_hi v229, v229 offset:18432\n"
"v_mul_f32_e32 v254, v254, v234\n"
"v_lshlrev_b32_sdwa v230, 1, v230 dst_sel:DWORD dst_unused:UNUSED_PAD src0_sel:DWORD src1_sel:WORD_1\n"
"s_waitcnt lgkmcnt(10)\n"
"v_mfma_f32_16x16x32_bf16 v[236:239], v[180:183], v[120:123], 0\n"
"ds_read_u16_d16 v230, v230 offset:18432\n"
"v_mul_f32_e32 v255, v255, v235\n"
"s_setprio 1\n"
"v_mfma_f32_16x16x32_bf16 v[236:239], v[184:187], v[124:127], v[236:239]\n"
"s_waitcnt lgkmcnt(3)\n"
"v_or_b32 v244, v244, v245\n"
"v_exp_f32_e32 v254, v254\n"
"v_lshlrev_b32_sdwa v231, 1, v231 dst_sel:DWORD dst_unused:UNUSED_PAD src0_sel:DWORD src1_sel:WORD_1\n"
"v_mfma_f32_16x16x32_bf16 v[236:239], v[188:191], v[128:131], v[236:239]\n"
"v_or_b32 v245, v246, v247\n"
"v_exp_f32_e32 v255, v255\n"
"v_mfma_f32_16x16x32_bf16 v[240:243], v[192:195], v[120:123], 0\n"
"ds_read_u16_d16_hi v231, v231 offset:18432\n"
"v_cvt_pk_bf16_f32 v246, v248, v249\n"
"v_fma_f32 v254, v254, s63, s63\n"
"v_lshlrev_b32_sdwa v232, 1, v232 dst_sel:DWORD dst_unused:UNUSED_PAD src0_sel:DWORD src1_sel:WORD_1\n"
"v_mfma_f32_16x16x32_bf16 v[240:243], v[196:199], v[124:127], v[240:243]\n"
"ds_read_u16_d16 v232, v232 offset:18432\n"
"v_cvt_pk_bf16_f32 v247, v250, v251\n"
"v_fma_f32 v255, v255, s63, s63\n"
"v_mfma_f32_16x16x32_bf16 v[240:243], v[200:203], v[128:131], v[240:243]\n"
"v_rcp_f32_e32 v254, v254\n"
"v_lshlrev_b32_sdwa v233, 1, v233 dst_sel:DWORD dst_unused:UNUSED_PAD src0_sel:DWORD src1_sel:WORD_1\n"
"v_mfma_f32_16x16x32_bf16 v[48:51], v[204:207], v[244:247], v[48:51]\n"
"v_rcp_f32_e32 v255, v255\n"
"v_mfma_f32_16x16x32_bf16 v[52:55], v[208:211], v[244:247], v[52:55]\n"
"ds_read_u16_d16_hi v233, v233 offset:18432\n"
"v_mul_f32_e32 v234, v254, v234\n"
"v_mul_f32_e32 v235, v255, v235\n"
"v_mfma_f32_16x16x32_bf16 v[56:59], v[212:215], v[244:247], v[56:59]\n"
"v_lshlrev_b32_sdwa v236, 1, v236 dst_sel:DWORD dst_unused:UNUSED_PAD src0_sel:DWORD src1_sel:WORD_1\n"
"v_mul_f32_e32 v241, s64, v241\n"
"v_mul_f32_e32 v242, s64, v242\n"
"v_mfma_f32_16x16x32_bf16 v[60:63], v[216:219], v[244:247], v[60:63]\n"
"ds_read_u16_d16 v236, v236 offset:18432\n"
"v_mul_f32_e32 v243, s64, v243\n"
"v_fma_f32 v168, -v241, v241, s62\n"
"v_mfma_f32_16x16x32_bf16 v[64:67], v[220:223], v[244:247], v[64:67]\n"
"v_lshlrev_b32_sdwa v237, 1, v237 dst_sel:DWORD dst_unused:UNUSED_PAD src0_sel:DWORD src1_sel:WORD_1\n"
"v_fma_f32 v169, -v242, v242, s62\n"
"v_fma_f32 v170, -v243, v243, s62\n"
"v_mfma_f32_16x16x32_bf16 v[68:71], v[224:227], v[244:247], v[68:71]\n"
"ds_read_u16_d16_hi v237, v237 offset:18432\n"
"v_mul_f32_e32 v168, v168, v241\n"
"v_mul_f32_e32 v169, v169, v242\n"
"s_setprio 0\n"
"v_mfma_f32_16x16x32_bf16 v[244:247], v[180:183], v[132:135], 0\n"
"v_lshlrev_b32_sdwa v238, 1, v238 dst_sel:DWORD dst_unused:UNUSED_PAD src0_sel:DWORD src1_sel:WORD_1\n"
"v_mul_f32_e32 v170, v170, v243\n"
"v_exp_f32_e32 v168, v168\n"
"s_setprio 1\n"
"v_mfma_f32_16x16x32_bf16 v[244:247], v[184:187], v[136:139], v[244:247]\n"
"s_waitcnt lgkmcnt(2)\n"
"ds_read_u16_d16 v238, v238 offset:18432\n"
"v_or_b32 v228, v228, v229\n"
"v_exp_f32_e32 v169, v169\n"
"v_mfma_f32_16x16x32_bf16 v[244:247], v[188:191], v[140:143], v[244:247]\n"
"v_or_b32 v229, v230, v231\n"
"v_exp_f32_e32 v170, v170\n"
"v_lshlrev_b32_sdwa v239, 1, v239 dst_sel:DWORD dst_unused:UNUSED_PAD src0_sel:DWORD src1_sel:WORD_1\n"
"v_mfma_f32_16x16x32_bf16 v[248:251], v[192:195], v[132:135], 0\n"
"ds_read_u16_d16_hi v239, v239 offset:18432\n"
"v_or_b32 v230, v232, v233\n"
"v_fma_f32 v168, v168, s63, s63\n"
"v_fma_f32 v169, v169, s63, s63\n"
"v_mfma_f32_16x16x32_bf16 v[248:251], v[196:199], v[136:139], v[248:251]\n"
"v_cvt_pk_bf16_f32 v231, v234, v235\n"
"v_fma_f32 v170, v170, s63, s63\n"
"v_rcp_f32_e32 v168, v168\n"
"v_mfma_f32_16x16x32_bf16 v[248:251], v[200:203], v[140:143], v[248:251]\n"
"v_lshlrev_b32_sdwa v240, 1, v240 dst_sel:DWORD dst_unused:UNUSED_PAD src0_sel:DWORD src1_sel:WORD_1\n"
"v_rcp_f32_e32 v169, v169\n"
"v_mfma_f32_16x16x32_bf16 v[72:75], v[204:207], v[228:231], v[72:75]\n"
"ds_read_b64_tr_b16 v[204:205], v253 offset:49280\n"
"ds_read_b64_tr_b16 v[206:207], v253 offset:50816\n"
"ds_read_u16_d16_hi v240, v240 offset:18432\n"
"v_rcp_f32_e32 v170, v170\n"
"v_mfma_f32_16x16x32_bf16 v[76:79], v[208:211], v[228:231], v[76:79]\n"
"ds_read_b64_tr_b16 v[208:209], v253 offset:49288\n"
"ds_read_b64_tr_b16 v[210:211], v253 offset:50824\n"
"v_mul_f32_e32 v241, v168, v241\n"
"v_mul_f32_e32 v242, v169, v242\n"
"v_mul_f32_e32 v243, v170, v243\n"
"v_mfma_f32_16x16x32_bf16 v[80:83], v[212:215], v[228:231], v[80:83]\n"
"ds_read_b64_tr_b16 v[212:213], v253 offset:49344\n"
"ds_read_b64_tr_b16 v[214:215], v253 offset:50880\n"
"v_lshlrev_b32_sdwa v244, 1, v244 dst_sel:DWORD dst_unused:UNUSED_PAD src0_sel:DWORD src1_sel:WORD_1\n"
"v_mul_f32_e32 v250, s64, v250\n"
"v_mfma_f32_16x16x32_bf16 v[84:87], v[216:219], v[228:231], v[84:87]\n"
"ds_read_b64_tr_b16 v[216:217], v253 offset:49352\n"
"ds_read_b64_tr_b16 v[218:219], v253 offset:50888\n"
"ds_read_u16_d16 v244, v244 offset:18432\n"
"v_mul_f32_e32 v251, s64, v251\n"
"v_lshlrev_b32_sdwa v245, 1, v245 dst_sel:DWORD dst_unused:UNUSED_PAD src0_sel:DWORD src1_sel:WORD_1\n"
"v_mfma_f32_16x16x32_bf16 v[88:91], v[220:223], v[228:231], v[88:91]\n"
"ds_read_b64_tr_b16 v[220:221], v253 offset:49408\n"
"ds_read_b64_tr_b16 v[222:223], v253 offset:50944\n"
"v_fma_f32 v254, -v250, v250, s62\n"
"v_fma_f32 v255, -v251, v251, s62\n"
"v_mfma_f32_16x16x32_bf16 v[92:95], v[224:227], v[228:231], v[92:95]\n"
"ds_read_b64_tr_b16 v[224:225], v253 offset:49416\n"
"ds_read_b64_tr_b16 v[226:227], v253 offset:50952\n"
"ds_read_u16_d16_hi v245, v245 offset:18432\n"
"v_mul_f32_e32 v254, v254, v250\n"
"v_lshlrev_b32_sdwa v246, 1, v246 dst_sel:DWORD dst_unused:UNUSED_PAD src0_sel:DWORD src1_sel:WORD_1\n"
"v_mfma_f32_16x16x32_bf16 v[228:231], v[180:183], v[144:147], 0\n"
"ds_read_u16_d16 v246, v246 offset:18432\n"
"v_mul_f32_e32 v255, v255, v251\n"
"s_setprio 1\n"
"v_mfma_f32_16x16x32_bf16 v[228:231], v[184:187], v[148:151], v[228:231]\n"
"s_waitcnt lgkmcnt(13)\n"
"v_or_b32 v236, v236, v237\n"
"v_exp_f32_e32 v254, v254\n"
"v_lshlrev_b32_sdwa v247, 1, v247 dst_sel:DWORD dst_unused:UNUSED_PAD src0_sel:DWORD src1_sel:WORD_1\n"
"v_mfma_f32_16x16x32_bf16 v[228:231], v[188:191], v[152:155], v[228:231]\n"
"v_or_b32 v237, v238, v239\n"
"v_exp_f32_e32 v255, v255\n"
"v_mfma_f32_16x16x32_bf16 v[232:235], v[192:195], v[144:147], 0\n"
"ds_read_u16_d16_hi v247, v247 offset:18432\n"
"v_cvt_pk_bf16_f32 v238, v240, v241\n"
"v_fma_f32 v254, v254, s63, s63\n"
"v_lshlrev_b32_sdwa v248, 1, v248 dst_sel:DWORD dst_unused:UNUSED_PAD src0_sel:DWORD src1_sel:WORD_1\n"
"v_mfma_f32_16x16x32_bf16 v[232:235], v[196:199], v[148:151], v[232:235]\n"
"ds_read_u16_d16 v248, v248 offset:18432\n"
"v_cvt_pk_bf16_f32 v239, v242, v243\n"
"v_fma_f32 v255, v255, s63, s63\n"
"v_mfma_f32_16x16x32_bf16 v[232:235], v[200:203], v[152:155], v[232:235]\n"
"v_rcp_f32_e32 v254, v254\n"
"v_lshlrev_b32_sdwa v249, 1, v249 dst_sel:DWORD dst_unused:UNUSED_PAD src0_sel:DWORD src1_sel:WORD_1\n"
"s_waitcnt lgkmcnt(11)\n"
"v_mfma_f32_16x16x32_bf16 v[0:3], v[204:207], v[236:239], v[0:3]\n"
"v_rcp_f32_e32 v255, v255\n"
"v_mfma_f32_16x16x32_bf16 v[4:7], v[208:211], v[236:239], v[4:7]\n"
"ds_read_u16_d16_hi v249, v249 offset:18432\n"
"v_mul_f32_e32 v250, v254, v250\n"
"v_mul_f32_e32 v251, v255, v251\n"
"v_mfma_f32_16x16x32_bf16 v[8:11], v[212:215], v[236:239], v[8:11]\n"
"v_lshlrev_b32_sdwa v228, 1, v228 dst_sel:DWORD dst_unused:UNUSED_PAD src0_sel:DWORD src1_sel:WORD_1\n"
"v_mul_f32_e32 v233, s64, v233\n"
"v_mul_f32_e32 v234, s64, v234\n"
"s_waitcnt lgkmcnt(5)\n"
"v_mfma_f32_16x16x32_bf16 v[12:15], v[216:219], v[236:239], v[12:15]\n"
"ds_read_u16_d16 v228, v228 offset:18432\n"
"v_mul_f32_e32 v235, s64, v235\n"
"v_fma_f32 v168, -v233, v233, s62\n"
"v_mfma_f32_16x16x32_bf16 v[16:19], v[220:223], v[236:239], v[16:19]\n"
"v_lshlrev_b32_sdwa v229, 1, v229 dst_sel:DWORD dst_unused:UNUSED_PAD src0_sel:DWORD src1_sel:WORD_1\n"
"v_fma_f32 v169, -v234, v234, s62\n"
"v_fma_f32 v170, -v235, v235, s62\n"
"v_mfma_f32_16x16x32_bf16 v[20:23], v[224:227], v[236:239], v[20:23]\n"
"ds_read_u16_d16_hi v229, v229 offset:18432\n"
"v_mul_f32_e32 v168, v168, v233\n"
"v_mul_f32_e32 v169, v169, v234\n"
"s_setprio 0\n"
"v_mfma_f32_16x16x32_bf16 v[236:239], v[180:183], v[156:159], 0\n"
"v_add_u32 v252, 0xffffa100, v252\n"
"ds_read_b64_tr_b16 v[180:181], v252 offset:0\n"
"ds_read_b64_tr_b16 v[182:183], v252 offset:3072\n"
"v_lshlrev_b32_sdwa v230, 1, v230 dst_sel:DWORD dst_unused:UNUSED_PAD src0_sel:DWORD src1_sel:WORD_1\n"
"v_mul_f32_e32 v170, v170, v235\n"
"v_exp_f32_e32 v168, v168\n"
"s_setprio 1\n"
"v_mfma_f32_16x16x32_bf16 v[236:239], v[184:187], v[160:163], v[236:239]\n"
"ds_read_b64_tr_b16 v[184:185], v252 offset:1024\n"
"ds_read_b64_tr_b16 v[186:187], v252 offset:4096\n"
"s_waitcnt lgkmcnt(6)\n"
"ds_read_u16_d16 v230, v230 offset:18432\n"
"v_or_b32 v244, v244, v245\n"
"v_exp_f32_e32 v169, v169\n"
"v_mfma_f32_16x16x32_bf16 v[236:239], v[188:191], v[164:167], v[236:239]\n"
"ds_read_b64_tr_b16 v[188:189], v252 offset:2048\n"
"ds_read_b64_tr_b16 v[190:191], v252 offset:5120\n"
"v_or_b32 v245, v246, v247\n"
"v_exp_f32_e32 v170, v170\n"
"v_lshlrev_b32_sdwa v231, 1, v231 dst_sel:DWORD dst_unused:UNUSED_PAD src0_sel:DWORD src1_sel:WORD_1\n"
"v_mfma_f32_16x16x32_bf16 v[240:243], v[192:195], v[156:159], 0\n"
"ds_read_b64_tr_b16 v[192:193], v252 offset:512\n"
"ds_read_b64_tr_b16 v[194:195], v252 offset:3584\n"
"ds_read_u16_d16_hi v231, v231 offset:18432\n"
"v_or_b32 v246, v248, v249\n"
"v_fma_f32 v168, v168, s63, s63\n"
"v_fma_f32 v169, v169, s63, s63\n"
"v_mfma_f32_16x16x32_bf16 v[240:243], v[196:199], v[160:163], v[240:243]\n"
"ds_read_b64_tr_b16 v[196:197], v252 offset:1536\n"
"ds_read_b64_tr_b16 v[198:199], v252 offset:4608\n"
"v_cvt_pk_bf16_f32 v247, v250, v251\n"
"v_fma_f32 v170, v170, s63, s63\n"
"v_rcp_f32_e32 v168, v168\n"
"v_mfma_f32_16x16x32_bf16 v[240:243], v[200:203], v[164:167], v[240:243]\n"
"ds_read_b64_tr_b16 v[200:201], v252 offset:2560\n"
"ds_read_b64_tr_b16 v[202:203], v252 offset:5632\n"
"v_lshlrev_b32_sdwa v232, 1, v232 dst_sel:DWORD dst_unused:UNUSED_PAD src0_sel:DWORD src1_sel:WORD_1\n"
"v_rcp_f32_e32 v169, v169\n"
"v_mfma_f32_16x16x32_bf16 v[24:27], v[204:207], v[244:247], v[24:27]\n"
"ds_read_u16_d16_hi v232, v232 offset:18432\n"
"v_rcp_f32_e32 v170, v170\n"
"v_mfma_f32_16x16x32_bf16 v[28:31], v[208:211], v[244:247], v[28:31]\n"
"v_mul_f32_e32 v233, v168, v233\n"
"v_mul_f32_e32 v234, v169, v234\n"
"v_mul_f32_e32 v235, v170, v235\n"
"v_mfma_f32_16x16x32_bf16 v[32:35], v[212:215], v[244:247], v[32:35]\n"
"v_lshlrev_b32_sdwa v236, 1, v236 dst_sel:DWORD dst_unused:UNUSED_PAD src0_sel:DWORD src1_sel:WORD_1\n"
"v_mul_f32_e32 v242, s64, v242\n"
"v_mfma_f32_16x16x32_bf16 v[36:39], v[216:219], v[244:247], v[36:39]\n"
"ds_read_u16_d16 v236, v236 offset:18432\n"
"v_mul_f32_e32 v243, s64, v243\n"
"v_lshlrev_b32_sdwa v237, 1, v237 dst_sel:DWORD dst_unused:UNUSED_PAD src0_sel:DWORD src1_sel:WORD_1\n"
"v_mfma_f32_16x16x32_bf16 v[40:43], v[220:223], v[244:247], v[40:43]\n"
"v_fma_f32 v254, -v242, v242, s62\n"
"v_fma_f32 v255, -v243, v243, s62\n"
"v_mfma_f32_16x16x32_bf16 v[44:47], v[224:227], v[244:247], v[44:47]\n"
"ds_read_u16_d16_hi v237, v237 offset:18432\n"
"v_mul_f32_e32 v254, v254, v242\n"
"v_lshlrev_b32_sdwa v238, 1, v238 dst_sel:DWORD dst_unused:UNUSED_PAD src0_sel:DWORD src1_sel:WORD_1\n"
"s_waitcnt lgkmcnt(10)\n"
"v_mfma_f32_16x16x32_bf16 v[244:247], v[180:183], v[120:123], 0\n"
"ds_read_u16_d16 v238, v238 offset:18432\n"
"v_mul_f32_e32 v255, v255, v243\n"
"s_setprio 1\n"
"v_mfma_f32_16x16x32_bf16 v[244:247], v[184:187], v[124:127], v[244:247]\n"
"s_waitcnt lgkmcnt(3)\n"
"v_or_b32 v228, v228, v229\n"
"v_exp_f32_e32 v254, v254\n"
"v_lshlrev_b32_sdwa v239, 1, v239 dst_sel:DWORD dst_unused:UNUSED_PAD src0_sel:DWORD src1_sel:WORD_1\n"
"v_mfma_f32_16x16x32_bf16 v[244:247], v[188:191], v[128:131], v[244:247]\n"
"v_or_b32 v229, v230, v231\n"
"v_exp_f32_e32 v255, v255\n"
"v_mfma_f32_16x16x32_bf16 v[248:251], v[192:195], v[120:123], 0\n"
"ds_read_u16_d16_hi v239, v239 offset:18432\n"
"v_cvt_pk_bf16_f32 v230, v232, v233\n"
"v_fma_f32 v254, v254, s63, s63\n"
"v_lshlrev_b32_sdwa v240, 1, v240 dst_sel:DWORD dst_unused:UNUSED_PAD src0_sel:DWORD src1_sel:WORD_1\n"
"v_mfma_f32_16x16x32_bf16 v[248:251], v[196:199], v[124:127], v[248:251]\n"
"ds_read_u16_d16 v240, v240 offset:18432\n"
"v_cvt_pk_bf16_f32 v231, v234, v235\n"
"v_fma_f32 v255, v255, s63, s63\n"
"v_mfma_f32_16x16x32_bf16 v[248:251], v[200:203], v[128:131], v[248:251]\n"
"v_rcp_f32_e32 v254, v254\n"
"v_lshlrev_b32_sdwa v241, 1, v241 dst_sel:DWORD dst_unused:UNUSED_PAD src0_sel:DWORD src1_sel:WORD_1\n"
"v_mfma_f32_16x16x32_bf16 v[48:51], v[204:207], v[228:231], v[48:51]\n"
"v_rcp_f32_e32 v255, v255\n"
"v_mfma_f32_16x16x32_bf16 v[52:55], v[208:211], v[228:231], v[52:55]\n"
"ds_read_u16_d16_hi v241, v241 offset:18432\n"
"v_mul_f32_e32 v242, v254, v242\n"
"v_mul_f32_e32 v243, v255, v243\n"
"v_mfma_f32_16x16x32_bf16 v[56:59], v[212:215], v[228:231], v[56:59]\n"
"v_lshlrev_b32_sdwa v244, 1, v244 dst_sel:DWORD dst_unused:UNUSED_PAD src0_sel:DWORD src1_sel:WORD_1\n"
"v_mul_f32_e32 v249, s64, v249\n"
"v_mul_f32_e32 v250, s64, v250\n"
"v_mfma_f32_16x16x32_bf16 v[60:63], v[216:219], v[228:231], v[60:63]\n"
"ds_read_u16_d16 v244, v244 offset:18432\n"
"v_mul_f32_e32 v251, s64, v251\n"
"v_fma_f32 v168, -v249, v249, s62\n"
"v_mfma_f32_16x16x32_bf16 v[64:67], v[220:223], v[228:231], v[64:67]\n"
"v_lshlrev_b32_sdwa v245, 1, v245 dst_sel:DWORD dst_unused:UNUSED_PAD src0_sel:DWORD src1_sel:WORD_1\n"
"v_fma_f32 v169, -v250, v250, s62\n"
"v_fma_f32 v170, -v251, v251, s62\n"
"v_mfma_f32_16x16x32_bf16 v[68:71], v[224:227], v[228:231], v[68:71]\n"
"ds_read_u16_d16_hi v245, v245 offset:18432\n"
"v_mul_f32_e32 v168, v168, v249\n"
"v_mul_f32_e32 v169, v169, v250\n"
"v_mfma_f32_16x16x32_bf16 v[228:231], v[180:183], v[132:135], 0\n"
"v_lshlrev_b32_sdwa v246, 1, v246 dst_sel:DWORD dst_unused:UNUSED_PAD src0_sel:DWORD src1_sel:WORD_1\n"
"v_mul_f32_e32 v170, v170, v251\n"
"v_exp_f32_e32 v168, v168\n"
"s_setprio 1\n"
"v_mfma_f32_16x16x32_bf16 v[228:231], v[184:187], v[136:139], v[228:231]\n"
"s_waitcnt lgkmcnt(2)\n"
"ds_read_u16_d16 v246, v246 offset:18432\n"
"v_or_b32 v236, v236, v237\n"
"v_exp_f32_e32 v169, v169\n"
"v_mfma_f32_16x16x32_bf16 v[228:231], v[188:191], v[140:143], v[228:231]\n"
"v_or_b32 v237, v238, v239\n"
"v_exp_f32_e32 v170, v170\n"
"v_lshlrev_b32_sdwa v247, 1, v247 dst_sel:DWORD dst_unused:UNUSED_PAD src0_sel:DWORD src1_sel:WORD_1\n"
"v_mfma_f32_16x16x32_bf16 v[232:235], v[192:195], v[132:135], 0\n"
"ds_read_u16_d16_hi v247, v247 offset:18432\n"
"v_or_b32 v238, v240, v241\n"
"v_fma_f32 v168, v168, s63, s63\n"
"v_fma_f32 v169, v169, s63, s63\n"
"v_mfma_f32_16x16x32_bf16 v[232:235], v[196:199], v[136:139], v[232:235]\n"
"v_cvt_pk_bf16_f32 v239, v242, v243\n"
"v_fma_f32 v170, v170, s63, s63\n"
"v_rcp_f32_e32 v168, v168\n"
"v_mfma_f32_16x16x32_bf16 v[232:235], v[200:203], v[140:143], v[232:235]\n"
"v_lshlrev_b32_sdwa v248, 1, v248 dst_sel:DWORD dst_unused:UNUSED_PAD src0_sel:DWORD src1_sel:WORD_1\n"
"v_rcp_f32_e32 v169, v169\n"
"v_mfma_f32_16x16x32_bf16 v[72:75], v[204:207], v[236:239], v[72:75]\n"
"v_add_u32 v253, 0xffffb7d0, v253\n"
"ds_read_b64_tr_b16 v[204:205], v253 offset:0\n"
"ds_read_b64_tr_b16 v[206:207], v253 offset:1536\n"
"ds_read_u16_d16_hi v248, v248 offset:18432\n"
"v_rcp_f32_e32 v170, v170\n"
"v_mfma_f32_16x16x32_bf16 v[76:79], v[208:211], v[236:239], v[76:79]\n"
"ds_read_b64_tr_b16 v[208:209], v253 offset:8\n"
"ds_read_b64_tr_b16 v[210:211], v253 offset:1544\n"
"v_mul_f32_e32 v249, v168, v249\n"
"v_mul_f32_e32 v250, v169, v250\n"
"v_mul_f32_e32 v251, v170, v251\n"
"v_mfma_f32_16x16x32_bf16 v[80:83], v[212:215], v[236:239], v[80:83]\n"
"ds_read_b64_tr_b16 v[212:213], v253 offset:64\n"
"ds_read_b64_tr_b16 v[214:215], v253 offset:1600\n"
"v_lshlrev_b32_sdwa v228, 1, v228 dst_sel:DWORD dst_unused:UNUSED_PAD src0_sel:DWORD src1_sel:WORD_1\n"
"v_mul_f32_e32 v234, s64, v234\n"
"v_mfma_f32_16x16x32_bf16 v[84:87], v[216:219], v[236:239], v[84:87]\n"
"ds_read_b64_tr_b16 v[216:217], v253 offset:72\n"
"ds_read_b64_tr_b16 v[218:219], v253 offset:1608\n"
"ds_read_u16_d16 v228, v228 offset:18432\n"
"v_mul_f32_e32 v235, s64, v235\n"
"v_lshlrev_b32_sdwa v229, 1, v229 dst_sel:DWORD dst_unused:UNUSED_PAD src0_sel:DWORD src1_sel:WORD_1\n"
"v_mfma_f32_16x16x32_bf16 v[88:91], v[220:223], v[236:239], v[88:91]\n"
"ds_read_b64_tr_b16 v[220:221], v253 offset:128\n"
"ds_read_b64_tr_b16 v[222:223], v253 offset:1664\n"
"v_fma_f32 v254, -v234, v234, s62\n"
"v_fma_f32 v255, -v235, v235, s62\n"
"v_mfma_f32_16x16x32_bf16 v[92:95], v[224:227], v[236:239], v[92:95]\n"
"ds_read_b64_tr_b16 v[224:225], v253 offset:136\n"
"ds_read_b64_tr_b16 v[226:227], v253 offset:1672\n"
"ds_read_u16_d16_hi v229, v229 offset:18432\n"
"v_mul_f32_e32 v254, v254, v234\n"
"v_lshlrev_b32_sdwa v230, 1, v230 dst_sel:DWORD dst_unused:UNUSED_PAD src0_sel:DWORD src1_sel:WORD_1\n"
"s_setprio 0\n"
"v_mfma_f32_16x16x32_bf16 v[236:239], v[180:183], v[144:147], 0\n"
"ds_read_u16_d16 v230, v230 offset:18432\n"
"v_mul_f32_e32 v255, v255, v235\n"
"s_setprio 1\n"
"v_mfma_f32_16x16x32_bf16 v[236:239], v[184:187], v[148:151], v[236:239]\n"
"s_waitcnt lgkmcnt(13)\n"
"v_or_b32 v244, v244, v245\n"
"v_exp_f32_e32 v254, v254\n"
"v_lshlrev_b32_sdwa v231, 1, v231 dst_sel:DWORD dst_unused:UNUSED_PAD src0_sel:DWORD src1_sel:WORD_1\n"
"v_mfma_f32_16x16x32_bf16 v[236:239], v[188:191], v[152:155], v[236:239]\n"
"v_or_b32 v245, v246, v247\n"
"v_exp_f32_e32 v255, v255\n"
"v_mfma_f32_16x16x32_bf16 v[240:243], v[192:195], v[144:147], 0\n"
"ds_read_u16_d16_hi v231, v231 offset:18432\n"
"v_cvt_pk_bf16_f32 v246, v248, v249\n"
"v_fma_f32 v254, v254, s63, s63\n"
"v_lshlrev_b32_sdwa v232, 1, v232 dst_sel:DWORD dst_unused:UNUSED_PAD src0_sel:DWORD src1_sel:WORD_1\n"
"v_mfma_f32_16x16x32_bf16 v[240:243], v[196:199], v[148:151], v[240:243]\n"
"ds_read_u16_d16 v232, v232 offset:18432\n"
"v_cvt_pk_bf16_f32 v247, v250, v251\n"
"v_fma_f32 v255, v255, s63, s63\n"
"v_mfma_f32_16x16x32_bf16 v[240:243], v[200:203], v[152:155], v[240:243]\n"
"v_rcp_f32_e32 v254, v254\n"
"v_lshlrev_b32_sdwa v233, 1, v233 dst_sel:DWORD dst_unused:UNUSED_PAD src0_sel:DWORD src1_sel:WORD_1\n"
"s_waitcnt lgkmcnt(11)\n"
"v_mfma_f32_16x16x32_bf16 v[0:3], v[204:207], v[244:247], v[0:3]\n"
"v_rcp_f32_e32 v255, v255\n"
"v_mfma_f32_16x16x32_bf16 v[4:7], v[208:211], v[244:247], v[4:7]\n"
"ds_read_u16_d16_hi v233, v233 offset:18432\n"
"v_mul_f32_e32 v234, v254, v234\n"
"v_mul_f32_e32 v235, v255, v235\n"
"v_mfma_f32_16x16x32_bf16 v[8:11], v[212:215], v[244:247], v[8:11]\n"
"v_lshlrev_b32_sdwa v236, 1, v236 dst_sel:DWORD dst_unused:UNUSED_PAD src0_sel:DWORD src1_sel:WORD_1\n"
"v_mul_f32_e32 v241, s64, v241\n"
"v_mul_f32_e32 v242, s64, v242\n"
"s_waitcnt lgkmcnt(5)\n"
"v_mfma_f32_16x16x32_bf16 v[12:15], v[216:219], v[244:247], v[12:15]\n"
"ds_read_u16_d16 v236, v236 offset:18432\n"
"v_mul_f32_e32 v243, s64, v243\n"
"v_fma_f32 v168, -v241, v241, s62\n"
"v_mfma_f32_16x16x32_bf16 v[16:19], v[220:223], v[244:247], v[16:19]\n"
"v_lshlrev_b32_sdwa v237, 1, v237 dst_sel:DWORD dst_unused:UNUSED_PAD src0_sel:DWORD src1_sel:WORD_1\n"
"v_fma_f32 v169, -v242, v242, s62\n"
"v_fma_f32 v170, -v243, v243, s62\n"
"v_mfma_f32_16x16x32_bf16 v[20:23], v[224:227], v[244:247], v[20:23]\n"
"ds_read_u16_d16_hi v237, v237 offset:18432\n"
"v_mul_f32_e32 v168, v168, v241\n"
"v_mul_f32_e32 v169, v169, v242\n"
"v_mfma_f32_16x16x32_bf16 v[244:247], v[180:183], v[156:159], 0\n"
"ds_read_b64_tr_b16 v[180:181], v252 offset:6144\n"
"ds_read_b64_tr_b16 v[182:183], v252 offset:9216\n"
"v_lshlrev_b32_sdwa v238, 1, v238 dst_sel:DWORD dst_unused:UNUSED_PAD src0_sel:DWORD src1_sel:WORD_1\n"
"v_mul_f32_e32 v170, v170, v243\n"
"v_exp_f32_e32 v168, v168\n"
"s_setprio 1\n"
"v_mfma_f32_16x16x32_bf16 v[244:247], v[184:187], v[160:163], v[244:247]\n"
"ds_read_b64_tr_b16 v[184:185], v252 offset:7168\n"
"ds_read_b64_tr_b16 v[186:187], v252 offset:10240\n"
"s_waitcnt lgkmcnt(6)\n"
"ds_read_u16_d16 v238, v238 offset:18432\n"
"v_or_b32 v228, v228, v229\n"
"v_exp_f32_e32 v169, v169\n"
"v_mfma_f32_16x16x32_bf16 v[244:247], v[188:191], v[164:167], v[244:247]\n"
"ds_read_b64_tr_b16 v[188:189], v252 offset:8192\n"
"ds_read_b64_tr_b16 v[190:191], v252 offset:11264\n"
"v_or_b32 v229, v230, v231\n"
"v_exp_f32_e32 v170, v170\n"
"v_lshlrev_b32_sdwa v239, 1, v239 dst_sel:DWORD dst_unused:UNUSED_PAD src0_sel:DWORD src1_sel:WORD_1\n"
"v_mfma_f32_16x16x32_bf16 v[248:251], v[192:195], v[156:159], 0\n"
"ds_read_b64_tr_b16 v[192:193], v252 offset:6656\n"
"ds_read_b64_tr_b16 v[194:195], v252 offset:9728\n"
"ds_read_u16_d16_hi v239, v239 offset:18432\n"
"v_or_b32 v230, v232, v233\n"
"v_fma_f32 v168, v168, s63, s63\n"
"v_fma_f32 v169, v169, s63, s63\n"
"v_mfma_f32_16x16x32_bf16 v[248:251], v[196:199], v[160:163], v[248:251]\n"
"ds_read_b64_tr_b16 v[196:197], v252 offset:7680\n"
"ds_read_b64_tr_b16 v[198:199], v252 offset:10752\n"
"v_cvt_pk_bf16_f32 v231, v234, v235\n"
"v_fma_f32 v170, v170, s63, s63\n"
"v_rcp_f32_e32 v168, v168\n"
"v_mfma_f32_16x16x32_bf16 v[248:251], v[200:203], v[164:167], v[248:251]\n"
"ds_read_b64_tr_b16 v[200:201], v252 offset:8704\n"
"ds_read_b64_tr_b16 v[202:203], v252 offset:11776\n"
"v_lshlrev_b32_sdwa v240, 1, v240 dst_sel:DWORD dst_unused:UNUSED_PAD src0_sel:DWORD src1_sel:WORD_1\n"
"v_rcp_f32_e32 v169, v169\n"
"v_mfma_f32_16x16x32_bf16 v[24:27], v[204:207], v[228:231], v[24:27]\n"
"ds_read_u16_d16_hi v240, v240 offset:18432\n"
"v_rcp_f32_e32 v170, v170\n"
"v_mfma_f32_16x16x32_bf16 v[28:31], v[208:211], v[228:231], v[28:31]\n"
"v_mul_f32_e32 v241, v168, v241\n"
"v_mul_f32_e32 v242, v169, v242\n"
"v_mul_f32_e32 v243, v170, v243\n"
"v_mfma_f32_16x16x32_bf16 v[32:35], v[212:215], v[228:231], v[32:35]\n"
"v_lshlrev_b32_sdwa v244, 1, v244 dst_sel:DWORD dst_unused:UNUSED_PAD src0_sel:DWORD src1_sel:WORD_1\n"
"v_mul_f32_e32 v250, s64, v250\n"
"v_mfma_f32_16x16x32_bf16 v[36:39], v[216:219], v[228:231], v[36:39]\n"
"ds_read_u16_d16 v244, v244 offset:18432\n"
"v_mul_f32_e32 v251, s64, v251\n"
"v_lshlrev_b32_sdwa v245, 1, v245 dst_sel:DWORD dst_unused:UNUSED_PAD src0_sel:DWORD src1_sel:WORD_1\n"
"v_mfma_f32_16x16x32_bf16 v[40:43], v[220:223], v[228:231], v[40:43]\n"
"v_fma_f32 v254, -v250, v250, s62\n"
"v_fma_f32 v255, -v251, v251, s62\n"
"v_mfma_f32_16x16x32_bf16 v[44:47], v[224:227], v[228:231], v[44:47]\n"
"ds_read_u16_d16_hi v245, v245 offset:18432\n"
"v_mul_f32_e32 v254, v254, v250\n"
"v_lshlrev_b32_sdwa v246, 1, v246 dst_sel:DWORD dst_unused:UNUSED_PAD src0_sel:DWORD src1_sel:WORD_1\n"
"s_setprio 0\n"
"s_waitcnt lgkmcnt(10)\n"
"v_mfma_f32_16x16x32_bf16 v[228:231], v[180:183], v[120:123], 0\n"
"ds_read_u16_d16 v246, v246 offset:18432\n"
"v_mul_f32_e32 v255, v255, v251\n"
"s_setprio 1\n"
"v_mfma_f32_16x16x32_bf16 v[228:231], v[184:187], v[124:127], v[228:231]\n"
"s_waitcnt lgkmcnt(3)\n"
"v_or_b32 v236, v236, v237\n"
"v_exp_f32_e32 v254, v254\n"
"v_lshlrev_b32_sdwa v247, 1, v247 dst_sel:DWORD dst_unused:UNUSED_PAD src0_sel:DWORD src1_sel:WORD_1\n"
"v_mfma_f32_16x16x32_bf16 v[228:231], v[188:191], v[128:131], v[228:231]\n"
"v_or_b32 v237, v238, v239\n"
"v_exp_f32_e32 v255, v255\n"
"v_mfma_f32_16x16x32_bf16 v[232:235], v[192:195], v[120:123], 0\n"
"ds_read_u16_d16_hi v247, v247 offset:18432\n"
"v_cvt_pk_bf16_f32 v238, v240, v241\n"
"v_fma_f32 v254, v254, s63, s63\n"
"v_lshlrev_b32_sdwa v248, 1, v248 dst_sel:DWORD dst_unused:UNUSED_PAD src0_sel:DWORD src1_sel:WORD_1\n"
"v_mfma_f32_16x16x32_bf16 v[232:235], v[196:199], v[124:127], v[232:235]\n"
"ds_read_u16_d16 v248, v248 offset:18432\n"
"v_cvt_pk_bf16_f32 v239, v242, v243\n"
"v_fma_f32 v255, v255, s63, s63\n"
"v_mfma_f32_16x16x32_bf16 v[232:235], v[200:203], v[128:131], v[232:235]\n"
"v_rcp_f32_e32 v254, v254\n"
"v_lshlrev_b32_sdwa v249, 1, v249 dst_sel:DWORD dst_unused:UNUSED_PAD src0_sel:DWORD src1_sel:WORD_1\n"
"v_mfma_f32_16x16x32_bf16 v[48:51], v[204:207], v[236:239], v[48:51]\n"
"v_rcp_f32_e32 v255, v255\n"
"v_mfma_f32_16x16x32_bf16 v[52:55], v[208:211], v[236:239], v[52:55]\n"
"ds_read_u16_d16_hi v249, v249 offset:18432\n"
"v_mul_f32_e32 v250, v254, v250\n"
"v_mul_f32_e32 v251, v255, v251\n"
"v_mfma_f32_16x16x32_bf16 v[56:59], v[212:215], v[236:239], v[56:59]\n"
"v_lshlrev_b32_sdwa v228, 1, v228 dst_sel:DWORD dst_unused:UNUSED_PAD src0_sel:DWORD src1_sel:WORD_1\n"
"v_mul_f32_e32 v233, s64, v233\n"
"v_mul_f32_e32 v234, s64, v234\n"
"v_mfma_f32_16x16x32_bf16 v[60:63], v[216:219], v[236:239], v[60:63]\n"
"ds_read_u16_d16 v228, v228 offset:18432\n"
"v_mul_f32_e32 v235, s64, v235\n"
"v_fma_f32 v168, -v233, v233, s62\n"
"v_mfma_f32_16x16x32_bf16 v[64:67], v[220:223], v[236:239], v[64:67]\n"
"v_lshlrev_b32_sdwa v229, 1, v229 dst_sel:DWORD dst_unused:UNUSED_PAD src0_sel:DWORD src1_sel:WORD_1\n"
"v_fma_f32 v169, -v234, v234, s62\n"
"v_fma_f32 v170, -v235, v235, s62\n"
"v_mfma_f32_16x16x32_bf16 v[68:71], v[224:227], v[236:239], v[68:71]\n"
"ds_read_u16_d16_hi v229, v229 offset:18432\n"
"v_mul_f32_e32 v168, v168, v233\n"
"v_mul_f32_e32 v169, v169, v234\n"
"v_mfma_f32_16x16x32_bf16 v[236:239], v[180:183], v[132:135], 0\n"
"v_lshlrev_b32_sdwa v230, 1, v230 dst_sel:DWORD dst_unused:UNUSED_PAD src0_sel:DWORD src1_sel:WORD_1\n"
"v_mul_f32_e32 v170, v170, v235\n"
"v_exp_f32_e32 v168, v168\n"
"s_setprio 1\n"
"v_mfma_f32_16x16x32_bf16 v[236:239], v[184:187], v[136:139], v[236:239]\n"
"s_waitcnt lgkmcnt(2)\n"
"ds_read_u16_d16 v230, v230 offset:18432\n"
"v_or_b32 v244, v244, v245\n"
"v_exp_f32_e32 v169, v169\n"
"v_mfma_f32_16x16x32_bf16 v[236:239], v[188:191], v[140:143], v[236:239]\n"
"v_or_b32 v245, v246, v247\n"
"v_exp_f32_e32 v170, v170\n"
"v_lshlrev_b32_sdwa v231, 1, v231 dst_sel:DWORD dst_unused:UNUSED_PAD src0_sel:DWORD src1_sel:WORD_1\n"
"v_mfma_f32_16x16x32_bf16 v[240:243], v[192:195], v[132:135], 0\n"
"ds_read_u16_d16_hi v231, v231 offset:18432\n"
"v_or_b32 v246, v248, v249\n"
"v_fma_f32 v168, v168, s63, s63\n"
"v_fma_f32 v169, v169, s63, s63\n"
"v_mfma_f32_16x16x32_bf16 v[240:243], v[196:199], v[136:139], v[240:243]\n"
"v_cvt_pk_bf16_f32 v247, v250, v251\n"
"v_fma_f32 v170, v170, s63, s63\n"
"v_rcp_f32_e32 v168, v168\n"
"v_mfma_f32_16x16x32_bf16 v[240:243], v[200:203], v[140:143], v[240:243]\n"
"v_lshlrev_b32_sdwa v232, 1, v232 dst_sel:DWORD dst_unused:UNUSED_PAD src0_sel:DWORD src1_sel:WORD_1\n"
"v_rcp_f32_e32 v169, v169\n"
"v_mfma_f32_16x16x32_bf16 v[72:75], v[204:207], v[244:247], v[72:75]\n"
"ds_read_b64_tr_b16 v[204:205], v253 offset:6160\n"
"ds_read_b64_tr_b16 v[206:207], v253 offset:7696\n"
"ds_read_u16_d16_hi v232, v232 offset:18432\n"
"v_rcp_f32_e32 v170, v170\n"
"v_mfma_f32_16x16x32_bf16 v[76:79], v[208:211], v[244:247], v[76:79]\n"
"ds_read_b64_tr_b16 v[208:209], v253 offset:6168\n"
"ds_read_b64_tr_b16 v[210:211], v253 offset:7704\n"
"v_mul_f32_e32 v233, v168, v233\n"
"v_mul_f32_e32 v234, v169, v234\n"
"v_mul_f32_e32 v235, v170, v235\n"
"v_mfma_f32_16x16x32_bf16 v[80:83], v[212:215], v[244:247], v[80:83]\n"
"ds_read_b64_tr_b16 v[212:213], v253 offset:6224\n"
"ds_read_b64_tr_b16 v[214:215], v253 offset:7760\n"
"v_lshlrev_b32_sdwa v236, 1, v236 dst_sel:DWORD dst_unused:UNUSED_PAD src0_sel:DWORD src1_sel:WORD_1\n"
"v_mul_f32_e32 v242, s64, v242\n"
"v_mfma_f32_16x16x32_bf16 v[84:87], v[216:219], v[244:247], v[84:87]\n"
"ds_read_b64_tr_b16 v[216:217], v253 offset:6232\n"
"ds_read_b64_tr_b16 v[218:219], v253 offset:7768\n"
"ds_read_u16_d16 v236, v236 offset:18432\n"
"v_mul_f32_e32 v243, s64, v243\n"
"v_lshlrev_b32_sdwa v237, 1, v237 dst_sel:DWORD dst_unused:UNUSED_PAD src0_sel:DWORD src1_sel:WORD_1\n"
"v_mfma_f32_16x16x32_bf16 v[88:91], v[220:223], v[244:247], v[88:91]\n"
"ds_read_b64_tr_b16 v[220:221], v253 offset:6288\n"
"ds_read_b64_tr_b16 v[222:223], v253 offset:7824\n"
"v_fma_f32 v254, -v242, v242, s62\n"
"v_fma_f32 v255, -v243, v243, s62\n"
"v_mfma_f32_16x16x32_bf16 v[92:95], v[224:227], v[244:247], v[92:95]\n"
"ds_read_b64_tr_b16 v[224:225], v253 offset:6296\n"
"ds_read_b64_tr_b16 v[226:227], v253 offset:7832\n"
"ds_read_u16_d16_hi v237, v237 offset:18432\n"
"v_mul_f32_e32 v254, v254, v242\n"
"v_lshlrev_b32_sdwa v238, 1, v238 dst_sel:DWORD dst_unused:UNUSED_PAD src0_sel:DWORD src1_sel:WORD_1\n"
"v_mfma_f32_16x16x32_bf16 v[244:247], v[180:183], v[144:147], 0\n"
"ds_read_u16_d16 v238, v238 offset:18432\n"
"v_mul_f32_e32 v255, v255, v243\n"
"s_setprio 1\n"
"v_mfma_f32_16x16x32_bf16 v[244:247], v[184:187], v[148:151], v[244:247]\n"
"s_waitcnt lgkmcnt(13)\n"
"v_or_b32 v228, v228, v229\n"
"v_exp_f32_e32 v254, v254\n"
"v_lshlrev_b32_sdwa v239, 1, v239 dst_sel:DWORD dst_unused:UNUSED_PAD src0_sel:DWORD src1_sel:WORD_1\n"
"v_mfma_f32_16x16x32_bf16 v[244:247], v[188:191], v[152:155], v[244:247]\n"
"v_or_b32 v229, v230, v231\n"
"v_exp_f32_e32 v255, v255\n"
"v_mfma_f32_16x16x32_bf16 v[248:251], v[192:195], v[144:147], 0\n"
"ds_read_u16_d16_hi v239, v239 offset:18432\n"
"v_cvt_pk_bf16_f32 v230, v232, v233\n"
"v_fma_f32 v254, v254, s63, s63\n"
"v_lshlrev_b32_sdwa v240, 1, v240 dst_sel:DWORD dst_unused:UNUSED_PAD src0_sel:DWORD src1_sel:WORD_1\n"
"v_mfma_f32_16x16x32_bf16 v[248:251], v[196:199], v[148:151], v[248:251]\n"
"ds_read_u16_d16 v240, v240 offset:18432\n"
"v_cvt_pk_bf16_f32 v231, v234, v235\n"
"v_fma_f32 v255, v255, s63, s63\n"
"v_mfma_f32_16x16x32_bf16 v[248:251], v[200:203], v[152:155], v[248:251]\n"
"v_rcp_f32_e32 v254, v254\n"
"v_lshlrev_b32_sdwa v241, 1, v241 dst_sel:DWORD dst_unused:UNUSED_PAD src0_sel:DWORD src1_sel:WORD_1\n"
"s_waitcnt lgkmcnt(11)\n"
"v_mfma_f32_16x16x32_bf16 v[0:3], v[204:207], v[228:231], v[0:3]\n"
"v_rcp_f32_e32 v255, v255\n"
"v_mfma_f32_16x16x32_bf16 v[4:7], v[208:211], v[228:231], v[4:7]\n"
"ds_read_u16_d16_hi v241, v241 offset:18432\n"
"v_mul_f32_e32 v242, v254, v242\n"
"v_mul_f32_e32 v243, v255, v243\n"
"v_mfma_f32_16x16x32_bf16 v[8:11], v[212:215], v[228:231], v[8:11]\n"
"v_lshlrev_b32_sdwa v244, 1, v244 dst_sel:DWORD dst_unused:UNUSED_PAD src0_sel:DWORD src1_sel:WORD_1\n"
"v_mul_f32_e32 v249, s64, v249\n"
"v_mul_f32_e32 v250, s64, v250\n"
"s_waitcnt lgkmcnt(5)\n"
"v_mfma_f32_16x16x32_bf16 v[12:15], v[216:219], v[228:231], v[12:15]\n"
"ds_read_u16_d16 v244, v244 offset:18432\n"
"v_mul_f32_e32 v251, s64, v251\n"
"v_fma_f32 v168, -v249, v249, s62\n"
"v_mfma_f32_16x16x32_bf16 v[16:19], v[220:223], v[228:231], v[16:19]\n"
"v_lshlrev_b32_sdwa v245, 1, v245 dst_sel:DWORD dst_unused:UNUSED_PAD src0_sel:DWORD src1_sel:WORD_1\n"
"v_fma_f32 v169, -v250, v250, s62\n"
"v_fma_f32 v170, -v251, v251, s62\n"
"v_mfma_f32_16x16x32_bf16 v[20:23], v[224:227], v[228:231], v[20:23]\n"
"ds_read_u16_d16_hi v245, v245 offset:18432\n"
"v_mul_f32_e32 v168, v168, v249\n"
"v_mul_f32_e32 v169, v169, v250\n"
"s_setprio 0\n"
"v_mfma_f32_16x16x32_bf16 v[228:231], v[180:183], v[156:159], 0\n"
"ds_read_b64_tr_b16 v[180:181], v252 offset:12288\n"
"ds_read_b64_tr_b16 v[182:183], v252 offset:15360\n"
"v_lshlrev_b32_sdwa v246, 1, v246 dst_sel:DWORD dst_unused:UNUSED_PAD src0_sel:DWORD src1_sel:WORD_1\n"
"v_mul_f32_e32 v170, v170, v251\n"
"v_exp_f32_e32 v168, v168\n"
"s_setprio 1\n"
"v_mfma_f32_16x16x32_bf16 v[228:231], v[184:187], v[160:163], v[228:231]\n"
"ds_read_b64_tr_b16 v[184:185], v252 offset:13312\n"
"ds_read_b64_tr_b16 v[186:187], v252 offset:16384\n"
"s_waitcnt lgkmcnt(6)\n"
"ds_read_u16_d16 v246, v246 offset:18432\n"
"v_or_b32 v236, v236, v237\n"
"v_exp_f32_e32 v169, v169\n"
"v_mfma_f32_16x16x32_bf16 v[228:231], v[188:191], v[164:167], v[228:231]\n"
"ds_read_b64_tr_b16 v[188:189], v252 offset:14336\n"
"ds_read_b64_tr_b16 v[190:191], v252 offset:17408\n"
"v_or_b32 v237, v238, v239\n"
"v_exp_f32_e32 v170, v170\n"
"v_lshlrev_b32_sdwa v247, 1, v247 dst_sel:DWORD dst_unused:UNUSED_PAD src0_sel:DWORD src1_sel:WORD_1\n"
"v_mfma_f32_16x16x32_bf16 v[232:235], v[192:195], v[156:159], 0\n"
"ds_read_b64_tr_b16 v[192:193], v252 offset:12800\n"
"ds_read_b64_tr_b16 v[194:195], v252 offset:15872\n"
"ds_read_u16_d16_hi v247, v247 offset:18432\n"
"v_or_b32 v238, v240, v241\n"
"v_fma_f32 v168, v168, s63, s63\n"
"v_fma_f32 v169, v169, s63, s63\n"
"v_mfma_f32_16x16x32_bf16 v[232:235], v[196:199], v[160:163], v[232:235]\n"
"ds_read_b64_tr_b16 v[196:197], v252 offset:13824\n"
"ds_read_b64_tr_b16 v[198:199], v252 offset:16896\n"
"v_cvt_pk_bf16_f32 v239, v242, v243\n"
"v_fma_f32 v170, v170, s63, s63\n"
"v_rcp_f32_e32 v168, v168\n"
"v_mfma_f32_16x16x32_bf16 v[232:235], v[200:203], v[164:167], v[232:235]\n"
"ds_read_b64_tr_b16 v[200:201], v252 offset:14848\n"
"ds_read_b64_tr_b16 v[202:203], v252 offset:17920\n"
"v_lshlrev_b32_sdwa v248, 1, v248 dst_sel:DWORD dst_unused:UNUSED_PAD src0_sel:DWORD src1_sel:WORD_1\n"
"v_rcp_f32_e32 v169, v169\n"
"v_mfma_f32_16x16x32_bf16 v[24:27], v[204:207], v[236:239], v[24:27]\n"
"ds_read_u16_d16_hi v248, v248 offset:18432\n"
"v_rcp_f32_e32 v170, v170\n"
"v_mfma_f32_16x16x32_bf16 v[28:31], v[208:211], v[236:239], v[28:31]\n"
"v_mul_f32_e32 v249, v168, v249\n"
"v_mul_f32_e32 v250, v169, v250\n"
"v_mul_f32_e32 v251, v170, v251\n"
"v_mfma_f32_16x16x32_bf16 v[32:35], v[212:215], v[236:239], v[32:35]\n"
"v_lshlrev_b32_sdwa v228, 1, v228 dst_sel:DWORD dst_unused:UNUSED_PAD src0_sel:DWORD src1_sel:WORD_1\n"
"v_mul_f32_e32 v234, s64, v234\n"
"v_mfma_f32_16x16x32_bf16 v[36:39], v[216:219], v[236:239], v[36:39]\n"
"ds_read_u16_d16 v228, v228 offset:18432\n"
"v_mul_f32_e32 v235, s64, v235\n"
"v_lshlrev_b32_sdwa v229, 1, v229 dst_sel:DWORD dst_unused:UNUSED_PAD src0_sel:DWORD src1_sel:WORD_1\n"
"v_mfma_f32_16x16x32_bf16 v[40:43], v[220:223], v[236:239], v[40:43]\n"
"v_fma_f32 v254, -v234, v234, s62\n"
"v_fma_f32 v255, -v235, v235, s62\n"
"v_mfma_f32_16x16x32_bf16 v[44:47], v[224:227], v[236:239], v[44:47]\n"
"ds_read_u16_d16_hi v229, v229 offset:18432\n"
"v_mul_f32_e32 v254, v254, v234\n"
"v_lshlrev_b32_sdwa v230, 1, v230 dst_sel:DWORD dst_unused:UNUSED_PAD src0_sel:DWORD src1_sel:WORD_1\n"
"s_waitcnt lgkmcnt(10)\n"
"v_mfma_f32_16x16x32_bf16 v[236:239], v[180:183], v[120:123], 0\n"
"ds_read_u16_d16 v230, v230 offset:18432\n"
"v_mul_f32_e32 v255, v255, v235\n"
"s_setprio 1\n"
"v_mfma_f32_16x16x32_bf16 v[236:239], v[184:187], v[124:127], v[236:239]\n"
"s_waitcnt lgkmcnt(3)\n"
"v_or_b32 v244, v244, v245\n"
"v_exp_f32_e32 v254, v254\n"
"v_lshlrev_b32_sdwa v231, 1, v231 dst_sel:DWORD dst_unused:UNUSED_PAD src0_sel:DWORD src1_sel:WORD_1\n"
"v_mfma_f32_16x16x32_bf16 v[236:239], v[188:191], v[128:131], v[236:239]\n"
"v_or_b32 v245, v246, v247\n"
"v_exp_f32_e32 v255, v255\n"
"v_mfma_f32_16x16x32_bf16 v[240:243], v[192:195], v[120:123], 0\n"
"ds_read_u16_d16_hi v231, v231 offset:18432\n"
"v_cvt_pk_bf16_f32 v246, v248, v249\n"
"v_fma_f32 v254, v254, s63, s63\n"
"v_lshlrev_b32_sdwa v232, 1, v232 dst_sel:DWORD dst_unused:UNUSED_PAD src0_sel:DWORD src1_sel:WORD_1\n"
"v_mfma_f32_16x16x32_bf16 v[240:243], v[196:199], v[124:127], v[240:243]\n"
"ds_read_u16_d16 v232, v232 offset:18432\n"
"v_cvt_pk_bf16_f32 v247, v250, v251\n"
"v_fma_f32 v255, v255, s63, s63\n"
"v_mfma_f32_16x16x32_bf16 v[240:243], v[200:203], v[128:131], v[240:243]\n"
"v_rcp_f32_e32 v254, v254\n"
"v_lshlrev_b32_sdwa v233, 1, v233 dst_sel:DWORD dst_unused:UNUSED_PAD src0_sel:DWORD src1_sel:WORD_1\n"
"v_mfma_f32_16x16x32_bf16 v[48:51], v[204:207], v[244:247], v[48:51]\n"
"v_rcp_f32_e32 v255, v255\n"
"v_mfma_f32_16x16x32_bf16 v[52:55], v[208:211], v[244:247], v[52:55]\n"
"ds_read_u16_d16_hi v233, v233 offset:18432\n"
"v_mul_f32_e32 v234, v254, v234\n"
"v_mul_f32_e32 v235, v255, v235\n"
"v_mfma_f32_16x16x32_bf16 v[56:59], v[212:215], v[244:247], v[56:59]\n"
"v_lshlrev_b32_sdwa v236, 1, v236 dst_sel:DWORD dst_unused:UNUSED_PAD src0_sel:DWORD src1_sel:WORD_1\n"
"v_mul_f32_e32 v241, s64, v241\n"
"v_mul_f32_e32 v242, s64, v242\n"
"v_mfma_f32_16x16x32_bf16 v[60:63], v[216:219], v[244:247], v[60:63]\n"
"ds_read_u16_d16 v236, v236 offset:18432\n"
"v_mul_f32_e32 v243, s64, v243\n"
"v_fma_f32 v168, -v241, v241, s62\n"
"v_mfma_f32_16x16x32_bf16 v[64:67], v[220:223], v[244:247], v[64:67]\n"
"v_lshlrev_b32_sdwa v237, 1, v237 dst_sel:DWORD dst_unused:UNUSED_PAD src0_sel:DWORD src1_sel:WORD_1\n"
"v_fma_f32 v169, -v242, v242, s62\n"
"v_fma_f32 v170, -v243, v243, s62\n"
"v_mfma_f32_16x16x32_bf16 v[68:71], v[224:227], v[244:247], v[68:71]\n"
"ds_read_u16_d16_hi v237, v237 offset:18432\n"
"v_mul_f32_e32 v168, v168, v241\n"
"v_mul_f32_e32 v169, v169, v242\n"
"s_setprio 0\n"
"v_mfma_f32_16x16x32_bf16 v[244:247], v[180:183], v[132:135], 0\n"
"v_lshlrev_b32_sdwa v238, 1, v238 dst_sel:DWORD dst_unused:UNUSED_PAD src0_sel:DWORD src1_sel:WORD_1\n"
"v_mul_f32_e32 v170, v170, v243\n"
"v_exp_f32_e32 v168, v168\n"
"s_setprio 1\n"
"v_mbcnt_lo_u32_b32 v123, -1, 0\n"
"v_mbcnt_hi_u32_b32 v123, -1, v123\n"
"v_lshrrev_b32 v121, 2, v123\n"
"v_xor_b32 v121, s59, v121\n"
"v_bfe_u32 v122, v121, 1, 1\n"
"v_mul_i32_i24 v120, s96, v122\n"
"v_and_b32 v121, 1, v121\n"
"v_mfma_f32_16x16x32_bf16 v[244:247], v[184:187], v[136:139], v[244:247]\n"
"s_waitcnt lgkmcnt(2)\n"
"ds_read_u16_d16 v238, v238 offset:18432\n"
"v_or_b32 v228, v228, v229\n"
"v_exp_f32_e32 v169, v169\n"
"v_mul_u32_u24 v121, 192, v121\n"
"v_add3_u32 v120, v120, v121, s56\n"
"v_and_b32 v121, 3, v123\n"
"v_mul_u32_u24 v121, 1152, v121\n"
"v_and_b32 v122, 48, v123\n"
"v_add3_u32 v120, v120, v121, v122\n"
"s_mov_b64 s[48:49], s[34:35]\n"
"s_and_b32 s49, s49, 0xffff\n"
"s_mov_b32 s50, 28311552\n"
"s_mov_b32 s51, 0x20000\n"
"v_mfma_f32_16x16x32_bf16 v[244:247], v[188:191], v[140:143], v[244:247]\n"
"v_or_b32 v229, v230, v231\n"
"v_exp_f32_e32 v170, v170\n"
"v_lshlrev_b32_sdwa v239, 1, v239 dst_sel:DWORD dst_unused:UNUSED_PAD src0_sel:DWORD src1_sel:WORD_1\n"
"v_mfma_f32_16x16x32_bf16 v[248:251], v[192:195], v[132:135], 0\n"
"ds_read_u16_d16_hi v239, v239 offset:18432\n"
"v_or_b32 v230, v232, v233\n"
"v_fma_f32 v168, v168, s63, s63\n"
"v_fma_f32 v169, v169, s63, s63\n"
"v_mfma_f32_16x16x32_bf16 v[248:251], v[196:199], v[136:139], v[248:251]\n"
"v_cvt_pk_bf16_f32 v231, v234, v235\n"
"v_fma_f32 v170, v170, s63, s63\n"
"v_rcp_f32_e32 v168, v168\n"
"v_mfma_f32_16x16x32_bf16 v[248:251], v[200:203], v[140:143], v[248:251]\n"
"v_lshlrev_b32_sdwa v240, 1, v240 dst_sel:DWORD dst_unused:UNUSED_PAD src0_sel:DWORD src1_sel:WORD_1\n"
"v_rcp_f32_e32 v169, v169\n"
"v_mfma_f32_16x16x32_bf16 v[72:75], v[204:207], v[228:231], v[72:75]\n"
"ds_read_b64_tr_b16 v[204:205], v253 offset:12320\n"
"ds_read_b64_tr_b16 v[206:207], v253 offset:13856\n"
"ds_read_u16_d16_hi v240, v240 offset:18432\n"
"v_rcp_f32_e32 v170, v170\n"
"v_mfma_f32_16x16x32_bf16 v[76:79], v[208:211], v[228:231], v[76:79]\n"
"ds_read_b64_tr_b16 v[208:209], v253 offset:12328\n"
"ds_read_b64_tr_b16 v[210:211], v253 offset:13864\n"
"v_mul_f32_e32 v241, v168, v241\n"
"v_mul_f32_e32 v242, v169, v242\n"
"v_mul_f32_e32 v243, v170, v243\n"
"v_mfma_f32_16x16x32_bf16 v[80:83], v[212:215], v[228:231], v[80:83]\n"
"ds_read_b64_tr_b16 v[212:213], v253 offset:12384\n"
"ds_read_b64_tr_b16 v[214:215], v253 offset:13920\n"
"v_lshlrev_b32_sdwa v244, 1, v244 dst_sel:DWORD dst_unused:UNUSED_PAD src0_sel:DWORD src1_sel:WORD_1\n"
"v_mul_f32_e32 v250, s64, v250\n"
"v_mfma_f32_16x16x32_bf16 v[84:87], v[216:219], v[228:231], v[84:87]\n"
"ds_read_b64_tr_b16 v[216:217], v253 offset:12392\n"
"ds_read_b64_tr_b16 v[218:219], v253 offset:13928\n"
"ds_read_u16_d16 v244, v244 offset:18432\n"
"v_mul_f32_e32 v251, s64, v251\n"
"v_lshlrev_b32_sdwa v245, 1, v245 dst_sel:DWORD dst_unused:UNUSED_PAD src0_sel:DWORD src1_sel:WORD_1\n"
"v_mfma_f32_16x16x32_bf16 v[88:91], v[220:223], v[228:231], v[88:91]\n"
"ds_read_b64_tr_b16 v[220:221], v253 offset:12448\n"
"ds_read_b64_tr_b16 v[222:223], v253 offset:13984\n"
"v_fma_f32 v254, -v250, v250, s62\n"
"v_fma_f32 v255, -v251, v251, s62\n"
"v_mfma_f32_16x16x32_bf16 v[92:95], v[224:227], v[228:231], v[92:95]\n"
"ds_read_b64_tr_b16 v[224:225], v253 offset:12456\n"
"ds_read_b64_tr_b16 v[226:227], v253 offset:13992\n"
"ds_read_u16_d16_hi v245, v245 offset:18432\n"
"v_mul_f32_e32 v254, v254, v250\n"
"v_lshlrev_b32_sdwa v246, 1, v246 dst_sel:DWORD dst_unused:UNUSED_PAD src0_sel:DWORD src1_sel:WORD_1\n"
"v_mfma_f32_16x16x32_bf16 v[228:231], v[180:183], v[144:147], 0\n"
"ds_read_u16_d16 v246, v246 offset:18432\n"
"v_mul_f32_e32 v255, v255, v251\n"
"s_setprio 1\n"
"v_mfma_f32_16x16x32_bf16 v[228:231], v[184:187], v[148:151], v[228:231]\n"
"s_waitcnt lgkmcnt(13)\n"
"v_or_b32 v236, v236, v237\n"
"v_exp_f32_e32 v254, v254\n"
"v_lshlrev_b32_sdwa v247, 1, v247 dst_sel:DWORD dst_unused:UNUSED_PAD src0_sel:DWORD src1_sel:WORD_1\n"
"v_mfma_f32_16x16x32_bf16 v[228:231], v[188:191], v[152:155], v[228:231]\n"
"v_or_b32 v237, v238, v239\n"
"v_exp_f32_e32 v255, v255\n"
"v_mfma_f32_16x16x32_bf16 v[232:235], v[192:195], v[144:147], 0\n"
"ds_read_u16_d16_hi v247, v247 offset:18432\n"
"v_cvt_pk_bf16_f32 v238, v240, v241\n"
"v_fma_f32 v254, v254, s63, s63\n"
"v_lshlrev_b32_sdwa v248, 1, v248 dst_sel:DWORD dst_unused:UNUSED_PAD src0_sel:DWORD src1_sel:WORD_1\n"
"v_mfma_f32_16x16x32_bf16 v[232:235], v[196:199], v[148:151], v[232:235]\n"
"ds_read_u16_d16 v248, v248 offset:18432\n"
"v_cvt_pk_bf16_f32 v239, v242, v243\n"
"v_fma_f32 v255, v255, s63, s63\n"
"v_mfma_f32_16x16x32_bf16 v[232:235], v[200:203], v[152:155], v[232:235]\n"
"v_rcp_f32_e32 v254, v254\n"
"v_lshlrev_b32_sdwa v249, 1, v249 dst_sel:DWORD dst_unused:UNUSED_PAD src0_sel:DWORD src1_sel:WORD_1\n"
"s_waitcnt lgkmcnt(11)\n"
"v_mfma_f32_16x16x32_bf16 v[0:3], v[204:207], v[236:239], v[0:3]\n"
"v_rcp_f32_e32 v255, v255\n"
"v_mfma_f32_16x16x32_bf16 v[4:7], v[208:211], v[236:239], v[4:7]\n"
"ds_read_u16_d16_hi v249, v249 offset:18432\n"
"v_mul_f32_e32 v250, v254, v250\n"
"v_mul_f32_e32 v251, v255, v251\n"
"v_mfma_f32_16x16x32_bf16 v[8:11], v[212:215], v[236:239], v[8:11]\n"
"v_lshlrev_b32_sdwa v228, 1, v228 dst_sel:DWORD dst_unused:UNUSED_PAD src0_sel:DWORD src1_sel:WORD_1\n"
"v_mul_f32_e32 v233, s64, v233\n"
"v_mul_f32_e32 v234, s64, v234\n"
"s_waitcnt lgkmcnt(5)\n"
"v_mfma_f32_16x16x32_bf16 v[12:15], v[216:219], v[236:239], v[12:15]\n"
"ds_read_u16_d16 v228, v228 offset:18432\n"
"v_mul_f32_e32 v235, s64, v235\n"
"v_fma_f32 v168, -v233, v233, s62\n"
"v_mfma_f32_16x16x32_bf16 v[16:19], v[220:223], v[236:239], v[16:19]\n"
"v_lshlrev_b32_sdwa v229, 1, v229 dst_sel:DWORD dst_unused:UNUSED_PAD src0_sel:DWORD src1_sel:WORD_1\n"
"v_fma_f32 v169, -v234, v234, s62\n"
"v_fma_f32 v170, -v235, v235, s62\n"
"s_add_u32 s68, s57, 768\n"
"v_cvt_pk_bf16_f32 v0, v0, v1\n"
"v_cvt_pk_bf16_f32 v1, v2, v3\n"
"v_cvt_pk_bf16_f32 v2, v4, v5\n"
"v_cvt_pk_bf16_f32 v3, v6, v7\n"
"v_mfma_f32_16x16x32_bf16 v[20:23], v[224:227], v[236:239], v[20:23]\n"
"buffer_store_dwordx4 v[0:3], v120, s[48:51], s68 offen offset:0 sc1\n"
"ds_read_u16_d16_hi v229, v229 offset:18432\n"
"v_mul_f32_e32 v168, v168, v233\n"
"v_mul_f32_e32 v169, v169, v234\n"
"v_mfma_f32_16x16x32_bf16 v[236:239], v[180:183], v[156:159], 0\n"
"ds_read_b64_tr_b16 v[180:181], v252 offset:24320\n"
"ds_read_b64_tr_b16 v[182:183], v252 offset:27392\n"
"v_lshlrev_b32_sdwa v230, 1, v230 dst_sel:DWORD dst_unused:UNUSED_PAD src0_sel:DWORD src1_sel:WORD_1\n"
"v_mul_f32_e32 v170, v170, v235\n"
"v_exp_f32_e32 v168, v168\n"
"s_setprio 1\n"
"v_mfma_f32_16x16x32_bf16 v[236:239], v[184:187], v[160:163], v[236:239]\n"
"ds_read_b64_tr_b16 v[184:185], v252 offset:25344\n"
"ds_read_b64_tr_b16 v[186:187], v252 offset:28416\n"
"s_waitcnt lgkmcnt(6)\n"
"ds_read_u16_d16 v230, v230 offset:18432\n"
"v_or_b32 v244, v244, v245\n"
"v_exp_f32_e32 v169, v169\n"
"v_mfma_f32_16x16x32_bf16 v[236:239], v[188:191], v[164:167], v[236:239]\n"
"ds_read_b64_tr_b16 v[188:189], v252 offset:26368\n"
"ds_read_b64_tr_b16 v[190:191], v252 offset:29440\n"
"v_or_b32 v245, v246, v247\n"
"v_exp_f32_e32 v170, v170\n"
"v_lshlrev_b32_sdwa v231, 1, v231 dst_sel:DWORD dst_unused:UNUSED_PAD src0_sel:DWORD src1_sel:WORD_1\n"
"v_cvt_pk_bf16_f32 v8, v8, v9\n"
"v_cvt_pk_bf16_f32 v9, v10, v11\n"
"v_cvt_pk_bf16_f32 v10, v12, v13\n"
"v_cvt_pk_bf16_f32 v11, v14, v15\n"
"v_mfma_f32_16x16x32_bf16 v[240:243], v[192:195], v[156:159], 0\n"
"buffer_store_dwordx4 v[8:11], v120, s[48:51], s68 offen offset:64 sc1\n"
"ds_read_b64_tr_b16 v[192:193], v252 offset:24832\n"
"ds_read_b64_tr_b16 v[194:195], v252 offset:27904\n"
"ds_read_u16_d16_hi v231, v231 offset:18432\n"
"v_or_b32 v246, v248, v249\n"
"v_fma_f32 v168, v168, s63, s63\n"
"v_fma_f32 v169, v169, s63, s63\n"
"v_mfma_f32_16x16x32_bf16 v[240:243], v[196:199], v[160:163], v[240:243]\n"
"ds_read_b64_tr_b16 v[196:197], v252 offset:25856\n"
"ds_read_b64_tr_b16 v[198:199], v252 offset:28928\n"
"v_cvt_pk_bf16_f32 v247, v250, v251\n"
"v_fma_f32 v170, v170, s63, s63\n"
"v_rcp_f32_e32 v168, v168\n"
"v_mfma_f32_16x16x32_bf16 v[240:243], v[200:203], v[164:167], v[240:243]\n"
"ds_read_b64_tr_b16 v[200:201], v252 offset:26880\n"
"ds_read_b64_tr_b16 v[202:203], v252 offset:29952\n"
"v_lshlrev_b32_sdwa v232, 1, v232 dst_sel:DWORD dst_unused:UNUSED_PAD src0_sel:DWORD src1_sel:WORD_1\n"
"v_rcp_f32_e32 v169, v169\n"
"v_mfma_f32_16x16x32_bf16 v[24:27], v[204:207], v[244:247], v[24:27]\n"
"ds_read_u16_d16_hi v232, v232 offset:18432\n"
"v_rcp_f32_e32 v170, v170\n"
"v_cvt_pk_bf16_f32 v16, v16, v17\n"
"v_cvt_pk_bf16_f32 v17, v18, v19\n"
"v_cvt_pk_bf16_f32 v18, v20, v21\n"
"v_cvt_pk_bf16_f32 v19, v22, v23\n"
"v_mfma_f32_16x16x32_bf16 v[28:31], v[208:211], v[244:247], v[28:31]\n"
"buffer_store_dwordx4 v[16:19], v120, s[48:51], s68 offen offset:128 sc1\n"
"v_mul_f32_e32 v233, v168, v233\n"
"v_mul_f32_e32 v234, v169, v234\n"
"v_mul_f32_e32 v235, v170, v235\n"
"v_mfma_f32_16x16x32_bf16 v[32:35], v[212:215], v[244:247], v[32:35]\n"
"v_lshlrev_b32_sdwa v236, 1, v236 dst_sel:DWORD dst_unused:UNUSED_PAD src0_sel:DWORD src1_sel:WORD_1\n"
"v_mul_f32_e32 v242, s64, v242\n"
"v_mfma_f32_16x16x32_bf16 v[36:39], v[216:219], v[244:247], v[36:39]\n"
"ds_read_u16_d16 v236, v236 offset:18432\n"
"v_mul_f32_e32 v243, s64, v243\n"
"v_lshlrev_b32_sdwa v237, 1, v237 dst_sel:DWORD dst_unused:UNUSED_PAD src0_sel:DWORD src1_sel:WORD_1\n"
"v_mfma_f32_16x16x32_bf16 v[40:43], v[220:223], v[244:247], v[40:43]\n"
"v_fma_f32 v254, -v242, v242, s62\n"
"v_fma_f32 v255, -v243, v243, s62\n"
"s_mov_b32 s68, 590592\n"
"v_cvt_pk_bf16_f32 v24, v24, v25\n"
"v_cvt_pk_bf16_f32 v25, v26, v27\n"
"v_cvt_pk_bf16_f32 v26, v28, v29\n"
"v_cvt_pk_bf16_f32 v27, v30, v31\n"
"v_mfma_f32_16x16x32_bf16 v[44:47], v[224:227], v[244:247], v[44:47]\n"
"buffer_store_dwordx4 v[24:27], v120, s[48:51], s68 offen offset:0 sc1\n"
"ds_read_u16_d16_hi v237, v237 offset:18432\n"
"v_mul_f32_e32 v254, v254, v242\n"
"v_lshlrev_b32_sdwa v238, 1, v238 dst_sel:DWORD dst_unused:UNUSED_PAD src0_sel:DWORD src1_sel:WORD_1\n"
"s_setprio 0\n"
"s_waitcnt lgkmcnt(10)\n"
"v_mfma_f32_16x16x32_bf16 v[244:247], v[180:183], v[156:159], 0\n"
"ds_read_u16_d16 v238, v238 offset:18432\n"
"v_mul_f32_e32 v255, v255, v243\n"
"s_setprio 1\n"
"v_mfma_f32_16x16x32_bf16 v[244:247], v[184:187], v[160:163], v[244:247]\n"
"s_waitcnt lgkmcnt(3)\n"
"v_or_b32 v228, v228, v229\n"
"v_exp_f32_e32 v254, v254\n"
"v_lshlrev_b32_sdwa v239, 1, v239 dst_sel:DWORD dst_unused:UNUSED_PAD src0_sel:DWORD src1_sel:WORD_1\n"
"v_mfma_f32_16x16x32_bf16 v[244:247], v[188:191], v[164:167], v[244:247]\n"
"v_or_b32 v229, v230, v231\n"
"v_exp_f32_e32 v255, v255\n"
"v_cvt_pk_bf16_f32 v32, v32, v33\n"
"v_cvt_pk_bf16_f32 v33, v34, v35\n"
"v_cvt_pk_bf16_f32 v34, v36, v37\n"
"v_cvt_pk_bf16_f32 v35, v38, v39\n"
"v_mfma_f32_16x16x32_bf16 v[248:251], v[192:195], v[156:159], 0\n"
"buffer_store_dwordx4 v[32:35], v120, s[48:51], s68 offen offset:64 sc1\n"
"ds_read_u16_d16_hi v239, v239 offset:18432\n"
"v_cvt_pk_bf16_f32 v230, v232, v233\n"
"v_fma_f32 v254, v254, s63, s63\n"
"v_lshlrev_b32_sdwa v240, 1, v240 dst_sel:DWORD dst_unused:UNUSED_PAD src0_sel:DWORD src1_sel:WORD_1\n"
"v_mfma_f32_16x16x32_bf16 v[248:251], v[196:199], v[160:163], v[248:251]\n"
"ds_read_u16_d16 v240, v240 offset:18432\n"
"v_cvt_pk_bf16_f32 v231, v234, v235\n"
"v_fma_f32 v255, v255, s63, s63\n"
"v_mfma_f32_16x16x32_bf16 v[248:251], v[200:203], v[164:167], v[248:251]\n"
"v_rcp_f32_e32 v254, v254\n"
"v_lshlrev_b32_sdwa v241, 1, v241 dst_sel:DWORD dst_unused:UNUSED_PAD src0_sel:DWORD src1_sel:WORD_1\n"
"v_mfma_f32_16x16x32_bf16 v[48:51], v[204:207], v[228:231], v[48:51]\n"
"v_rcp_f32_e32 v255, v255\n"
"v_cvt_pk_bf16_f32 v40, v40, v41\n"
"v_cvt_pk_bf16_f32 v41, v42, v43\n"
"v_cvt_pk_bf16_f32 v42, v44, v45\n"
"v_cvt_pk_bf16_f32 v43, v46, v47\n"
"v_mfma_f32_16x16x32_bf16 v[52:55], v[208:211], v[228:231], v[52:55]\n"
"buffer_store_dwordx4 v[40:43], v120, s[48:51], s68 offen offset:128 sc1\n"
"ds_read_u16_d16_hi v241, v241 offset:18432\n"
"v_mul_f32_e32 v242, v254, v242\n"
"v_mul_f32_e32 v243, v255, v243\n"
"v_mfma_f32_16x16x32_bf16 v[56:59], v[212:215], v[228:231], v[56:59]\n"
"v_lshlrev_b32_sdwa v244, 1, v244 dst_sel:DWORD dst_unused:UNUSED_PAD src0_sel:DWORD src1_sel:WORD_1\n"
"v_mul_f32_e32 v249, s64, v249\n"
"v_mul_f32_e32 v250, s64, v250\n"
"v_mfma_f32_16x16x32_bf16 v[60:63], v[216:219], v[228:231], v[60:63]\n"
"ds_read_u16_d16 v244, v244 offset:18432\n"
"v_mul_f32_e32 v251, s64, v251\n"
"v_fma_f32 v168, -v249, v249, s62\n"
"v_mfma_f32_16x16x32_bf16 v[64:67], v[220:223], v[228:231], v[64:67]\n"
"v_lshlrev_b32_sdwa v245, 1, v245 dst_sel:DWORD dst_unused:UNUSED_PAD src0_sel:DWORD src1_sel:WORD_1\n"
"v_fma_f32 v169, -v250, v250, s62\n"
"v_fma_f32 v170, -v251, v251, s62\n"
"s_add_u32 s68, s69, 384\n"
"v_cvt_pk_bf16_f32 v48, v48, v49\n"
"v_cvt_pk_bf16_f32 v49, v50, v51\n"
"v_cvt_pk_bf16_f32 v50, v52, v53\n"
"v_cvt_pk_bf16_f32 v51, v54, v55\n"
"v_mfma_f32_16x16x32_bf16 v[68:71], v[224:227], v[228:231], v[68:71]\n"
"buffer_store_dwordx4 v[48:51], v120, s[48:51], s68 offen offset:0 sc1\n"
"ds_read_u16_d16_hi v245, v245 offset:18432\n"
"v_mul_f32_e32 v168, v168, v249\n"
"v_mul_f32_e32 v169, v169, v250\n"
"v_lshlrev_b32_sdwa v246, 1, v246 dst_sel:DWORD dst_unused:UNUSED_PAD src0_sel:DWORD src1_sel:WORD_1\n"
"v_mul_f32_e32 v170, v170, v251\n"
"v_exp_f32_e32 v168, v168\n"
"s_waitcnt lgkmcnt(2)\n"
"ds_read_u16_d16 v246, v246 offset:18432\n"
"v_or_b32 v236, v236, v237\n"
"v_exp_f32_e32 v169, v169\n"
"v_or_b32 v237, v238, v239\n"
"v_exp_f32_e32 v170, v170\n"
"v_lshlrev_b32_sdwa v247, 1, v247 dst_sel:DWORD dst_unused:UNUSED_PAD src0_sel:DWORD src1_sel:WORD_1\n"
"v_cvt_pk_bf16_f32 v56, v56, v57\n"
"v_cvt_pk_bf16_f32 v57, v58, v59\n"
"v_cvt_pk_bf16_f32 v58, v60, v61\n"
"v_cvt_pk_bf16_f32 v59, v62, v63\n"
"buffer_store_dwordx4 v[56:59], v120, s[48:51], s68 offen offset:64 sc1\n"
"ds_read_u16_d16_hi v247, v247 offset:18432\n"
"v_or_b32 v238, v240, v241\n"
"v_fma_f32 v168, v168, s63, s63\n"
"v_fma_f32 v169, v169, s63, s63\n"
"v_cvt_pk_bf16_f32 v239, v242, v243\n"
"v_fma_f32 v170, v170, s63, s63\n"
"v_rcp_f32_e32 v168, v168\n"
"v_lshlrev_b32_sdwa v248, 1, v248 dst_sel:DWORD dst_unused:UNUSED_PAD src0_sel:DWORD src1_sel:WORD_1\n"
"v_rcp_f32_e32 v169, v169\n"
"v_mfma_f32_16x16x32_bf16 v[72:75], v[204:207], v[236:239], v[72:75]\n"
"ds_read_b64_tr_b16 v[204:205], v253 offset:18480\n"
"ds_read_b64_tr_b16 v[206:207], v253 offset:20016\n"
"ds_read_u16_d16_hi v248, v248 offset:18432\n"
"v_rcp_f32_e32 v170, v170\n"
"v_cvt_pk_bf16_f32 v64, v64, v65\n"
"v_cvt_pk_bf16_f32 v65, v66, v67\n"
"v_cvt_pk_bf16_f32 v66, v68, v69\n"
"v_cvt_pk_bf16_f32 v67, v70, v71\n"
"v_mfma_f32_16x16x32_bf16 v[76:79], v[208:211], v[236:239], v[76:79]\n"
"buffer_store_dwordx4 v[64:67], v120, s[48:51], s68 offen offset:128 sc1\n"
"ds_read_b64_tr_b16 v[208:209], v253 offset:18488\n"
"ds_read_b64_tr_b16 v[210:211], v253 offset:20024\n"
"v_mul_f32_e32 v249, v168, v249\n"
"v_mul_f32_e32 v250, v169, v250\n"
"v_mul_f32_e32 v251, v170, v251\n"
"v_mfma_f32_16x16x32_bf16 v[80:83], v[212:215], v[236:239], v[80:83]\n"
"ds_read_b64_tr_b16 v[212:213], v253 offset:18544\n"
"ds_read_b64_tr_b16 v[214:215], v253 offset:20080\n"
"v_mfma_f32_16x16x32_bf16 v[84:87], v[216:219], v[236:239], v[84:87]\n"
"ds_read_b64_tr_b16 v[216:217], v253 offset:18552\n"
"ds_read_b64_tr_b16 v[218:219], v253 offset:20088\n"
"v_mfma_f32_16x16x32_bf16 v[88:91], v[220:223], v[236:239], v[88:91]\n"
"ds_read_b64_tr_b16 v[220:221], v253 offset:18608\n"
"ds_read_b64_tr_b16 v[222:223], v253 offset:20144\n"
"v_mfma_f32_16x16x32_bf16 v[92:95], v[224:227], v[236:239], v[92:95]\n"
"ds_read_b64_tr_b16 v[224:225], v253 offset:18616\n"
"ds_read_b64_tr_b16 v[226:227], v253 offset:20152\n"
"s_waitcnt lgkmcnt(10)\n"
"v_or_b32 v244, v244, v245\n"
"v_or_b32 v245, v246, v247\n"
"v_cvt_pk_bf16_f32 v246, v248, v249\n"
"v_cvt_pk_bf16_f32 v247, v250, v251\n"
"s_waitcnt lgkmcnt(6)\n"
"s_nop 0\n"
"v_mfma_f32_16x16x32_bf16 v[72:75], v[204:207], v[244:247], v[72:75]\n"
"v_mfma_f32_16x16x32_bf16 v[76:79], v[208:211], v[244:247], v[76:79]\n"
"v_mfma_f32_16x16x32_bf16 v[80:83], v[212:215], v[244:247], v[80:83]\n"
"s_waitcnt lgkmcnt(0)\n"
"v_mfma_f32_16x16x32_bf16 v[84:87], v[216:219], v[244:247], v[84:87]\n"
"v_mfma_f32_16x16x32_bf16 v[88:91], v[220:223], v[244:247], v[88:91]\n"
"s_add_u32 s68, s69, 768\n"
"s_nop 1\n"
"v_cvt_pk_bf16_f32 v72, v72, v73\n"
"v_cvt_pk_bf16_f32 v73, v74, v75\n"
"v_cvt_pk_bf16_f32 v74, v76, v77\n"
"v_cvt_pk_bf16_f32 v75, v78, v79\n"
"v_mfma_f32_16x16x32_bf16 v[92:95], v[224:227], v[244:247], v[92:95]\n"
"buffer_store_dwordx4 v[72:75], v120, s[48:51], s68 offen offset:0 sc1\n"
"v_cvt_pk_bf16_f32 v80, v80, v81\n"
"v_cvt_pk_bf16_f32 v81, v82, v83\n"
"v_cvt_pk_bf16_f32 v82, v84, v85\n"
"v_cvt_pk_bf16_f32 v83, v86, v87\n"
"buffer_store_dwordx4 v[80:83], v120, s[48:51], s68 offen offset:64 sc1\n"
"v_cvt_pk_bf16_f32 v88, v88, v89\n"
"v_cvt_pk_bf16_f32 v89, v90, v91\n"
"v_cvt_pk_bf16_f32 v90, v92, v93\n"
"v_cvt_pk_bf16_f32 v91, v94, v95\n"
"buffer_store_dwordx4 v[88:91], v120, s[48:51], s68 offen offset:128 sc1\n"
"s_setprio 0\n"
"v_mbcnt_lo_u32_b32 v130, -1, 0\n"
"v_mbcnt_hi_u32_b32 v130, -1, v130\n"
"v_lshlrev_b32 v130, 4, v130\n"
"s_mul_i32 s66, s59, 18432\n"
"s_cmp_eq_u32 s59, 0\n"
"s_cselect_b32 s67, 0, 5888\n"
"s_add_u32 s66, s66, s67\n"
"v_add_u32 v130, s66, v130\n"
"ds_read_b128 v[132:135], v130 offset:0\n"
"ds_read_b128 v[136:139], v130 offset:1024\n"
"ds_read_b128 v[140:143], v130 offset:2048\n"
"ds_read_b128 v[144:147], v130 offset:3072\n"
"ds_read_b128 v[148:151], v130 offset:4096\n"
"ds_read_b128 v[152:155], v130 offset:5120\n"
"ds_read_b128 v[156:159], v130 offset:6144\n"
"ds_read_b128 v[160:163], v130 offset:7168\n"
"ds_read_b128 v[164:167], v130 offset:8192\n"
"ds_read_b128 v[168:171], v130 offset:9216\n"
"ds_read_b128 v[172:175], v130 offset:10240\n"
"ds_read_b128 v[176:179], v130 offset:11264\n"
"ds_read_b128 v[180:183], v130 offset:12288\n"
"ds_read_b128 v[184:187], v130 offset:13312\n"
"ds_read_b128 v[188:191], v130 offset:14336\n"
"ds_read_b128 v[192:195], v130 offset:15360\n"
"ds_read_b128 v[196:199], v130 offset:16384\n"
"ds_read_b128 v[200:203], v130 offset:17408\n"
"v_mov_b32 v128, 0\n"
"v_mov_b32 v129, 0\n"
"s_waitcnt lgkmcnt(14)\n"
"v_pk_maximum3_f16 v128, v128, v132, v133\n"
"v_pk_minimum3_f16 v129, v129, v132, v133\n"
"v_pk_maximum3_f16 v128, v128, v134, v135\n"
"v_pk_minimum3_f16 v129, v129, v134, v135\n"
"v_pk_maximum3_f16 v128, v128, v136, v137\n"
"v_pk_minimum3_f16 v129, v129, v136, v137\n"
"v_pk_maximum3_f16 v128, v128, v138, v139\n"
"v_pk_minimum3_f16 v129, v129, v138, v139\n"
"v_pk_maximum3_f16 v128, v128, v140, v141\n"
"v_pk_minimum3_f16 v129, v129, v140, v141\n"
"v_pk_maximum3_f16 v128, v128, v142, v143\n"
"v_pk_minimum3_f16 v129, v129, v142, v143\n"
"v_pk_maximum3_f16 v128, v128, v144, v145\n"
"v_pk_minimum3_f16 v129, v129, v144, v145\n"
"v_pk_maximum3_f16 v128, v128, v146, v147\n"
"v_pk_minimum3_f16 v129, v129, v146, v147\n"
"s_waitcnt lgkmcnt(13)\n"
"v_pk_maximum3_f16 v128, v128, v148, v149\n"
"v_pk_minimum3_f16 v129, v129, v148, v149\n"
"v_pk_maximum3_f16 v128, v128, v150, v151\n"
"v_pk_minimum3_f16 v129, v129, v150, v151\n"
"s_waitcnt lgkmcnt(12)\n"
"v_pk_maximum3_f16 v128, v128, v152, v153\n"
"v_pk_minimum3_f16 v129, v129, v152, v153\n"
"v_pk_maximum3_f16 v128, v128, v154, v155\n"
"v_pk_minimum3_f16 v129, v129, v154, v155\n"
"s_waitcnt lgkmcnt(11)\n"
"v_pk_maximum3_f16 v128, v128, v156, v157\n"
"v_pk_minimum3_f16 v129, v129, v156, v157\n"
"v_pk_maximum3_f16 v128, v128, v158, v159\n"
"v_pk_minimum3_f16 v129, v129, v158, v159\n"
"s_waitcnt lgkmcnt(10)\n"
"v_pk_maximum3_f16 v128, v128, v160, v161\n"
"v_pk_minimum3_f16 v129, v129, v160, v161\n"
"v_pk_maximum3_f16 v128, v128, v162, v163\n"
"v_pk_minimum3_f16 v129, v129, v162, v163\n"
"s_waitcnt lgkmcnt(9)\n"
"v_pk_maximum3_f16 v128, v128, v164, v165\n"
"v_pk_minimum3_f16 v129, v129, v164, v165\n"
"v_pk_maximum3_f16 v128, v128, v166, v167\n"
"v_pk_minimum3_f16 v129, v129, v166, v167\n"
"s_waitcnt lgkmcnt(8)\n"
"v_pk_maximum3_f16 v128, v128, v168, v169\n"
"v_pk_minimum3_f16 v129, v129, v168, v169\n"
"v_pk_maximum3_f16 v128, v128, v170, v171\n"
"v_pk_minimum3_f16 v129, v129, v170, v171\n"
"s_waitcnt lgkmcnt(7)\n"
"v_pk_maximum3_f16 v128, v128, v172, v173\n"
"v_pk_minimum3_f16 v129, v129, v172, v173\n"
"v_pk_maximum3_f16 v128, v128, v174, v175\n"
"v_pk_minimum3_f16 v129, v129, v174, v175\n"
"s_waitcnt lgkmcnt(6)\n"
"v_pk_maximum3_f16 v128, v128, v176, v177\n"
"v_pk_minimum3_f16 v129, v129, v176, v177\n"
"v_pk_maximum3_f16 v128, v128, v178, v179\n"
"v_pk_minimum3_f16 v129, v129, v178, v179\n"
"s_waitcnt lgkmcnt(5)\n"
"v_pk_maximum3_f16 v128, v128, v180, v181\n"
"v_pk_minimum3_f16 v129, v129, v180, v181\n"
"v_pk_maximum3_f16 v128, v128, v182, v183\n"
"v_pk_minimum3_f16 v129, v129, v182, v183\n"
"s_waitcnt lgkmcnt(4)\n"
"v_pk_maximum3_f16 v128, v128, v184, v185\n"
"v_pk_minimum3_f16 v129, v129, v184, v185\n"
"v_pk_maximum3_f16 v128, v128, v186, v187\n"
"v_pk_minimum3_f16 v129, v129, v186, v187\n"
"s_waitcnt lgkmcnt(3)\n"
"v_pk_maximum3_f16 v128, v128, v188, v189\n"
"v_pk_minimum3_f16 v129, v129, v188, v189\n"
"v_pk_maximum3_f16 v128, v128, v190, v191\n"
"v_pk_minimum3_f16 v129, v129, v190, v191\n"
"s_waitcnt lgkmcnt(2)\n"
"v_pk_maximum3_f16 v128, v128, v192, v193\n"
"v_pk_minimum3_f16 v129, v129, v192, v193\n"
"v_pk_maximum3_f16 v128, v128, v194, v195\n"
"v_pk_minimum3_f16 v129, v129, v194, v195\n"
"s_waitcnt lgkmcnt(1)\n"
"v_pk_maximum3_f16 v128, v128, v196, v197\n"
"v_pk_minimum3_f16 v129, v129, v196, v197\n"
"v_pk_maximum3_f16 v128, v128, v198, v199\n"
"v_pk_minimum3_f16 v129, v129, v198, v199\n"
"s_waitcnt lgkmcnt(0)\n"
"v_pk_maximum3_f16 v128, v128, v200, v201\n"
"v_pk_minimum3_f16 v129, v129, v200, v201\n"
"v_pk_maximum3_f16 v128, v128, v202, v203\n"
"v_pk_minimum3_f16 v129, v129, v202, v203\n"
"v_and_b32 v128, 0x7fff7fff, v128\n"
"v_and_b32 v129, 0x7fff7fff, v129\n"
"v_pk_max_u16 v128, v128, v129\n"
"v_lshrrev_b32 v129, 16, v128\n"
"v_and_b32 v128, 0xffff, v128\n"
"v_max_u32 v128, v128, v129\n"
"v_cmp_lt_u32 vcc, 0x40a0, v128\n"
"s_nop 0\n"
"s_or_b64 s[80:81], s[80:81], vcc\n"
"v_cmp_le_u32 vcc, 0x3a80, v128\n"
"s_nop 0\n"
"s_cmp_lg_u64 vcc, 0\n"
"s_cselect_b32 s67, 2, 0\n"
".Le_%=:\n"
"s_cmp_lg_u64 s[80:81], 0\n"
"s_cselect_b32 s66, 1, 0\n"
"s_or_b32 s66, s66, s67\n"
"v_mov_b32 v128, 163780\n"
"v_mov_b32 v129, s66\n"
"s_mov_b64 exec, 1\n"
"ds_or_b32 v128, v129\n"
"s_mov_b64 exec, -1\n"
"s_waitcnt lgkmcnt(0)\n"
"s_barrier\n"
"ds_read_b32 v129, v128\n"
"s_waitcnt lgkmcnt(0)\n"
"v_readfirstlane_b32 s66, v129\n"
"s_nop 1\n"
"s_xor_b32 %[fail], s66, 2\n"
"s_cmp_lg_u32 %[fail], 0\n"
"s_cbranch_scc1 .Lz_%=\n"
"s_endpgm\n"
".Lz_%=:\n"
      : [fail] "=&s"(fail)
      : [ka] "s"(__builtin_amdgcn_kernarg_segment_ptr()), [wid] "s"(wid), [bid] "s"(bid)
      : "v0","v1","v2","v3","v4","v5","v6","v7","v8","v9","v10","v11","v12","v13","v14","v15","v16","v17","v18","v19","v20","v21","v22","v23","v24","v25","v26","v27","v28","v29","v30","v31","v32","v33","v34","v35","v36","v37","v38","v39","v40","v41","v42","v43","v44","v45","v46","v47","v48","v49","v50","v51","v52","v53","v54","v55","v56","v57","v58","v59","v60","v61","v62","v63","v64","v65","v66","v67","v68","v69","v70","v71","v72","v73","v74","v75","v76","v77","v78","v79","v80","v81","v82","v83","v84","v85","v86","v87","v88","v89","v90","v91","v92","v93","v94","v95","v96","v97","v98","v99","v100","v101","v102","v103","v104","v105","v106","v107","v108","v109","v110","v111","v112","v113","v114","v115","v116","v117","v118","v119","v120","v121","v122","v123","v124","v125","v126","v127","v128","v129","v130","v131","v132","v133","v134","v135","v136","v137","v138","v139","v140","v141","v142","v143","v144","v145","v146","v147","v148","v149","v150","v151","v152","v153","v154","v155","v156","v157","v158","v159","v160","v161","v162","v163","v164","v165","v166","v167","v168","v169","v170","v171","v172","v173","v174","v175","v176","v177","v178","v179","v180","v181","v182","v183","v184","v185","v186","v187","v188","v189","v190","v191","v192","v193","v194","v195","v196","v197","v198","v199","v200","v201","v202","v203","v204","v205","v206","v207","v208","v209","v210","v211","v212","v213","v214","v215","v216","v217","v218","v219","v220","v221","v222","v223","v224","v225","v226","v227","v228","v229","v230","v231","v232","v233","v234","v235","v236","v237","v238","v239","v240","v241","v242","v243","v244","v245","v246","v247","v248","v249","v250","v251","v252","v253","v254","v255","s34","s35","s36","s37","s38","s39","s40","s41","s42","s43","s44","s45","s46","s47","s48","s49","s50","s51","s52","s53","s54","s55","s56","s57","s58","s59","s60","s61","s62","s63","s64","s65","s66","s67","s68","s69","s70","s71","s72","s73","s74","s75","s76","s77","s78","s79","s80","s81","s82","s83","s84","s85","s86","s87","s88","s89","s90","s91","s92","s93","s94","s95","s96","s97","s98","s99","scc","vcc","memory");
  if (fail) {
    asm volatile("s_waitcnt vmcnt(0)" ::: "memory");
    __syncthreads();
    const int lane = __builtin_amdgcn_mbcnt_hi(~0u, __builtin_amdgcn_mbcnt_lo(~0u, 0));
    float* hs = (float*)lds + wid * 512;
    float* gs = hs + 96;
    const int g = bid & 7, j = bid >> 3;
    const int ty = (g >> 1) * 4 + (j >> 3), tx = (g & 1) * 8 + (j & 7);
    const bool two = lane < 32;
    const int c1 = two ? lane + 64 : lane;
#pragma unroll 1
    for (int p = wid; p < 576; p += 8) {
      const int py = ty * 24 + p / 24, px = tx * 24 + p % 24;
      float y0 = 0.f, y1 = 0.f;
#pragma unroll 1
      for (int t = 0; t < 25; ++t) {
        const int ky = t / 5, kx = t % 5, yy = py + ky - 2, xx = px + kx - 2;
        if (yy < 0 || yy >= 384 || xx < 0 || xx >= 384) continue;
        const u16* xp = x + (yy * 384 + xx) * 96;
        const float a0 = bf2f(xp[lane]), a1 = bf2f(xp[c1]);
        y0 += a0 * bf2f(dws[t * 96 + lane]); y1 += a1 * bf2f(dws[t * 96 + c1]);
        if (ky >= 1 && ky <= 3 && kx >= 1 && kx <= 3) {
          const int u = (ky - 1) * 3 + kx - 1;
          y0 += a0 * bf2f(dwl[u * 96 + lane]); y1 += a1 * bf2f(dwl[u * 96 + c1]);
        }
      }
      const float m = wsum(y0 + (two ? y1 : 0.f)) * (1.f / 96.f);
      const float d0 = y0 - m, d1 = y1 - m;
      const float r = 1.f / sqrtf(wsum(d0 * d0 + (two ? d1 * d1 : 0.f)) * (1.f / 96.f) + 1e-5f);
      hs[lane] = d0 * r;
      if (two) hs[c1] = d1 * r;
      __syncthreads();
#pragma unroll 1
      for (int e = lane; e < 384; e += 64) {
        float z = 0.f;
#pragma unroll 4
        for (int c = 0; c < 96; ++c) z += hs[c] * bf2f(w1[c * 384 + e]);
        gs[e] = 0.5f * z * (1.f + tanhf(0.7978845608028654f * (z + 0.044715f * z * z * z)));
      }
      __syncthreads();
      float o0 = 0.f, o1 = 0.f;
#pragma unroll 4
      for (int e = 0; e < 384; ++e) { o0 += gs[e] * bf2f(w2[e * 96 + lane]); o1 += gs[e] * bf2f(w2[e * 96 + c1]); }
      const int ob = (py * 384 + px) * 96;
      __hip_bfloat16 b0 = __float2bfloat16(bf2f(x[ob + lane]) + o0), b1 = __float2bfloat16(bf2f(x[ob + c1]) + o1);
      out[ob + lane] = __builtin_bit_cast(u16, b0);
      if (two) out[ob + c1] = __builtin_bit_cast(u16, b1);
    }
  }
}

extern "C" void kernel_launch(void* const* d_in, const int* in_sizes, int n_in, void* d_out, int out_size, void* d_ws,
                              size_t ws_size, hipStream_t stream) {
  replk_block_kernel<<<256, 512, 0, stream>>>((const u16*)d_in[0], (const u16*)d_in[1], (const u16*)d_in[2],
                                              (const u16*)d_in[3], (const u16*)d_in[4], (u16*)d_out);
}
```
